```python
import torch

def _softmax(x, axis=-1):
    return torch.softmax(x, dim=axis)
def _gelu(x):
    return torch.nn.functional.gelu(x, approximate="tanh")
def _silu(x):
    return torch.nn.functional.silu(x)
def _sigmoid(x):
    return torch.sigmoid(x)
def _layernorm(x, axis=-1, eps=1e-5):
    m = x.mean(dim=axis, keepdim=True)
    v = x.var(dim=axis, keepdim=True, unbiased=False)
    return (x - m) / torch.sqrt(v + eps)
def _scatter_add(tgt, idx, src, axis=0):
    return tgt.clone().index_add_(axis, idx, src)
def _segment_sum(src, seg, n):
    out = torch.zeros((n,) + src.shape[1:], dtype=src.dtype)
    return out.index_add_(0, seg, src)
def _pad(x, pw):
    flat = tuple(v for pair in reversed(pw) for v in pair)
    return torch.nn.functional.pad(x, flat)

kernel_name = 'recurrent_scan__ssm_conv_s4__dense_activation_mish__prompt'

def setup_inputs():

    _w = (lambda a: (lambda t: t.to(torch.bfloat16) if t.is_floating_point() else t)(torch.as_tensor(a)))
    def _A_setup():
        B, L, D, N = 1, 2048, 640, 128
        A_re = -(lambda _x: torch.exp(torch.as_tensor(_x)))((lambda *s: torch.randn(*s, dtype=torch.bfloat16) * 3.2)(D, N))
        A_im = torch.pi * (lambda *s: torch.randn(*s, dtype=torch.bfloat16) * 0.2)(D, N)
        B_re = (lambda *s: torch.randn(*s, dtype=torch.bfloat16) * 0.2)(D, N)
        B_im = (lambda *s: torch.randn(*s, dtype=torch.bfloat16) * 0.2)(D, N)
        C_re = (lambda *s: torch.randn(*s, dtype=torch.bfloat16) * 0.2)(D, N)
        C_im = (lambda *s: torch.randn(*s, dtype=torch.bfloat16) * 0.2)(D, N)
        log_dt = (lambda *s: torch.randn(*s, dtype=torch.bfloat16) * 0.2)(D) * 0.5 - 4.0
        Dskip = (lambda *s: torch.randn(*s, dtype=torch.bfloat16) * 0.2)(D)
        return {
            "u": _w((lambda *s: torch.randn(*s, dtype=torch.bfloat16) * 0.05)(B, L, D)),
            "A_re": _w(A_re),
            "A_im": _w(A_im),
            "B_re": _w(B_re),
            "B_im": _w(B_im),
            "C_re": _w(C_re),
            "C_im": _w(C_im),
            "log_dt": _w(log_dt),
            "Dskip": _w(Dskip),
        }
    _A_in = _A_setup()

    def _B_setup():
        B, S, D, F = 1, 2048, 640, 1728
        return {
            "x": _w((lambda *s: torch.randn(*s, dtype=torch.bfloat16) * 0.05)(B, S, D)),
            "w1": _w((lambda *s: torch.randn(*s, dtype=torch.bfloat16) * 0.8)(D, F) * (D ** -0.5)),
            "b1": _w((lambda *s: torch.randn(*s, dtype=torch.bfloat16) * 0.4)(F)),
            "w2": _w((lambda *s: torch.randn(*s, dtype=torch.bfloat16) * 0.1)(F, D) * (F ** -0.5)),
        }
    _B_in = _B_setup()

    return {
        "A_u": _A_in["u"],
        "A_A_re": _A_in["A_re"],
        "A_A_im": _A_in["A_im"],
        "A_B_re": _A_in["B_re"],
        "A_B_im": _A_in["B_im"],
        "A_C_re": _A_in["C_re"],
        "A_C_im": _A_in["C_im"],
        "A_log_dt": _A_in["log_dt"],
        "A_Dskip": _A_in["Dskip"],
        "B_x": _B_in["x"],
        "B_w1": _B_in["w1"],
        "B_b1": _B_in["b1"],
        "B_w2": _B_in["w2"],
    }

def reference(A_u, A_A_re, A_A_im, A_B_re, A_B_im, A_C_re, A_C_im, A_log_dt, A_Dskip, B_x, B_w1, B_b1, B_w2):
    def _A_ref(u, A_re, A_im, B_re, B_im, C_re, C_im, log_dt, Dskip):
        Bsz, L, D = u.shape
        dt = torch.exp(log_dt)[:, None]
        A = A_re + 1j * A_im
        Bc = B_re + 1j * B_im
        Cc = C_re + 1j * C_im
        logdA = dt * A
        dB = dt * Bc
        l = (lambda x, dt: x.to(dt))(torch.arange(L), A_re.dtype)
        P = torch.exp(logdA[:, :, None] * l[None, None, :])
        K = torch.einsum("dn,dnl,dn->dl", Cc, P, dB).real
        L2 = 2 * L
        Kp = _pad(K, ((0, 0), (0, L)))
        up = (lambda x, *d: x.permute(*d))(u, 0, 2, 1)
        up = _pad(up, ((0, 0), (0, 0), (0, L)))
        Kf = (lambda x, axes=(-2,-1): torch.fft.rfft2(x.float(), dim=axes))(Kp[:, None, :], axes=(-2, -1))
        Uf = (lambda x, axes=(-2,-1): torch.fft.rfft2(x.float(), dim=axes))(up[:, :, None, :], axes=(-2, -1))
        Yf = Uf * Kf[None, :, :, :]
        y = (lambda x, s, axes=(-2,-1): torch.fft.irfft2(x, s=s, dim=axes).to(torch.bfloat16))(Yf, (1, L2), axes=(-2, -1))[:, :, 0, :L]
        y = (lambda x, *d: x.permute(*d))(y, 0, 2, 1) + u * Dskip[None, None, :]
        return y

    def _B_ref(x, w1, b1, w2):
        h = torch.einsum("bsd,df->bsf", x, w1) + b1
        sp = torch.log1p(torch.exp(-torch.abs(h))) + torch.where(h > 0, h, torch.zeros_like(h))
        act = h * torch.tanh(sp)
        return torch.einsum("bsf,fd->bsd", act, w2)

    x = A_u
    xn = _layernorm(x, axis=-1)
    s = _A_ref(xn, A_A_re, A_A_im, A_B_re, A_B_im, A_C_re, A_C_im, A_log_dt, A_Dskip)
    g = _silu(_B_ref(xn, B_w1, B_b1, B_w2))
    return x + s * g
```

```cpp
#include <hip/hip_runtime.h>
#include <hip/hip_bf16.h>
#include <stdint.h>

typedef unsigned short u16;
static constexpr int L = 2048, D = 640, NS = 128, F = 1728;

__device__ __forceinline__ float bf2f(u16 v) { return __uint_as_float(((unsigned)v) << 16); }
__device__ __forceinline__ u16 f2bf(float f) { __hip_bfloat16 b = __float2bfloat16(f); return *reinterpret_cast<u16*>(&b); }
__device__ __forceinline__ float rbf(float f) { return bf2f(f2bf(f)); }

__global__ void k_ln(const u16* x, u16* xn) {
    int t = blockIdx.x; int lane = threadIdx.x;
    float v[10]; float s = 0.f;
    for (int i = 0; i < 10; ++i) { v[i] = bf2f(x[t * D + lane + 64 * i]); s += v[i]; }
    for (int o = 32; o > 0; o >>= 1) s += __shfl_xor(s, o);
    float mean = s * (1.0f / D);
    float q = 0.f;
    for (int i = 0; i < 10; ++i) { float d = v[i] - mean; q += d * d; }
    for (int o = 32; o > 0; o >>= 1) q += __shfl_xor(q, o);
    float var = q * (1.0f / D);
    float mb = rbf(mean), vb = rbf(var);
    float den = rbf(sqrtf(rbf(vb + 1e-5f)));
    for (int i = 0; i < 10; ++i) { float d = rbf(v[i] - mb); xn[t * D + lane + 64 * i] = f2bf(d / den); }
}

__global__ void k_kgen(const u16* Are, const u16* Aim, const u16* Bre, const u16* Bim, const u16* Cre, const u16* Cim,
                       const u16* logdt, float* K) {
    int d = blockIdx.x;
    float dt = rbf(expf(bf2f(logdt[d])));
    for (int l = threadIdx.x; l < L; l += blockDim.x) {
        float lf = rbf((float)l);
        float acc = 0.f;
        for (int n = 0; n < NS; ++n) {
            float zr = dt * bf2f(Are[d * NS + n]), zi = dt * bf2f(Aim[d * NS + n]);
            float br = dt * bf2f(Bre[d * NS + n]), bi = dt * bf2f(Bim[d * NS + n]);
            float cr = bf2f(Cre[d * NS + n]), ci = bf2f(Cim[d * NS + n]);
            float wr = cr * br - ci * bi, wi = cr * bi + ci * br;
            float e = expf(zr * lf), c = cosf(zi * lf), s = sinf(zi * lf);
            acc += e * (wr * c - wi * s);
        }
        K[d * L + l] = acc;
    }
}

__global__ void k_conv(const u16* xn, const float* K, const u16* Dskip, u16* s) {
    int d = blockIdx.x;
    __shared__ float sk[L], sx[L];
    for (int i = threadIdx.x; i < L; i += blockDim.x) { sk[i] = K[d * L + i]; sx[i] = bf2f(xn[i * D + d]); }
    __syncthreads();
    float ds = bf2f(Dskip[d]);
    for (int t = threadIdx.x; t < L; t += blockDim.x) {
        float acc = 0.f;
        for (int j = 0; j <= t; ++j) acc += sk[j] * sx[t - j];
        s[t * D + d] = f2bf(rbf(acc) + rbf(sx[t] * ds));
    }
}

__global__ void k_gemm1(const u16* xn, const u16* w1, const u16* b1, u16* act) {
    int t = blockIdx.x;
    __shared__ float sx[D];
    for (int i = threadIdx.x; i < D; i += blockDim.x) sx[i] = bf2f(xn[t * D + i]);
    __syncthreads();
    for (int f = threadIdx.x; f < F; f += blockDim.x) {
        float acc = 0.f;
        for (int k = 0; k < D; ++k) acc += sx[k] * bf2f(w1[k * F + f]);
        float h = rbf(rbf(acc) + bf2f(b1[f]));
        float e = __expf(fminf(h, 20.f));
        float n = e * (e + 2.f);
        act[t * F + f] = f2bf(h * n / (n + 2.f));
    }
}

__global__ void k_gemm2(const u16* act, const u16* w2, const u16* x, const u16* s, u16* out) {
    int t = blockIdx.x;
    __shared__ float sa[F];
    for (int i = threadIdx.x; i < F; i += blockDim.x) sa[i] = bf2f(act[t * F + i]);
    __syncthreads();
    for (int d = threadIdx.x; d < D; d += blockDim.x) {
        float acc = 0.f;
        for (int k = 0; k < F; ++k) acc += sa[k] * bf2f(w2[k * D + d]);
        float p = rbf(acc);
        float g = rbf(p / (1.f + __expf(-p)));
        float sg = rbf(bf2f(s[t * D + d]) * g);
        out[t * D + d] = f2bf(bf2f(x[t * D + d]) + sg);
    }
}

extern "C" void kernel_launch(void* const* d_in, const int* in_sizes, int n_in,
                              void* d_out, int out_size, void* d_ws, size_t ws_size,
                              hipStream_t stream) {
    const u16* x = (const u16*)d_in[0];
    const u16 *Are = (const u16*)d_in[1], *Aim = (const u16*)d_in[2], *Bre = (const u16*)d_in[3], *Bim = (const u16*)d_in[4];
    const u16 *Cre = (const u16*)d_in[5], *Cim = (const u16*)d_in[6], *logdt = (const u16*)d_in[7], *Dskip = (const u16*)d_in[8];
    const u16 *w1 = (const u16*)d_in[10], *b1 = (const u16*)d_in[11], *w2 = (const u16*)d_in[12];
    char* ws = (char*)d_ws;
    u16* xn = (u16*)ws;
    float* K = (float*)(ws + (4 << 20));
    u16* s = (u16*)(ws + (12 << 20));
    u16* act = (u16*)(ws + (16 << 20));
    k_ln<<<L, 64, 0, stream>>>(x, xn);
    k_kgen<<<D, 256, 0, stream>>>(Are, Aim, Bre, Bim, Cre, Cim, logdt, K);
    k_conv<<<D, 256, 0, stream>>>(xn, K, Dskip, s);
    k_gemm1<<<L, 256, 0, stream>>>(xn, w1, b1, act);
    k_gemm2<<<L, 256, 0, stream>>>(act, w2, x, s, (u16*)d_out);
}
```

```cpp
#include <hip/hip_runtime.h>
#include <stdint.h>

typedef unsigned short u16;
typedef unsigned int u32;
typedef short s4v __attribute__((ext_vector_type(4)));
typedef u32 u2v __attribute__((ext_vector_type(2)));
typedef u32 u4v __attribute__((ext_vector_type(4)));
typedef float f2v __attribute__((ext_vector_type(2)));
typedef float f4v __attribute__((ext_vector_type(4)));
typedef _Float16 h2v __attribute__((ext_vector_type(2)));
typedef _Float16 h8v __attribute__((ext_vector_type(8)));
typedef __bf16 b2v __attribute__((ext_vector_type(2)));
typedef __bf16 b8v __attribute__((ext_vector_type(8)));
#define LDSP(T, p) ((__attribute__((address_space(3))) T*)(p))
#define GLB(T, p) ((__attribute__((address_space(1))) T*)(p))

static constexpr int L = 2048, D = 640, NS = 128, F = 1728;
static constexpr int KS2 = F / 32;

static constexpr size_t WS_ACT = 0;
static constexpr size_t WS_W2P = WS_ACT + (size_t)L * F * 2;
static constexpr size_t WS_XT = WS_W2P + (size_t)F * D * 2;
static constexpr size_t WS_KX = WS_XT + (size_t)L * D * 2;
static constexpr size_t WS_YT = WS_KX + (size_t)L * D * 2;
static constexpr size_t WS_ST = WS_YT + (size_t)L * D * 2;
static constexpr size_t WS_KS = WS_ST + (size_t)L * 8;
static constexpr size_t WS_CNT = WS_KS + 4096;

static constexpr int LDS_BYTES = 80 * 1024;

__device__ __forceinline__ float bf2f(u32 v) { return __uint_as_float(v << 16); }
__device__ __forceinline__ float bflo(u32 v) { return __uint_as_float(v << 16); }
__device__ __forceinline__ float bfhi(u32 v) { return __uint_as_float(v & 0xffff0000u); }
__device__ __forceinline__ u32 pkbf(float a, float b) {
    f2v v = {a, b};
    return __builtin_bit_cast(u32, __builtin_convertvector(v, b2v));
}
__device__ __forceinline__ u32 pkh(float a, float b) {
    f2v v = {a, b};
    return __builtin_bit_cast(u32, __builtin_convertvector(v, h2v));
}
__device__ __forceinline__ float rbf(float a) { return bflo(pkbf(a, a)); }
__device__ __forceinline__ f4v mfma_bf(u4v a, u4v b, f4v c) {
    return __builtin_amdgcn_mfma_f32_16x16x32_bf16(__builtin_bit_cast(b8v, a), __builtin_bit_cast(b8v, b), c, 0, 0, 0);
}
__device__ __forceinline__ f4v mfma_h(u4v a, u4v b, f4v c) {
    return __builtin_amdgcn_mfma_f32_16x16x32_f16(__builtin_bit_cast(h8v, a), __builtin_bit_cast(h8v, b), c, 0, 0, 0);
}
__device__ __forceinline__ u2v tr_read(const char* p) {
    return __builtin_bit_cast(u2v, __builtin_amdgcn_ds_read_tr16_b64_v4i16(LDSP(s4v, p)));
}
__device__ __forceinline__ float dot2bf(u32 a, u32 b, float c) {
    return __builtin_amdgcn_fdot2_f32_bf16(__builtin_bit_cast(b2v, a), __builtin_bit_cast(b2v, b), c, false);
}
__device__ __forceinline__ void glds16(const void* g, void* l) {
    __builtin_amdgcn_global_load_lds((const __attribute__((address_space(1))) void*)g,
                                     (__attribute__((address_space(3))) void*)l, 16, 0, 0);
}
__device__ __forceinline__ u32 lds_addr(const void* l) { return (u32)(size_t)(__attribute__((address_space(3))) const char*)l; }
#define GLDS16(ldsa, voff, sbase) \
    asm volatile("s_mov_b32 m0, %0\n\ts_nop 0\n\tglobal_load_lds_dwordx4 %1, %2" ::"s"(ldsa), "v"(voff), "s"(sbase) : "memory")
#define GLDS4(ldsa, voff, sbase) \
    asm volatile("s_mov_b32 m0, %0\n\ts_nop 0\n\tglobal_load_lds_dword %1, %2" ::"s"(ldsa), "v"(voff), "s"(sbase) : "memory")
__device__ __forceinline__ void ln_stats(float s1, float s2, float& mb, float& den) {
    float mean = s1 * (1.0f / D);
    float var = fmaxf(s2 * (1.0f / D) - mean * mean, 0.f);
    mb = rbf(mean);
    den = rbf(__builtin_sqrtf(rbf(rbf(var) + 1e-5f)));
}
__device__ __forceinline__ float ln_apply(float x, float mb, float rden) { return rbf(rbf(x - mb) * rden); }

struct Params {
    const u16 *x, *Are, *Aim, *Bre, *Bim, *Cre, *Cim, *logdt, *Dskip, *w1, *b1, *w2;
    u16* out;
    char* ws;
};

#define VMWAIT4(N, a, b, c, d) asm volatile("s_waitcnt vmcnt(" #N ")" : "+v"(a), "+v"(b), "+v"(c), "+v"(d)::"memory")
#define XLOAD(dst, voff, sbase, imm) \
    asm volatile("global_load_dwordx4 %0, %1, %2 offset:%3" : "=v"(dst) : "v"(voff), "s"(sbase), "i"(imm) : "memory")

__device__ __forceinline__ void gemm1_role(const Params& p, int b, char* smem) {
    const int tid = threadIdx.x, lane = tid & 63;
    const int w = __builtin_amdgcn_readfirstlane(tid >> 6);
    const int xcd = b & 7, j = b >> 3;
    const int mb = 4 * (xcd >> 1) + (j & 3), nt = 7 * (xcd & 1) + (j >> 2);
    const int l15 = lane & 15, q4 = lane >> 4;
    const bool half = (nt == 13);
    char* misc = smem + 65536;

    u32 woff[4];
#pragma unroll
    for (int i = 0; i < 4; ++i) {
        int P = (w * 4 + i) * 64 + lane;
        int k = P >> 4, slot = P & 15;
        int id = (k & 3) | (((k >> 3) & 1) << 2);
        int cc = slot ^ (2 * id);
        if (half) cc &= 7;
        woff[i] = (u32)(k * F + nt * 128 + cc * 8) * 2u;
    }
    const char* w1b = (const char*)p.w1;
    const u32 lbase = lds_addr(smem) + w * 4096;
    const char* xb0 = (const char*)p.x + (size_t)(mb * 128 + w * 32) * (D * 2);
    const char* xb1 = xb0 + 16 * D * 2;
    const u32 xoff = (u32)(l15 * D * 2 + q4 * 16);

#define G1_G(s)                                                                                       \
    {                                                                                                 \
        _Pragma("unroll") for (int i = 0; i < 4; ++i)                                                 \
            GLDS16(lbase + ((s)&3) * 16384 + i * 1024, woff[i], w1b + (size_t)(s) * (64 * F * 2)); \
    }
    u4v xf[6][4];
#define G1_X(s)                                       \
    {                                                 \
        XLOAD(xf[(s) % 6][0], xoff, xb0, (s)*128);    \
        XLOAD(xf[(s) % 6][1], xoff, xb1, (s)*128);    \
        XLOAD(xf[(s) % 6][2], xoff, xb0, (s)*128 + 64); \
        XLOAD(xf[(s) % 6][3], xoff, xb1, (s)*128 + 64); \
    }
    {
        u32 fo = (u32)(nt * 128 + (half ? (lane & 31) : lane) * 2) * 2u;
        GLDS4(lds_addr(misc), fo, p.b1);
    }
    G1_G(0) G1_X(0) G1_G(1) G1_X(1) G1_G(2) G1_X(2) G1_G(3) G1_X(3) G1_X(4) G1_X(5)

    const int q = l15 >> 2, pp = lane & 3;
    const int idl = q | ((q4 & 1) << 2);
    const char* wrd[8];
#pragma unroll
    for (int ft = 0; ft < 8; ++ft) wrd[ft] = smem + (8 * q4 + q) * 256 + 32 * (((ft + 2 * w) & 7) ^ idl) + 8 * pp;

    f4v acc[8][2];
#pragma unroll
    for (int ft = 0; ft < 8; ++ft) acc[ft][0] = acc[ft][1] = (f4v){0.f, 0.f, 0.f, 0.f};
    float s1[2] = {0.f, 0.f}, s2[2] = {0.f, 0.f}, cs[2] = {0.f, 0.f};
    const u32 ONES = 0x3f803f80u;
    u4v xt[4];
    const int xks0 = (nt < 6) ? 2 * nt : nt + 6;
    const int xks1 = (nt < 6) ? 2 * nt + 1 : nt + 6;
    const char* xtb0 = xb0 + xks0 * 64;
    const char* xtb1 = xb0 + xks1 * 64;
    const u32 xoff2 = xoff + 16 * D * 2;

#define G1_STAGE(s, NW)                                                                                \
    {                                                                                                  \
        VMWAIT4(NW, xf[(s) % 6][0], xf[(s) % 6][1], xf[(s) % 6][2], xf[(s) % 6][3]);                   \
        __builtin_amdgcn_s_barrier();                                                                  \
        if ((s) >= 1 && (s) + 3 <= 9) G1_G((s) + 3)                                                    \
        if ((s) >= 1 && (s) + 5 <= 9) G1_X((s) + 5)                                                    \
        _Pragma("unroll") for (int k2 = 0; k2 < 2; ++k2) {                                             \
            _Pragma("unroll") for (int ft = 0; ft < 8; ++ft) {                                         \
                u2v lo = tr_read(wrd[ft] + ((s)&3) * 16384 + k2 * 8192);                               \
                u2v hi = tr_read(wrd[ft] + ((s)&3) * 16384 + k2 * 8192 + 1024);                        \
                u4v wf = {lo[0], lo[1], hi[0], hi[1]};                                                 \
                acc[ft][0] = mfma_bf(wf, xf[(s) % 6][k2 * 2], acc[ft][0]);                             \
                acc[ft][1] = mfma_bf(wf, xf[(s) % 6][k2 * 2 + 1], acc[ft][1]);                         \
                if (ft < 2) {                                                                          \
                    _Pragma("unroll") for (int e = 0; e < 4; ++e) cs[ft & 1] = dot2bf(wf[e], ONES, cs[ft & 1]); \
                }                                                                                      \
            }                                                                                          \
            _Pragma("unroll") for (int tt = 0; tt < 2; ++tt) _Pragma("unroll") for (int e = 0; e < 4; ++e) { \
                u32 v = xf[(s) % 6][k2 * 2 + tt][e];                                                   \
                s1[tt] = dot2bf(v, ONES, s1[tt]);                                                      \
                s2[tt] = dot2bf(v, v, s2[tt]);                                                         \
            }                                                                                          \
        }                                                                                              \
        asm volatile("" : "+v"(s1[0]), "+v"(s1[1]), "+v"(s2[0]), "+v"(s2[1]), "+v"(cs[0]), "+v"(cs[1])); \
        __builtin_amdgcn_sched_barrier(0);                                                             \
    }
    G1_STAGE(0, 32) G1_STAGE(1, 24) G1_STAGE(2, 24) G1_STAGE(3, 24) G1_STAGE(4, 20) G1_STAGE(5, 20)
    G1_STAGE(6, 16)
    XLOAD(xt[0], xoff, xtb0, 0); XLOAD(xt[1], xoff2, xtb0, 0); XLOAD(xt[2], xoff, xtb1, 0); XLOAD(xt[3], xoff2, xtb1, 0);
    G1_STAGE(7, 16) G1_STAGE(8, 8) G1_STAGE(9, 4)
    VMWAIT4(0, xt[0], xt[1], xt[2], xt[3]);

#pragma unroll
    for (int tt = 0; tt < 2; ++tt) {
        s1[tt] += __shfl_xor(s1[tt], 16); s1[tt] += __shfl_xor(s1[tt], 32);
        s2[tt] += __shfl_xor(s2[tt], 16); s2[tt] += __shfl_xor(s2[tt], 32);
        cs[tt] += __shfl_xor(cs[tt], 16); cs[tt] += __shfl_xor(cs[tt], 32);
    }
    if (q4 == 0) {
        ((float*)(misc + 256))[(2 * w) * 16 + l15] = cs[0];
        ((float*)(misc + 256))[(2 * w + 1) * 16 + l15] = cs[1];
    }
    float mean[2], den[2], rden[2];
#pragma unroll
    for (int tt = 0; tt < 2; ++tt) {
        ln_stats(s1[tt], s2[tt], mean[tt], den[tt]);
        rden[tt] = 1.0f / den[tt];
    }
    if (nt == 0 && q4 < 2) {
        float m = q4 ? mean[1] : mean[0], r = q4 ? rden[1] : rden[0];
        *(f2v*)(p.ws + WS_ST + (size_t)(mb * 128 + w * 32 + q4 * 16 + l15) * 8) = (f2v){m, r};
    }
    {
        char* st = smem + 32768 + w * 4096;
#pragma unroll
        for (int u = 0; u < 2; ++u)
#pragma unroll
            for (int tt = 0; tt < 2; ++tt) {
                int pos = tt * 16 + 4 * (l15 & 3) + (l15 >> 2);
#pragma unroll
                for (int e = 0; e < 4; ++e) {
                    u32 v = xt[u * 2 + tt][e];
                    float a = ln_apply(bflo(v), mean[tt], rden[tt]), c = ln_apply(bfhi(v), mean[tt], rden[tt]);
                    u32 h = pkh(a, c);
                    int ch = 8 * q4 + 2 * e;
                    *(u16*)(st + u * 2048 + ch * 64 + pos * 2) = (u16)h;
                    *(u16*)(st + u * 2048 + (ch + 1) * 64 + pos * 2) = (u16)(h >> 16);
                }
            }
#pragma unroll
        for (int u = 0; u < 2; ++u) {
            if (u == 1 && nt >= 6) break;
            int ks = u ? xks1 : xks0;
#pragma unroll
            for (int i = 0; i < 2; ++i) {
                int pc = lane + 64 * i, ch = pc >> 2, part = pc & 3;
                u4v v = *(const u4v*)(st + u * 2048 + pc * 16);
                *(u4v*)(p.ws + WS_XT + (size_t)(ks * 32 + ch) * (L * 2) + (size_t)(mb * 128 + w * 32) * 2 + part * 16) = v;
            }
        }
    }
    __syncthreads();
    char* actb = p.ws + WS_ACT + ((size_t)(mb * 2 + (w >> 1)) * KS2 + nt * 4) * 4096 + (size_t)((w & 1) * 2) * 1024 + lane * 16;
#pragma unroll
    for (int fr = 0; fr < 4; ++fr) {
        const int fp = (fr + w) & 3;
        if (half && fp >= 2) continue;
        f4v c0 = *(const f4v*)(misc + 256 + (fp * 32 + 4 * q4) * 4);
        f4v c1 = *(const f4v*)(misc + 256 + (fp * 32 + 16 + 4 * q4) * 4);
        u2v bb0 = *(const u2v*)(misc + (fp * 32 + 4 * q4) * 2);
        u2v bb1 = *(const u2v*)(misc + (fp * 32 + 16 + 4 * q4) * 2);
        float bs[8] = {bflo(bb0[0]), bfhi(bb0[0]), bflo(bb0[1]), bfhi(bb0[1]), bflo(bb1[0]), bfhi(bb1[0]), bflo(bb1[1]), bfhi(bb1[1])};
#pragma unroll
        for (int tt = 0; tt < 2; ++tt) {
            float a[8];
#pragma unroll
            for (int r = 0; r < 8; ++r) {
                float ac = r < 4 ? acc[2 * fr][tt][r & 3] : acc[2 * fr + 1][tt][r & 3];
                float cc = r < 4 ? c0[r & 3] : c1[r & 3];
                float h = fmaf(fmaf(-mean[tt], cc, ac), rden[tt], bs[r]);
                float e = __builtin_amdgcn_exp2f(fminf(h * 1.44269504f, 60.f));
                float n = e * (e + 2.f);
                a[r] = h * n * __builtin_amdgcn_rcpf(n + 2.f);
            }
            u4v o = {pkbf(a[0], a[1]), pkbf(a[2], a[3]), pkbf(a[4], a[5]), pkbf(a[6], a[7])};
            *(u4v*)(actb + (size_t)fp * 4096 + tt * 1024) = o;
        }
    }
}

struct cplx2 { f2v r, i; };
__device__ __forceinline__ cplx2 cmul(cplx2 a, cplx2 b) {
    cplx2 o;
    o.r = a.r * b.r - a.i * b.i;
    o.i = a.r * b.i + a.i * b.r;
    return o;
}
__device__ __forceinline__ cplx2 cexp2(f2v zr, f2v zi, float c) {
    cplx2 o;
#pragma unroll
    for (int m = 0; m < 2; ++m) {
        float mag = __builtin_amdgcn_exp2f(zr[m] * c * 1.44269504089f);
        float ph = zi[m] * c;
        const float CH = 0.15915494f, CL = 6.4206382e-9f;
        float hi = ph * CH;
        float lo = fmaf(ph, CH, -hi) + ph * CL;
        float fr = __builtin_amdgcn_fractf(hi) + lo;
        o.r[m] = mag * __builtin_amdgcn_cosf(fr);
        o.i[m] = mag * __builtin_amdgcn_sinf(fr);
    }
    return o;
}
static constexpr int KG_ROW = 544;

__device__ __forceinline__ void kgen_role(const Params& p, int d, char* sw  ) {
    const int lane = threadIdx.x & 63, l15 = lane & 15, q4 = lane >> 4;
    const int o = d * NS + 2 * lane;
    u32 are = *(const u32*)(p.Are + o), aim = *(const u32*)(p.Aim + o), bre = *(const u32*)(p.Bre + o);
    u32 bim = *(const u32*)(p.Bim + o), cre = *(const u32*)(p.Cre + o), cim = *(const u32*)(p.Cim + o);
    float dt = rbf(expf(bf2f(p.logdt[d])));
    f2v zr = {dt * bflo(are), dt * bfhi(are)}, zi = {dt * bflo(aim), dt * bfhi(aim)};
    cplx2 dB = {{dt * bflo(bre), dt * bfhi(bre)}, {dt * bflo(bim), dt * bfhi(bim)}};
    cplx2 C = {{bflo(cre), bfhi(cre)}, {bflo(cim), bfhi(cim)}};
    cplx2 W = cmul(C, dB);
    float wm = fmaxf(fmaxf(fabsf(W.r[0]), fabsf(W.r[1])), fmaxf(fabsf(W.i[0]), fabsf(W.i[1])));
#pragma unroll
    for (int s = 32; s > 0; s >>= 1) wm = fmaxf(wm, __shfl_xor(wm, s));
    int eU = 14 - __builtin_amdgcn_frexp_expf(wm);
    eU = min(max(eU, -100), 100);
    const float sU = __builtin_amdgcn_ldexpf(1.0f, eU);
    W.r *= sU; W.i *= sU;

    char* r0 = sw;
    char* r1 = sw + 16 * KG_ROW;
    char* kc = sw + 32 * KG_ROW;
#define KG_PUT(reg, row, v) *(u2v*)((reg) + (row)*KG_ROW + lane * 8) = (u2v){pkh((v).r[0], (v).r[1]), pkh((v).i[0], (v).i[1])}
    {
        cplx2 v1 = cexp2(zr, zi, 8.f);
        cplx2 v = {{16384.f, 16384.f}, {0.f, 0.f}};
#pragma unroll
        for (int jj = 0; jj < 16; ++jj) {
            cplx2 t = {v.r, -v.i};
            KG_PUT(r0, jj, t);
            if (jj < 15) v = cmul(v, v1);
        }
    }
    u4v vf[8];
#pragma unroll
    for (int ks = 0; ks < 8; ++ks) vf[ks] = *(const u4v*)(r0 + l15 * KG_ROW + ks * 64 + q4 * 16);
    cplx2 E[8];
    E[1] = cexp2(zr, zi, 1.f);
    E[2] = cmul(E[1], E[1]); E[3] = cmul(E[2], E[1]); E[4] = cmul(E[2], E[2]);
    E[5] = cmul(E[4], E[1]); E[6] = cmul(E[4], E[2]); E[7] = cmul(E[4], E[3]);
    const cplx2 G = cexp2(zr, zi, 128.f);
    f4v acc[3];
    cplx2 T = W;
#pragma unroll
    for (int a = 0; a < 2; ++a) {
        KG_PUT(r1, 8 * a, T);
#pragma unroll
        for (int c = 1; c < 8; ++c) { cplx2 u = cmul(T, E[c]); KG_PUT(r1, 8 * a + c, u); }
        T = cmul(T, G);
    }
    acc[0] = (f4v){0.f, 0.f, 0.f, 0.f};
#pragma unroll
    for (int ks = 0; ks < 8; ++ks) acc[0] = mfma_h(*(const u4v*)(r1 + l15 * KG_ROW + ks * 64 + q4 * 16), vf[ks], acc[0]);
#pragma unroll
    for (int a = 0; a < 2; ++a) {
        KG_PUT(r0, 4 * a, T);
#pragma unroll
        for (int c = 1; c < 4; ++c) { cplx2 u = cmul(T, E[2 * c]); KG_PUT(r0, 4 * a + c, u); }
        T = cmul(T, G);
    }
#pragma unroll
    for (int a = 0; a < 4; ++a) {
        KG_PUT(r0, 8 + 2 * a, T);
        cplx2 u = cmul(T, E[4]); KG_PUT(r0, 8 + 2 * a + 1, u);
        T = cmul(T, G);
    }
    acc[1] = (f4v){0.f, 0.f, 0.f, 0.f};
#pragma unroll
    for (int ks = 0; ks < 8; ++ks) acc[1] = mfma_h(*(const u4v*)(r0 + l15 * KG_ROW + ks * 64 + q4 * 16), vf[ks], acc[1]);
    T = cmul(W, cexp2(zr, zi, 1024.f));
#pragma unroll
    for (int a = 0; a < 9; ++a) {
        KG_PUT(r1, a, T);
        if (a < 8) T = cmul(T, G);
    }
    acc[2] = (f4v){0.f, 0.f, 0.f, 0.f};
#pragma unroll
    for (int ks = 0; ks < 8; ++ks) acc[2] = mfma_h(*(const u4v*)(r1 + l15 * KG_ROW + ks * 64 + q4 * 16), vf[ks], acc[2]);
    float km = 0.f;
#pragma unroll
    for (int r = 0; r < 4; ++r) {
        km = fmaxf(km, fmaxf(fabsf(acc[0][r]), fabsf(acc[1][r])));
        if (q4 < 2 || (q4 == 2 && r == 0)) km = fmaxf(km, fabsf(acc[2][r]));
    }
#pragma unroll
    for (int s = 32; s > 0; s >>= 1) km = fmaxf(km, __shfl_xor(km, s));
    int eK = 15 - (__builtin_amdgcn_frexp_expf(km) - eU - 14);
    eK = min(max(eK, -120), 120);
    const int sh = eK - eU - 14;
    if (lane == 0) ((float*)(p.ws + WS_KS))[d] = __builtin_amdgcn_ldexpf(1.0f, -eK);
    u16* kch = (u16*)kc;
#pragma unroll
    for (int r = 0; r < 4; ++r) {
        int i = 4 * q4 + r;
        u32 h01 = pkh(__builtin_amdgcn_ldexpf(acc[0][r], sh), __builtin_amdgcn_ldexpf(acc[1][r], sh));
        u32 h2 = pkh(__builtin_amdgcn_ldexpf(acc[2][r], sh), 0.f);
        kch[128 * (i >> 3) + (i & 7) + 8 * l15] = (u16)h01;
        int i1 = (i < 8) ? 256 + 64 * (i >> 2) + (i & 3) + 4 * l15 : 384 + 32 * ((i - 8) >> 1) + (i & 1) + 2 * l15;
        kch[i1] = (u16)(h01 >> 16);
        if (i < 8) kch[512 + 16 * i + l15] = (u16)h2;
        else if (i == 8 && l15 == 0) kch[640] = (u16)h2;
    }
    char* kx = p.ws + WS_KX + (size_t)d * (L * 2);
    {
        u4v a = *(const u4v*)(kc + (lane & 31) * 16);
        u2v s = *(const u2v*)(kc + 512 + (lane & 31) * 8);
        u32 s4 = kch[256 + (lane & 31) * 4 + 4];
        u4v b2;
        b2[0] = (s[0] & 0xffffu) | (s[0] << 16);
        b2[1] = (s[0] >> 16) | (s[1] << 16);
        b2[2] = (s[1] & 0xffffu) | (s[1] << 16);
        b2[3] = (s[1] >> 16) | (s4 << 16);
        *(u4v*)(kx + lane * 16) = lane < 32 ? a : b2;
    }
    {
        u32 s = *(const u32*)(kc + 768 + lane * 4);
        u32 c = kch[384 + lane * 2 + 2];
        u4v v;
        v[0] = (s & 0xffffu) | (s << 16);
        v[1] = s;
        v[2] = (s >> 16) | (s & 0xffff0000u);
        v[3] = c | (c << 16);
        *(u4v*)(kx + 1024 + lane * 16) = v;
    }
#pragma unroll
    for (int it = 0; it < 2; ++it) {
        int oc = lane + 64 * it;
        u32 a = kch[512 + oc], c = kch[513 + oc];
        u4v v;
        v[0] = a | (a << 16);
        v[1] = v[0];
        v[2] = ((oc & 1) ? c : a) | (c << 16);
        v[3] = c | (c << 16);
        *(u4v*)(kx + 2048 + oc * 16) = v;
    }
}

__device__ __forceinline__ void w2pack_role(const Params& p, int unit, char* sw) {
    const int lane = threadIdx.x & 63, l15 = lane & 15, q4 = lane >> 4;
    const int nb = unit / KS2, ks = unit - nb * KS2;
#pragma unroll
    for (int i = 0; i < 5; ++i) {
        int c = lane + 64 * i, row = c / 10, ch = c - row * 10;
        *(u4v*)(sw + c * 16) = *(const u4v*)(p.w2 + (size_t)(ks * 32 + row) * D + nb * 80 + ch * 8);
    }
    const int q = l15 >> 2, pp = lane & 3;
    char* dst = p.ws + WS_W2P + (size_t)unit * 5120 + lane * 16;
#pragma unroll
    for (int dt = 0; dt < 5; ++dt) {
        u2v lo = tr_read(sw + (4 * q4 + q) * 160 + dt * 32 + pp * 8);
        u2v hi = tr_read(sw + (16 + 4 * q4 + q) * 160 + dt * 32 + pp * 8);
        *(u4v*)(dst + dt * 1024) = (u4v){lo[0], lo[1], hi[0], hi[1]};
    }
}

__global__ __launch_bounds__(256, 2) void k1(Params p) {
    extern __shared__ __attribute__((aligned(16))) char smem[];
    const int b = blockIdx.x;
    const int w = __builtin_amdgcn_readfirstlane(threadIdx.x >> 6);
    if (b < 224) {
        gemm1_role(p, b, smem);
    } else if (b < 352 || b >= 480) {
        int kb = b < 352 ? b - 224 : b - 480 + 128;
        kgen_role(p, kb * 4 + w, smem + w * 20480);
    } else {
        int unit = (b - 352) * 4 + w;
        if (unit < 8 * KS2) w2pack_role(p, unit, smem + w * 20480);
        if (b == 479 && threadIdx.x < 8) *(u32*)(p.ws + WS_CNT + threadIdx.x * 256) = 0u;
    }
}

static constexpr int CV_KB = 32 + 4096;
static constexpr int CV_XB = 512 + 4096;
static constexpr int CV_CH = 8832;
static constexpr int CV_RED = 3 * CV_CH;
static constexpr int CV_REDS = 2056 * 4;

__device__ __forceinline__ void conv_role(const Params& p, int cb, char* smem) {
    const int tid = threadIdx.x, lane = tid & 63, l15 = lane & 15, q4 = lane >> 4;
    const int a = __builtin_amdgcn_readfirstlane(tid >> 6);
    const int cg = cb >> 5, jb = cb & 31;
    const int nch = jb < 16 ? 3 : 2;
    const int d0 = cg * 80 + (jb < 16 ? 3 * jb : 48 + 2 * (jb - 16));
    for (int c = 0; c < nch; ++c) {
        const char* kg = p.ws + WS_KX + (size_t)(d0 + c) * 4096;
        const char* xg = p.ws + WS_XT + (size_t)(d0 + c) * 4096;
        glds16(kg + a * 1024 + lane * 16, smem + c * CV_CH + 32 + a * 1024);
        int pos = a * 64 + lane;
        int src = pos ^ ((pos >> 4) & 1);
        glds16(xg + src * 16, smem + c * CV_CH + 4160 + 512 + a * 1024);
        if (tid < 2) *(u4v*)(smem + c * CV_CH + tid * 16) = (u4v){0u, 0u, 0u, 0u};
        if (tid >= 64 && tid < 96) *(u4v*)(smem + c * CV_CH + 4160 + (tid - 64) * 16) = (u4v){0u, 0u, 0u, 0u};
    }
    float ksc[3];
#pragma unroll
    for (int c = 0; c < 3; ++c) ksc[c] = ((const float*)(p.ws + WS_KS))[d0 + (c < nch ? c : 0)];
    asm volatile("s_waitcnt vmcnt(0)" ::: "memory");
    __syncthreads();
    const int q = l15 >> 2, pp = lane & 3;
    const int kro = (16 + 32 * q4 - 4 * q + 4 * pp) * 2;
    float* red = (float*)(smem + CV_RED);
    for (int c = 0; c < nch; ++c) {
        const char* kimg = smem + c * CV_CH + kro;
        const char* ximg = smem + c * CV_CH + 4160;
        u4v kf[16], xf[16];
#pragma unroll
        for (int e = 0; e < 16; ++e) {
            u2v lo = tr_read(kimg + e * 256), hi = tr_read(kimg + e * 256 + 32);
            kf[e] = (u4v){lo[0], lo[1], hi[0], hi[1]};
        }
#pragma unroll
        for (int g = 0; g < 16; ++g) {
            u2v v[2];
#pragma unroll
            for (int h = 0; h < 2; ++h) {
                int G = l15 + 8 * (g - 1) - 2 * q4 - h + 16;
                v[h] = *(const u2v*)(ximg + G * 32 + (((a >> 1) ^ ((G >> 3) & 1)) * 16) + (a & 1) * 8);
            }
            xf[g] = (u4v){v[0][0], v[0][1], v[1][0], v[1][1]};
        }
        f4v acc[8];
#pragma unroll
        for (int t = 0; t < 8; ++t) {
            acc[t] = (f4v){0.f, 0.f, 0.f, 0.f};
#pragma unroll
            for (int e = 0; e <= 2 * t + 1; ++e) acc[t] = mfma_h(xf[2 * t - e + 1], kf[e], acc[t]);
        }
        if (c > 0) __syncthreads();
#pragma unroll
        for (int t = 0; t < 8; ++t)
#pragma unroll
            for (int r = 0; r < 4; ++r) red[a * 2056 + 256 * t + 64 * q4 + 16 * r + l15 + a] = acc[t][r];
        __syncthreads();
        f4v s0 = *(const f4v*)(red + tid * 8), s1 = *(const f4v*)(red + tid * 8 + 4);
#pragma unroll
        for (int aa = 1; aa < 4; ++aa) {
            f4v u0 = *(const f4v*)(red + aa * 2056 + tid * 8), u1 = *(const f4v*)(red + aa * 2056 + tid * 8 + 4);
#pragma unroll
            for (int r = 0; r < aa; ++r) u0[r] = tid == 0 ? 0.f : u0[r];
            s0 += u0; s1 += u1;
        }
        const float sc = c == 0 ? ksc[0] : (c == 1 ? ksc[1] : ksc[2]);
        u4v o = {pkbf(s0[0] * sc, s0[1] * sc), pkbf(s0[2] * sc, s0[3] * sc), pkbf(s1[0] * sc, s1[1] * sc), pkbf(s1[2] * sc, s1[3] * sc)};
        char* dst = p.ws + WS_YT + (size_t)(d0 + c) * 4096 + tid * 16;
        asm volatile("global_store_dwordx4 %0, %1, off sc1\n\ts_nop 1" ::"v"(dst), "v"(o) : "memory");
    }
    asm volatile("s_waitcnt vmcnt(0)" ::: "memory");
    __syncthreads();
    if (tid == 0) __hip_atomic_fetch_add((u32*)(p.ws + WS_CNT + cg * 256), 1u, __ATOMIC_RELAXED, __HIP_MEMORY_SCOPE_AGENT);
}

__device__ __forceinline__ void gemm2_role(const Params& p, int b, char* smem) {
    const int tid = threadIdx.x, lane = tid & 63, l15 = lane & 15, q4 = lane >> 4;
    const int w = __builtin_amdgcn_readfirstlane(tid >> 6);
    const int xcd = b & 7, j = b >> 3;
    const int mb = 8 * (xcd >> 1) + (j & 7), nb = 4 * (xcd & 1) + (j >> 3);
    const char* ab = p.ws + WS_ACT + ((size_t)mb * KS2 + w) * 4096;
    const char* wb = p.ws + WS_W2P + ((size_t)nb * KS2 + w) * 5120 + 2048;
    const u32 lo16 = lane * 16;
    f4v acc[5][4];
#pragma unroll
    for (int dt = 0; dt < 5; ++dt)
#pragma unroll
        for (int tt = 0; tt < 4; ++tt) acc[dt][tt] = (f4v){0.f, 0.f, 0.f, 0.f};
    u4v af[3][4], wf[3][5];
#define G2_LOAD(i)                                                                                      \
    {                                                                                                   \
        _Pragma("unroll") for (int tt = 0; tt < 4; ++tt) af[(i)%3][tt] = *(const u4v*)(ab + (size_t)(i) * 16384 + (size_t)lo16 + tt * 1024); \
        _Pragma("unroll") for (int dt = 0; dt < 5; ++dt) wf[(i)%3][dt] = *(const u4v*)(wb + (size_t)(i) * 20480 + (size_t)lo16 + (dt - 2) * 1024); \
        __builtin_amdgcn_sched_barrier(0);                                                              \
    }
#define G2_MMA(i)                                                                                       \
    {                                                                                                   \
        _Pragma("unroll") for (int dt = 0; dt < 5; ++dt) _Pragma("unroll") for (int tt = 0; tt < 4; ++tt) \
            acc[dt][tt] = mfma_bf(wf[(i)%3][dt], af[(i)%3][tt], acc[dt][tt]);                           \
        __builtin_amdgcn_sched_barrier(0);                                                              \
    }
    G2_LOAD(0) G2_LOAD(1)
#pragma unroll
    for (int i = 0; i < 13; ++i) {
        if (i + 2 < 13) G2_LOAD(i + 2)
        if (i + 2 == 13 && w < 2) G2_LOAD(13)
        G2_MMA(i)
    }
    if (w < 2) G2_MMA(13)
    const int t = mb * 64 + w * 16 + l15;
    const int dbase = nb * 80 + 4 * q4;
    u2v xv[5], dv[5];
#pragma unroll
    for (int dt = 0; dt < 5; ++dt) {
        xv[dt] = *(const u2v*)(p.x + (size_t)t * D + dbase + dt * 16);
        dv[dt] = *(const u2v*)(p.Dskip + dbase + dt * 16);
    }
    const f2v st = *(const f2v*)(p.ws + WS_ST + (size_t)t * 8);
#pragma unroll
    for (int tt = 0; tt < 4; ++tt) {
        if (tt != w) {
            int slot = tt - (tt > w ? 1 : 0);
#pragma unroll
            for (int dt = 0; dt < 5; ++dt) *(f4v*)(smem + ((w * 3 + slot) * 5 + dt) * 1024 + lane * 16) = acc[dt][tt];
        }
    }
    if (tid == 0) {
        u32* cnt = (u32*)(p.ws + WS_CNT + nb * 256);
        unsigned spins = 0;
        while (__hip_atomic_load(cnt, __ATOMIC_RELAXED, __HIP_MEMORY_SCOPE_AGENT) < 32u && ++spins < (1u << 24)) __builtin_amdgcn_s_sleep(1);
    }
    __syncthreads();
    u2v yv[5];
    {
        const char* yb = p.ws + WS_YT + (size_t)(nb * 80 + (lane >> 2)) * 4096 + (size_t)(mb * 64 + w * 16 + (lane & 3) * 4) * 2;
        asm volatile(
            "global_load_dwordx2 %0, %5, off sc1\n\t"
            "global_load_dwordx2 %1, %6, off sc1\n\t"
            "global_load_dwordx2 %2, %7, off sc1\n\t"
            "global_load_dwordx2 %3, %8, off sc1\n\t"
            "global_load_dwordx2 %4, %9, off sc1\n\t"
            "s_waitcnt vmcnt(0)"
            : "=&v"(yv[0]), "=&v"(yv[1]), "=&v"(yv[2]), "=&v"(yv[3]), "=&v"(yv[4])
            : "v"(yb), "v"(yb + 16 * 4096), "v"(yb + 32 * 4096), "v"(yb + 48 * 4096), "v"(yb + 64 * 4096)
            : "memory");
    }
    char* ys = smem + 61440 + w * 2560;
#pragma unroll
    for (int i = 0; i < 5; ++i) *(u2v*)(ys + i * 512 + lane * 8) = yv[i];
    f4v g[5];
#pragma unroll
    for (int dt = 0; dt < 5; ++dt) {
        g[dt] = w == 0 ? acc[dt][0] : (w == 1 ? acc[dt][1] : (w == 2 ? acc[dt][2] : acc[dt][3]));
    }
#pragma unroll
    for (int sw = 0; sw < 4; ++sw) {
        if (sw != w) {
            int slot = w - (w > sw ? 1 : 0);
#pragma unroll
            for (int dt = 0; dt < 5; ++dt) g[dt] += *(const f4v*)(smem + ((sw * 3 + slot) * 5 + dt) * 1024 + lane * 16);
        }
    }
#pragma unroll
    for (int dt = 0; dt < 5; ++dt) {
        float o[4];
#pragma unroll
        for (int r = 0; r < 4; ++r) {
            float xx = (r & 1) ? bfhi(xv[dt][r >> 1]) : bflo(xv[dt][r >> 1]);
            float dk = (r & 1) ? bfhi(dv[dt][r >> 1]) : bflo(dv[dt][r >> 1]);
            float y = bf2f(*(const u16*)(ys + (dt * 16 + 4 * q4 + r) * 32 + l15 * 2));
            float xn = ln_apply(xx, st[0], st[1]);
            float s = rbf(y + rbf(xn * dk));
            float pz = rbf(g[dt][r]);
            float gate = rbf(pz * __builtin_amdgcn_rcpf(1.f + __builtin_amdgcn_exp2f(-1.44269504f * pz)));
            o[r] = xx + rbf(s * gate);
        }
        *(u2v*)(p.out + (size_t)t * D + dbase + dt * 16) = (u2v){pkbf(o[0], o[1]), pkbf(o[2], o[3])};
    }
}

__global__ __launch_bounds__(256, 2) void k2(Params p) {
    extern __shared__ __attribute__((aligned(16))) char smem[];
    const int b = blockIdx.x;
    if (b < 256) gemm2_role(p, b, smem);
    else conv_role(p, b - 256, smem);
}

extern "C" void kernel_launch(void* const* d_in, const int* in_sizes, int n_in, void* d_out, int out_size, void* d_ws,
                              size_t ws_size, hipStream_t stream) {
    Params p;
    p.x = (const u16*)d_in[0];
    p.Are = (const u16*)d_in[1]; p.Aim = (const u16*)d_in[2]; p.Bre = (const u16*)d_in[3]; p.Bim = (const u16*)d_in[4];
    p.Cre = (const u16*)d_in[5]; p.Cim = (const u16*)d_in[6]; p.logdt = (const u16*)d_in[7]; p.Dskip = (const u16*)d_in[8];
    p.w1 = (const u16*)d_in[10]; p.b1 = (const u16*)d_in[11]; p.w2 = (const u16*)d_in[12];
    p.out = (u16*)d_out;
    p.ws = (char*)d_ws;
    (void)hipFuncSetAttribute((const void*)k1, hipFuncAttributeMaxDynamicSharedMemorySize, LDS_BYTES);
    (void)hipFuncSetAttribute((const void*)k2, hipFuncAttributeMaxDynamicSharedMemorySize, LDS_BYTES);
    k1<<<512, 256, LDS_BYTES, stream>>>(p);
    k2<<<512, 256, LDS_BYTES, stream>>>(p);
}
```

```cpp
#include <hip/hip_runtime.h>
#include <stdint.h>

#if defined(__HIP_DEVICE_COMPILE__) && !defined(ALLOWPK)
#define NOPK __attribute__((target("no-packed-fp32-ops")))
#else
#define NOPK
#endif
typedef unsigned short u16;
typedef unsigned int u32;
typedef short s4v __attribute__((ext_vector_type(4)));
typedef u32 u2v __attribute__((ext_vector_type(2)));
typedef u32 u4v __attribute__((ext_vector_type(4)));
typedef float f2v __attribute__((ext_vector_type(2)));
typedef float f4v __attribute__((ext_vector_type(4)));
typedef _Float16 h2v __attribute__((ext_vector_type(2)));
typedef _Float16 h8v __attribute__((ext_vector_type(8)));
typedef __bf16 b2v __attribute__((ext_vector_type(2)));
typedef __bf16 b8v __attribute__((ext_vector_type(8)));
#define LDSP(T, p) ((__attribute__((address_space(3))) T*)(p))
#define GLB(T, p) ((__attribute__((address_space(1))) T*)(p))

static constexpr int L = 2048, D = 640, NS = 128, F = 1728;
static constexpr int KS2 = F / 32;

static constexpr size_t WS_ACT = 0;
static constexpr size_t WS_W2P = WS_ACT + (size_t)L * F * 2;
static constexpr int REC_K = 32, REC_X = 4160 + 512, REC = 8768;
static constexpr size_t WS_REC = WS_W2P + (size_t)F * D * 2;
static constexpr size_t WS_YT = WS_REC + (size_t)641 * REC + 192;
static constexpr size_t WS_ST = WS_YT + (size_t)L * D * 2;
static constexpr size_t WS_CNT = WS_ST + (size_t)L * 8;
static constexpr size_t WS_FLG = WS_CNT + 4096;
static constexpr size_t WS_CS = WS_FLG + 16384;
static_assert(WS_REC % 256 == 0 && WS_YT % 256 == 0, "alignment");

static constexpr int LDS_BYTES = 80 * 1024;

__device__ __forceinline__ NOPK u32 f2u(float v) { return __builtin_bit_cast(u32, v); }
__device__ __forceinline__ NOPK float bf2f(u32 v) { return __builtin_bit_cast(float, (u32)(v << 16)); }
__device__ __forceinline__ NOPK float bflo(u32 v) { return __builtin_bit_cast(float, (u32)(v << 16)); }
__device__ __forceinline__ NOPK float bfhi(u32 v) { return __builtin_bit_cast(float, (u32)(v & 0xffff0000u)); }
__device__ __forceinline__ NOPK u32 pkbf(float a, float b) {
    f2v v = {a, b};
    return __builtin_bit_cast(u32, __builtin_convertvector(v, b2v));
}
__device__ __forceinline__ NOPK u32 pkh(float a, float b) {
    f2v v = {a, b};
    return __builtin_bit_cast(u32, __builtin_convertvector(v, h2v));
}
__device__ __forceinline__ NOPK float rbf(float a) { return bflo(pkbf(a, a)); }
__device__ __forceinline__ NOPK f4v mfma_bf(u4v a, u4v b, f4v c) {
    return __builtin_amdgcn_mfma_f32_16x16x32_bf16(__builtin_bit_cast(b8v, a), __builtin_bit_cast(b8v, b), c, 0, 0, 0);
}
__device__ __forceinline__ NOPK f4v mfma_h(u4v a, u4v b, f4v c) {
    return __builtin_amdgcn_mfma_f32_16x16x32_f16(__builtin_bit_cast(h8v, a), __builtin_bit_cast(h8v, b), c, 0, 0, 0);
}
__device__ __forceinline__ NOPK u2v tr_read(const char* p) {
    return __builtin_bit_cast(u2v, __builtin_amdgcn_ds_read_tr16_b64_v4i16(LDSP(s4v, p)));
}
__device__ __forceinline__ NOPK float dot2bf(u32 a, u32 b, float c) {
    return __builtin_amdgcn_fdot2_f32_bf16(__builtin_bit_cast(b2v, a), __builtin_bit_cast(b2v, b), c, false);
}
__device__ __forceinline__ NOPK void glds16(const void* g, void* l) {
    __builtin_amdgcn_global_load_lds((const __attribute__((address_space(1))) void*)g,
                                     (__attribute__((address_space(3))) void*)l, 16, 0, 0);
}
__device__ __forceinline__ NOPK u32 lds_addr(const void* l) { return (u32)(size_t)(__attribute__((address_space(3))) const char*)l; }
#define GLDS16(ldsa, voff, sbase) \
    asm volatile("s_mov_b32 m0, %0\n\ts_nop 0\n\tglobal_load_lds_dwordx4 %1, %2" ::"s"(ldsa), "v"(voff), "s"(sbase) : "memory")
#define GLDS4(ldsa, voff, sbase) \
    asm volatile("s_mov_b32 m0, %0\n\ts_nop 0\n\tglobal_load_lds_dword %1, %2" ::"s"(ldsa), "v"(voff), "s"(sbase) : "memory")
template <int CTRL> __device__ __forceinline__ NOPK float dpp(float v) {
    return __builtin_bit_cast(float, __builtin_amdgcn_update_dpp(0, __builtin_bit_cast(int, v), CTRL, 0xf, 0xf, true));
}
__device__ __forceinline__ NOPK float red16(float v) {
    v += dpp<0xB1>(v); v += dpp<0x4E>(v); v += dpp<0x141>(v); v += dpp<0x140>(v);
    return v;
}
__device__ __forceinline__ NOPK float max64(float v) {
    v = fmaxf(v, dpp<0xB1>(v)); v = fmaxf(v, dpp<0x4E>(v)); v = fmaxf(v, dpp<0x141>(v)); v = fmaxf(v, dpp<0x140>(v));
    const int b = __builtin_bit_cast(int, v);
    float a0 = __builtin_bit_cast(float, __builtin_amdgcn_readlane(b, 0)), a1 = __builtin_bit_cast(float, __builtin_amdgcn_readlane(b, 16));
    float a2 = __builtin_bit_cast(float, __builtin_amdgcn_readlane(b, 32)), a3 = __builtin_bit_cast(float, __builtin_amdgcn_readlane(b, 48));
    return fmaxf(fmaxf(a0, a1), fmaxf(a2, a3));
}
__device__ __forceinline__ NOPK void st16(void* ptr, u4v v) {
    asm volatile("global_store_dwordx4 %0, %1, off sc1\n\ts_nop 1" ::"v"(ptr), "v"(v) : "memory");
}
__device__ __forceinline__ NOPK void ln_stats(float s1, float s2, float& mb, float& den) {
    float mean = s1 * (1.0f / D);
    float var = fmaxf(s2 * (1.0f / D) - mean * mean, 0.f);
    mb = rbf(mean);
    den = rbf(__builtin_sqrtf(rbf(rbf(var) + 1e-5f)));
}
__device__ __forceinline__ NOPK float ln_apply(float x, float mb, float rden) { return rbf(rbf(x - mb) * rden); }

#define SG(i) {}
#define SGDECL
#define SGDUMP(n)
#define TLB()
#define TLE(off)
struct Params {
    const u16 *x, *Are, *Aim, *Bre, *Bim, *Cre, *Cim, *logdt, *Dskip, *w1, *b1, *w2;
    u16* out;
    char* ws;
};

typedef float f16v __attribute__((ext_vector_type(16)));
__device__ __forceinline__ NOPK f16v mfma32_bf(u4v a, u4v b, f16v c) {
    return __builtin_amdgcn_mfma_f32_32x32x16_bf16(__builtin_bit_cast(b8v, a), __builtin_bit_cast(b8v, b), c, 0, 0, 0);
}
#define VMWAIT(N) asm volatile("s_waitcnt vmcnt(" #N ") lgkmcnt(0)" ::: "memory")

#define RAWBAR() { asm volatile("s_waitcnt lgkmcnt(0)" ::: "memory"); __builtin_amdgcn_s_barrier(); asm volatile("" ::: "memory"); }
static constexpr u32 MAGIC_LO = 0x7F4A7C15u, MAGIC_HI = 0x9E3779B9u;
__device__ __forceinline__ NOPK int g1_block(int mb, int nt) { return 2 * (mb >> 2) + (nt >= 7 ? 1 : 0) + 8 * ((mb & 3) + 4 * (nt >= 7 ? nt - 7 : nt)); }
__device__ __forceinline__ NOPK void st8_dev(void* ptr, u2v v) {
    asm volatile("global_store_dwordx2 %0, %1, off sc1" ::"v"(ptr), "v"(v) : "memory");
}

__device__ __forceinline__ NOPK void gemm1_role(const Params& p, int b, char* smem) {
    const int tid = threadIdx.x, lane = tid & 63;
    const int w = __builtin_amdgcn_readfirstlane(tid >> 6);
    const int wt = w >> 2, wf = w & 3;
    const int xcd = b & 7, j = b >> 3;
    const int mb = 4 * (xcd >> 1) + (j & 3), nt = 7 * (xcd & 1) + (j >> 2);
    const int rot = (nt + 3 * (j & 3) + (xcd >> 1)) % 10;
    const int l31 = lane & 31, hh = lane >> 5;
    const bool half = (nt == 13);
    SGDECL
#define STOFF 0
    SG(0)
    u2v bia[4];
    {
        const u32 bo = (u32)min(nt * 128 + 32 * wf + 4 * hh, F - 28) * 2u;
        asm volatile("global_load_dwordx2 %0, %4, %5\n\tglobal_load_dwordx2 %1, %4, %5 offset:16\n\t"
                     "global_load_dwordx2 %2, %4, %5 offset:32\n\tglobal_load_dwordx2 %3, %4, %5 offset:48"
                     : "=&v"(bia[0]), "=&v"(bia[1]), "=&v"(bia[2]), "=&v"(bia[3]) : "v"(bo), "s"(p.b1) : "memory");
    }
    u32 xo[2], wo;
#pragma unroll
    for (int i = 0; i < 2; ++i) {
        int P = (2 * w + i) * 64 + lane, row = P >> 3, ch = (P & 7) ^ ((row >> 1) & 7);
        xo[i] = (u32)(row * (D * 2) + ch * 16);
    }
    {
        int P = w * 64 + lane, k = P >> 4, ch = (P & 15) ^ ((k & 3) << 2);
        if (half) ch &= 7;
        wo = (u32)(k * (F * 2) + ch * 16);
    }
    const char* xb = (const char*)p.x + (size_t)mb * 128 * (D * 2);
    const char* wb = (const char*)p.w1 + nt * 256;
    const u32 lx = lds_addr(smem) + w * 2048, lw = lds_addr(smem) + 49152 + w * 1024;
#define SROT(st) (((st) + rot >= 10) ? (st) + rot - 10 : (st) + rot)
#define G1_X(st) { const char* s_ = xb + SROT(st) * 128; GLDS16(lx + ((st) % 3) * 16384, xo[0], s_); GLDS16(lx + ((st) % 3) * 16384 + 1024, xo[1], s_); }
#define G1_W(h) { const char* s_ = wb + (size_t)(SROT((h) >> 1) * 64 + ((h)&1) * 32) * (F * 2); GLDS16(lw + ((h)&3) * 8192, wo, s_); }
    G1_X(0) G1_W(0) G1_W(1) G1_X(1) G1_W(2) G1_W(3) G1_X(2)

    const int q = (lane & 15) >> 2, pp = lane & 3, blk = (lane >> 4) & 1;
    const char* xa[4];
#pragma unroll
    for (int u = 0; u < 4; ++u) xa[u] = smem + (64 * wt + l31) * 128 + (((2 * u + hh) ^ ((l31 >> 1) & 7)) << 4);
    const char* wa = smem + 49152 + (8 * hh + q) * 256 + (((4 * wf + 2 * blk + (pp >> 1)) ^ (q << 2)) << 4) + 8 * (pp & 1);

    f16v acc[2];
#pragma unroll
    for (int i = 0; i < 16; ++i) acc[0][i] = acc[1][i] = 0.f;
    u4v xf[2][2][2], wfr[2][2];
    const char* flg = p.ws + WS_FLG + (size_t)b * 64 + (lane & 7) * 8;
    const char* stg = p.ws + WS_ST + (size_t)mb * 1024 + lane * 16;
    const char* csg = p.ws + WS_CS + (size_t)nt * 2048 + lane * 16;
    u2v fl = {0u, 0u};
    u4v sv = {0u, 0u, 0u, 0u}, cv0 = sv, cv1 = sv;
    u32 sst = (w == 7) ? 0u : 2u;
    const unsigned long long magic = ((unsigned long long)MAGIC_HI << 32) | MAGIC_LO;
#define G1_POLL0()                                                                                        \
    asm volatile("s_cmp_lg_u32 %1, 0\n\ts_cbranch_scc1 .Lsv%=\n\tglobal_load_dwordx2 %0, %2, off sc1\n.Lsv%=:" \
                 : "+v"(fl) : "s"(sst), "v"(flg) : "memory", "scc")
#define G1_SERVE(REPOLL)                                                                                  \
    asm volatile("s_cmp_lg_u32 %4, 0\n\ts_cbranch_scc1 .Lsv%=\n\t"                                        \
                 "v_cmp_eq_u64 vcc, %8, %0\n\ts_cmp_eq_u64 vcc, exec\n\ts_cbranch_scc1 .Lft%=\n\t" REPOLL \
                 "s_branch .Lsv%=\n.Lft%=:\n\t"                                                           \
                 "global_load_dwordx4 %1, %6, off sc1\n\tglobal_load_dwordx4 %2, %7, off sc1\n\t"         \
                 "global_load_dwordx4 %3, %7, off offset:1024 sc1\n\ts_mov_b32 %4, 1\n.Lsv%=:"            \
                 : "+v"(fl), "+v"(sv), "+v"(cv0), "+v"(cv1), "+s"(sst) : "v"(flg), "v"(stg), "v"(csg), "s"(magic) : "memory", "scc", "vcc")
    SG(1)
#define G1_READ(h)                                                                                       \
    _Pragma("unroll") for (int o = 0; o < 2; ++o) {                                                      \
        u2v lo = tr_read(wa + ((h)&3) * 8192 + o * 4096), hi = tr_read(wa + ((h)&3) * 8192 + o * 4096 + 1024); \
        wfr[(h)&1][o] = (u4v){lo[0], lo[1], hi[0], hi[1]};                                               \
        xf[(h)&1][o][0] = *(const u4v*)(xa[((h)&1) * 2 + o] + (((h) >> 1) % 3) * 16384);                 \
        xf[(h)&1][o][1] = *(const u4v*)(xa[((h)&1) * 2 + o] + (((h) >> 1) % 3) * 16384 + 4096);          \
    }
#define G1_MMA(h)                                                                                        \
    _Pragma("unroll") for (int o = 0; o < 2; ++o) {                                                      \
        acc[0] = mfma32_bf(wfr[(h)&1][o], xf[(h)&1][o][0], acc[0]);                                      \
        acc[1] = mfma32_bf(wfr[(h)&1][o], xf[(h)&1][o][1], acc[1]);                                      \
    }
#define G1_IT(h, NW)                                                                                     \
    {                                                                                                    \
        VMWAIT(NW);                                                                                      \
        __builtin_amdgcn_s_barrier();                                                                    \
        if ((h) == 9) G1_POLL0();          \
        if ((h) == 11 || (h) == 13 || (h) == 15) G1_SERVE("global_load_dwordx2 %0, %5, off sc1\n\t");    \
        if ((h) == 17) G1_SERVE("");                                                                     \
        if ((h) >= 0 && (h) + 4 <= 19) G1_W((h) + 4)                                                     \
        if ((h) >= 1 && ((h)&1) && ((h) - 1) / 2 + 3 <= 9) G1_X(((h) - 1) / 2 + 3)                       \
        G1_READ((h) + 1)                                                                                 \
        __builtin_amdgcn_sched_barrier(0);                                                               \
        if ((h) >= 0) { G1_MMA(h) }                                                                      \
        asm volatile("" : "+v"(acc[0]), "+v"(acc[1]));                                                   \
        __builtin_amdgcn_sched_barrier(0);                                                               \
    }
    G1_IT(-1, 7) G1_IT(0, 6) G1_IT(1, 4) G1_IT(2, 6) G1_IT(3, 4) SG(2) G1_IT(4, 6) G1_IT(5, 4) G1_IT(6, 6) G1_IT(7, 4)
    G1_IT(8, 6) SG(3) G1_IT(9, 4) G1_IT(10, 6) G1_IT(11, 4) G1_IT(12, 6) G1_IT(13, 4) SG(4) G1_IT(14, 6) G1_IT(15, 4)
    G1_IT(16, 4) G1_IT(17, 1) G1_IT(18, 0)
    G1_MMA(19)
    SG(5)
    asm volatile("" : "+v"(bia[0]), "+v"(bia[1]), "+v"(bia[2]), "+v"(bia[3]), "+v"(sv), "+v"(cv0), "+v"(cv1));
    char* stl = smem + 16384;
    char* csl = smem + 16384 + 1024;
    if (w == 7) {
        if (sst == 0) {
            unsigned spins = 0;
            do {
                asm volatile("global_load_dwordx2 %0, %1, off sc1\n\ts_waitcnt vmcnt(0)" : "=v"(fl) : "v"(flg) : "memory");
                if ((__builtin_amdgcn_ballot_w64(fl[0] == MAGIC_LO && fl[1] == MAGIC_HI) == ~0ull)) break;
                __builtin_amdgcn_s_sleep(2);
            } while (++spins < (1u << 22));
            asm volatile("global_load_dwordx4 %0, %3, off sc1\n\tglobal_load_dwordx4 %1, %4, off sc1\n\t"
                         "global_load_dwordx4 %2, %4, off offset:1024 sc1\n\ts_waitcnt vmcnt(0)"
                         : "=&v"(sv), "=&v"(cv0), "=&v"(cv1) : "v"(stg), "v"(csg) : "memory");
        }
        if (lane < 8) st8_dev((void*)flg, (u2v){0u, 0u});
        *(u4v*)(stl + lane * 16) = sv;
        f4v c0 = __builtin_bit_cast(f4v, cv0), c1 = __builtin_bit_cast(f4v, cv1);
        *(float*)(csl + lane * 4) = (c0[0] + c0[1]) + (c0[2] + c0[3]);
        *(float*)(csl + 256 + lane * 4) = (c1[0] + c1[1]) + (c1[2] + c1[3]);
    }
    RAWBAR()
    SG(6)
    if (!(half && wf >= 2)) {
        f2v csv[8], bsv[8];
#pragma unroll
        for (int g = 0; g < 4; ++g) {
            f4v c0 = *(const f4v*)(csl + (32 * wf + 8 * g + 4 * hh) * 4);
            csv[2 * g] = (f2v){c0[0], c0[1]};
            csv[2 * g + 1] = (f2v){c0[2], c0[3]};
            bsv[2 * g] = (f2v){bflo(bia[g][0]), bfhi(bia[g][0])};
            bsv[2 * g + 1] = (f2v){bflo(bia[g][1]), bfhi(bia[g][1])};
        }
        char* actb = p.ws + WS_ACT + ((size_t)(mb * 2 + wt) * KS2 + nt * 4 + wf) * 4096 + (size_t)((l31 >> 4) * 1024 + ((l31 & 15) + 16 * hh) * 16);
#pragma unroll
        for (int i = 0; i < 2; ++i) {
            const f2v sr = *(const f2v*)(stl + (64 * wt + 32 * i + l31) * 8);
            const f2v nm = {-sr[0], -sr[0]}, rd = {sr[1], sr[1]};
#pragma unroll
            for (int c = 0; c < 2; ++c) {
                u4v o;
#pragma unroll
                for (int e = 0; e < 4; ++e) {
                    const int ix = 4 * c + e;
                    f2v a = {acc[i][2 * ix], acc[i][2 * ix + 1]};
                    f2v h = __builtin_elementwise_fma(__builtin_elementwise_fma(nm, csv[ix], a), rd, bsv[ix]);
                    f2v u = h * 1.44269504f;
                    f2v e1 = (f2v){__builtin_amdgcn_exp2f(u[0]), __builtin_amdgcn_exp2f(u[1])} + 1.0f;
                    f2v w1 = __builtin_elementwise_fma(e1, e1, (f2v){1.f, 1.f});
                    f2v hr = h * (f2v){__builtin_amdgcn_rcpf(w1[0]), __builtin_amdgcn_rcpf(w1[1])};
                    f2v r = __builtin_elementwise_fma(hr, (f2v){-2.f, -2.f}, h);
                    o[e] = pkbf(r[0], r[1]);
                }
                st16(actb + i * 2048 + c * 512, o);
            }
        }
    }
    SG(7)
    SGDUMP(8)
}

__device__ __forceinline__ NOPK void ln_role(const Params& p, int rb, char* smem) {
    const int tid = threadIdx.x, lane = tid & 63;
    const int w = __builtin_amdgcn_readfirstlane(tid >> 6);
    const int rl = 4 * w + (lane >> 4), t = rb * 32 + rl, c15 = lane & 15;
    u4v v[5];
#pragma unroll
    for (int i = 0; i < 5; ++i) v[i] = *(const u4v*)(p.x + (size_t)t * D + (c15 + 16 * i) * 8);
    float s = 0.f;
#pragma unroll
    for (int i = 0; i < 5; ++i)
#pragma unroll
        for (int e = 0; e < 4; ++e) s += bflo(v[i][e]) + bfhi(v[i][e]);
    const float mean = red16(s) * (1.0f / D);
    float qv = 0.f;
#pragma unroll
    for (int i = 0; i < 5; ++i)
#pragma unroll
        for (int e = 0; e < 4; ++e) {
            float a = bflo(v[i][e]) - mean, c = bfhi(v[i][e]) - mean;
            qv = fmaf(a, a, qv); qv = fmaf(c, c, qv);
        }
    const float var = red16(qv) * (1.0f / D);
    const float mbf = rbf(mean);
    const float rd = 1.0f / rbf(__builtin_sqrtf(rbf(rbf(var) + 1e-5f)));
    if (c15 == 0) st8_dev(p.ws + WS_ST + (size_t)t * 8, (u2v){f2u(mbf), f2u(rd)});
    asm volatile("s_waitcnt vmcnt(0)" ::: "memory");
    __builtin_amdgcn_s_barrier();
    if (tid < 14) st8_dev(p.ws + WS_FLG + (size_t)g1_block(rb >> 2, tid) * 64 + (rb & 3) * 8, (u2v){MAGIC_LO, MAGIC_HI});
#pragma unroll
    for (int i = 0; i < 5; ++i) {
        u2v o0, o1;
        o0[0] = pkh(ln_apply(bflo(v[i][0]), mbf, rd), ln_apply(bfhi(v[i][0]), mbf, rd));
        o0[1] = pkh(ln_apply(bflo(v[i][1]), mbf, rd), ln_apply(bfhi(v[i][1]), mbf, rd));
        o1[0] = pkh(ln_apply(bflo(v[i][2]), mbf, rd), ln_apply(bfhi(v[i][2]), mbf, rd));
        o1[1] = pkh(ln_apply(bflo(v[i][3]), mbf, rd), ln_apply(bfhi(v[i][3]), mbf, rd));
        *(u2v*)(smem + rl * 1288 + (c15 + 16 * i) * 16) = o0;
        *(u2v*)(smem + rl * 1288 + (c15 + 16 * i) * 16 + 8) = o1;
    }
    RAWBAR()
    const int G = lane >> 4, q = c15 >> 2, pp = lane & 3;
    const char* src = smem + (16 * (G >> 1) + 4 * q + 2 * (G & 1)) * 1288 + pp * 8;
    char* dst = p.ws + WS_REC + (size_t)c15 * REC + REC_X + (size_t)rb * 64 + (G ^ ((rb >> 2) & 1)) * 16;
#pragma unroll
    for (int it = 0; it < 5; ++it) {
        const int cg = w * 5 + it;
        u2v lo = tr_read(src + cg * 32), hi = tr_read(src + cg * 32 + 1288);
        st16(dst + (size_t)cg * 16 * REC, (u4v){lo[0], lo[1], hi[0], hi[1]});
    }
}

__device__ __forceinline__ NOPK void colsum_role(const Params& p, int cb, char* smem) {
    const int tid = threadIdx.x, lane = tid & 63;
    const int w = __builtin_amdgcn_readfirstlane(tid >> 6);
    const int nt = cb >> 2, kq = cb & 3;
    const int ch = (nt == 13) ? (lane & 7) : (lane & 15);
    u4v v[5];
#pragma unroll
    for (int i = 0; i < 5; ++i) v[i] = *(const u4v*)(p.w1 + (size_t)(160 * kq + 20 * w + 4 * i + (lane >> 4)) * F + nt * 128 + ch * 8);
    float s[8];
#pragma unroll
    for (int e = 0; e < 4; ++e) {
        s[2 * e] = ((bflo(v[0][e]) + bflo(v[1][e])) + (bflo(v[2][e]) + bflo(v[3][e]))) + bflo(v[4][e]);
        s[2 * e + 1] = ((bfhi(v[0][e]) + bfhi(v[1][e])) + (bfhi(v[2][e]) + bfhi(v[3][e]))) + bfhi(v[4][e]);
    }
    float* red = (float*)(smem + 65536);
    *(f4v*)(red + (4 * w + (lane >> 4)) * 128 + (lane & 15) * 8) = (f4v){s[0], s[1], s[2], s[3]};
    *(f4v*)(red + (4 * w + (lane >> 4)) * 128 + (lane & 15) * 8 + 4) = (f4v){s[4], s[5], s[6], s[7]};
    RAWBAR()
    if (tid < 128) {
        float a = 0.f;
#pragma unroll
        for (int r = 0; r < 32; ++r) a += red[r * 128 + tid];
        asm volatile("global_store_dword %0, %1, off sc1\n\ts_waitcnt vmcnt(0)" ::"v"(p.ws + WS_CS + (size_t)nt * 2048 + tid * 16 + kq * 4), "v"(a) : "memory");
    }
    __builtin_amdgcn_s_barrier();
    if (tid < 16) st8_dev(p.ws + WS_FLG + (size_t)g1_block(tid, nt) * 64 + (4 + kq) * 8, (u2v){MAGIC_LO, MAGIC_HI});
}

struct cplx2 { f2v r, i; };
__device__ __forceinline__ NOPK cplx2 cmul(cplx2 a, cplx2 b) {
    cplx2 o;
    o.r = a.r * b.r - a.i * b.i;
    o.i = a.r * b.i + a.i * b.r;
    return o;
}
__device__ __forceinline__ NOPK cplx2 cexp2(f2v zr, f2v zi, float c) {
    cplx2 o;
#pragma unroll
    for (int m = 0; m < 2; ++m) {
        float mag = __builtin_amdgcn_exp2f(zr[m] * c * 1.44269504089f);
        float ph = zi[m] * c;
        const float CH = 0.15915494f, CL = 6.4206382e-9f;
        float hi = ph * CH;
        float lo = fmaf(ph, CH, -hi) + ph * CL;
        float fr = __builtin_amdgcn_fractf(hi) + lo;
        o.r[m] = mag * __builtin_amdgcn_cosf(fr);
        o.i[m] = mag * __builtin_amdgcn_sinf(fr);
    }
    return o;
}
static constexpr int KG_ROW = 544;

__device__ __forceinline__ NOPK void kgen_role(const Params& p, int d, int part, char* sw  ) {
    const int lane = threadIdx.x & 63, l15 = lane & 15, q4 = lane >> 4;
    const int o = d * NS + 2 * lane;
    u32 are = *(const u32*)(p.Are + o), aim = *(const u32*)(p.Aim + o), bre = *(const u32*)(p.Bre + o);
    u32 bim = *(const u32*)(p.Bim + o), cre = *(const u32*)(p.Cre + o), cim = *(const u32*)(p.Cim + o);
    float dt = rbf(expf(bf2f(p.logdt[d])));
    f2v zr = {dt * bflo(are), dt * bfhi(are)}, zi = {dt * bflo(aim), dt * bfhi(aim)};
    cplx2 dB = {{dt * bflo(bre), dt * bfhi(bre)}, {dt * bflo(bim), dt * bfhi(bim)}};
    cplx2 C = {{bflo(cre), bfhi(cre)}, {bflo(cim), bfhi(cim)}};
    cplx2 W = cmul(C, dB);
    float wm = fmaxf(fmaxf(fabsf(W.r[0]), fabsf(W.r[1])), fmaxf(fabsf(W.i[0]), fabsf(W.i[1])));
    const float dsk = bf2f(p.Dskip[d]);
    wm = fmaxf(max64(wm), fabsf(dsk) * (1.0f / 128.0f));
    int eU = 14 - __builtin_amdgcn_frexp_expf(wm);
    eU = min(max(eU, -100), 100);
    const float sU = __builtin_amdgcn_ldexpf(1.0f, eU);
    W.r *= sU; W.i *= sU;
    char* rec = p.ws + WS_REC + (size_t)d * REC;
    if (part == 0 && lane < 34) {
        u4v z = {0u, 0u, 0u, 0u};
        if (lane == 0) z[0] = f2u(__builtin_amdgcn_ldexpf(1.0f, 8 - eU));
        *(u4v*)(rec + (lane < 2 ? lane * 16 : 4160 + (lane - 2) * 16)) = z;
    }
    const float KSC = 2.384185791015625e-07f;

    char* r0 = sw;
    char* kc = sw + 16 * KG_ROW;
#define KG_PUT(row, v) *(u2v*)(r0 + (row)*KG_ROW + lane * 8) = (u2v){pkh((v).r[0], (v).r[1]), pkh((v).i[0], (v).i[1])}
#define KG_MMA(A)                                         \
    A = (f4v){0.f, 0.f, 0.f, 0.f};                        \
    _Pragma("unroll") for (int ks = 0; ks < 8; ++ks) A = mfma_h(*(const u4v*)(r0 + l15 * KG_ROW + ks * 64 + q4 * 16), vf[ks], A);
    {
        cplx2 v1 = cexp2(zr, zi, 8.f);
        cplx2 v = {{16384.f, 16384.f}, {0.f, 0.f}};
#pragma unroll
        for (int jj = 0; jj < 16; ++jj) {
            cplx2 t = {v.r, -v.i};
            KG_PUT(jj, t);
            if (jj < 15) v = cmul(v, v1);
        }
    }
    u4v vf[8];
#pragma unroll
    for (int ks = 0; ks < 8; ++ks) vf[ks] = *(const u4v*)(r0 + l15 * KG_ROW + ks * 64 + q4 * 16);
    const cplx2 G = cexp2(zr, zi, 128.f);
    u16* kch = (u16*)kc;
    char* kx = rec + REC_K;
    if (part == 0) {
        cplx2 E[8];
        E[1] = cexp2(zr, zi, 1.f);
        E[2] = cmul(E[1], E[1]); E[3] = cmul(E[2], E[1]); E[4] = cmul(E[2], E[2]);
        E[5] = cmul(E[4], E[1]); E[6] = cmul(E[4], E[2]); E[7] = cmul(E[4], E[3]);
        cplx2 T = W;
#pragma unroll
        for (int a = 0; a < 2; ++a) {
            KG_PUT(8 * a, T);
#pragma unroll
            for (int c = 1; c < 8; ++c) { cplx2 u = cmul(T, E[c]); KG_PUT(8 * a + c, u); }
            if (a == 0) T = cmul(T, G);
        }
        f4v acc;
        KG_MMA(acc)
        if (lane == 0) acc[0] = fmaf(dsk * sU, 16384.f, acc[0]);
        *(u2v*)(kx + (128 * (q4 >> 1) + 4 * (q4 & 1) + 8 * l15) * 2) = (u2v){pkh(acc[0] * KSC, acc[1] * KSC), pkh(acc[2] * KSC, acc[3] * KSC)};
    } else {
        const cplx2 E2 = cexp2(zr, zi, 2.f);
        const cplx2 E4 = cmul(E2, E2), E6 = cmul(E4, E2);
        cplx2 T = cmul(W, cexp2(zr, zi, 256.f));
#pragma unroll
        for (int a = 0; a < 2; ++a) {
            KG_PUT(4 * a, T);
            { cplx2 u = cmul(T, E2); KG_PUT(4 * a + 1, u); }
            { cplx2 u = cmul(T, E4); KG_PUT(4 * a + 2, u); }
            { cplx2 u = cmul(T, E6); KG_PUT(4 * a + 3, u); }
            T = cmul(T, G);
        }
#pragma unroll
        for (int a = 0; a < 4; ++a) {
            KG_PUT(8 + 2 * a, T);
            cplx2 u = cmul(T, E4); KG_PUT(8 + 2 * a + 1, u);
            if (a < 3) T = cmul(T, G);
        }
        f4v acc1, acc2;
        KG_MMA(acc1)
        T = cmul(W, cexp2(zr, zi, 1024.f));
#pragma unroll
        for (int a = 0; a < 9; ++a) {
            KG_PUT(a, T);
            if (a < 8) T = cmul(T, G);
        }
        KG_MMA(acc2)
        {
            u32 h01 = pkh(acc1[0] * KSC, acc1[1] * KSC), h23 = pkh(acc1[2] * KSC, acc1[3] * KSC);
            if (q4 < 2) {
                *(u2v*)(kc + (64 * q4 + 4 * l15) * 2) = (u2v){h01, h23};
            } else {
                *(u32*)(kc + (128 + 64 * (q4 - 2) + 2 * l15) * 2) = h01;
                *(u32*)(kc + (128 + 64 * (q4 - 2) + 32 + 2 * l15) * 2) = h23;
            }
            u32 g01 = pkh(acc2[0] * KSC, acc2[1] * KSC), g23 = pkh(acc2[2] * KSC, acc2[3] * KSC);
            if (q4 < 2) {
                kch[256 + 64 * q4 + l15] = (u16)g01;
                kch[256 + 64 * q4 + 16 + l15] = (u16)(g01 >> 16);
                kch[256 + 64 * q4 + 32 + l15] = (u16)g23;
                kch[256 + 64 * q4 + 48 + l15] = (u16)(g23 >> 16);
            } else if (lane == 32) {
                kch[384] = (u16)g01;
            }
        }
        if (lane < 32) {
            u2v s = *(const u2v*)(kc + lane * 8);
            u32 s4 = kch[lane * 4 + 4];
            u4v b2;
            b2[0] = (s[0] & 0xffffu) | (s[0] << 16);
            b2[1] = (s[0] >> 16) | (s[1] << 16);
            b2[2] = (s[1] & 0xffffu) | (s[1] << 16);
            b2[3] = (s[1] >> 16) | (s4 << 16);
            st16(kx + 512 + lane * 16, b2);
        }
        {
            u32 s = *(const u32*)(kc + 256 + lane * 4);
            u32 c = kch[128 + lane * 2 + 2];
            u4v v;
            v[0] = (s & 0xffffu) | (s << 16);
            v[1] = s;
            v[2] = (s >> 16) | (s & 0xffff0000u);
            v[3] = c | (c << 16);
            st16(kx + 1024 + lane * 16, v);
        }
#pragma unroll
        for (int it = 0; it < 2; ++it) {
            int oc = lane + 64 * it;
            u32 a = kch[256 + oc], c = kch[257 + oc];
            u4v v;
            v[0] = a | (a << 16);
            v[1] = v[0];
            v[2] = ((oc & 1) ? c : a) | (c << 16);
            v[3] = c | (c << 16);
            st16(kx + 2048 + oc * 16, v);
        }
    }
}

__device__ __forceinline__ NOPK void w2pack_role(const Params& p, int unit, char* sw) {
    const int lane = threadIdx.x & 63, l15 = lane & 15, q4 = lane >> 4;
    const int nb = unit / KS2, ks = unit - nb * KS2;
#pragma unroll
    for (int i = 0; i < 5; ++i) {
        int c = lane + 64 * i, row = c / 10, ch = c - row * 10;
        *(u4v*)(sw + c * 16) = *(const u4v*)(p.w2 + (size_t)(ks * 32 + row) * D + nb * 80 + ch * 8);
    }
    const int q = l15 >> 2, pp = lane & 3;
    char* dst = p.ws + WS_W2P + (size_t)unit * 5120 + lane * 16;
    const char* src = sw + (16 * (q4 >> 1) + 4 * (q4 & 1) + q) * 160 + pp * 8;
#pragma unroll
    for (int dt = 0; dt < 5; ++dt) {
        u2v lo = tr_read(src + dt * 32);
        u2v hi = tr_read(src + 8 * 160 + dt * 32);
        st16(dst + dt * 1024, (u4v){lo[0], lo[1], hi[0], hi[1]});
    }
}

__global__ __launch_bounds__(512, 4) NOPK void k1(Params p) {
    extern __shared__ __attribute__((aligned(16))) char smem[];
    const int b = blockIdx.x;
    const int w = __builtin_amdgcn_readfirstlane(threadIdx.x >> 6);
    TLB()
    if (b < 224) {
        gemm1_role(p, b, smem);
    } else if (b < 288) {
        ln_role(p, b - 224, smem);
    } else if (b < 344) {
        colsum_role(p, b - 288, smem);
        const int unit = (b - 288) * 8 + w;
        if (unit < 8 * KS2) w2pack_role(p, unit, smem + w * 5120);
        if (b == 343 && threadIdx.x < 8) *(u32*)(p.ws + WS_CNT + threadIdx.x * 256) = 0u;
    } else {
        kgen_role(p, (b - 344) * 4 + (w >> 1), w & 1, smem + w * 10240);
    }
    TLE(0)
}

#define PF0 16
#define PFD 8
static constexpr int CV_WAVE = 9216;
static constexpr int K2_RED = 3 * CV_WAVE;
static constexpr int K2_YS = K2_RED + 61440;
static constexpr int K2_LDS = K2_YS + 10240;

#define G2_BUF(C, i) C "v"(af[i][0]), C "v"(af[i][1]), C "v"(af[i][2]), C "v"(af[i][3]), C "a"(wf[i][0]), C "a"(wf[i][1]), C "a"(wf[i][2]), C "a"(wf[i][3]), C "a"(wf[i][4])
#define G2_LOAD(i)                                                                                                   \
    asm volatile("buffer_load_dwordx4 %0, %9, %15, %17 offen\n\tbuffer_load_dwordx4 %4, %13, %16, %18 offen\n\t"     \
                 "buffer_load_dwordx4 %1, %10, %15, %17 offen\n\tbuffer_load_dwordx4 %2, %11, %15, %17 offen\n\t"    \
                 "buffer_load_dwordx4 %3, %12, %15, %17 offen\n\tbuffer_load_dwordx4 %5, %13, %16, %18 offen offset:1024\n\t" \
                 "buffer_load_dwordx4 %6, %13, %16, %18 offen offset:2048\n\tbuffer_load_dwordx4 %7, %13, %16, %18 offen offset:3072\n\t" \
                 "buffer_load_dwordx4 %8, %14, %16, %18 offen"                                                       \
                 : G2_BUF("=&", i) : "v"(ao[0]), "v"(ao[1]), "v"(ao[2]), "v"(ao[3]), "v"(ao[4]), "v"(ao[5]), "s"(ra), "s"(rw), \
                   "s"((i)*4096), "s"((i)*5120) : "memory");
#undef STOFF
#define STOFF (4 << 20)
__device__ __forceinline__ NOPK void conv_wave(const Params& p, int d, u32 lb, char* img, u4v (&af)[6][4], u4v (&wf)[6][5],
                                                u4v ra, u4v rw, const u32 (&ao)[6]) {
    const int lane = threadIdx.x & 63, l15 = lane & 15, q4 = lane >> 4;
    const int q = l15 >> 2, pp = lane & 3;
    const u32 kra = lb + REC_K + (32 * q4 - 4 * q + 4 * pp) * 2;
    u32 xra[2][2][2];
#pragma unroll
    for (int ap = 0; ap < 2; ++ap)
#pragma unroll
        for (int h = 0; h < 2; ++h)
#pragma unroll
            for (int g = 0; g < 2; ++g) {
                int G = l15 + 8 * g - 2 * q4 - h + 8;
                xra[ap][h][g] = lb + 4160 + G * 32 + ((ap ^ ((G >> 3) & 1)) * 16);
            }
    const u32 csrc = (lane >= 16 ? lane - 16 : (lane == 0 ? 63 : lane + 47)) * 4;
    asm volatile("s_waitcnt vmcnt(0)" ::: "memory");
    u4v kf[16], xf[4];
    f4v acc[8];
    float Y[8][7];
    float scale;
    asm volatile("ds_read_b32 %0, %1" : "=v"(scale) : "v"(lb));
#define CV_XRD(s)                                                                                          \
    {                                                                                                      \
        u2v v0, v1;                                                                                        \
        asm volatile("ds_read_b64 %0, %2 offset:%4\n\tds_read_b64 %1, %3 offset:%4" : "=&v"(v0), "=&v"(v1) \
                     : "v"(xra[(s) >> 5][0][(s)&1]), "v"(xra[(s) >> 5][1][(s)&1]), "i"((((s)&15) >> 1) * 512 + (((s) >> 4) & 1) * 8)); \
        xf[(s)&3] = (u4v){v0[0], v0[1], v1[0], v1[1]};                                                     \
    }
    CV_XRD(0) CV_XRD(1) CV_XRD(2)
#pragma unroll
    for (int e = 0; e < 16; ++e) {
        u2v lo, hi;
        asm volatile("ds_read_b64_tr_b16 %0, %2 offset:%3\n\tds_read_b64_tr_b16 %1, %2 offset:%4" : "=&v"(lo), "=&v"(hi)
                     : "v"(kra), "i"(e * 256), "i"(e * 256 + 32));
        kf[e] = (u4v){lo[0], lo[1], hi[0], hi[1]};
    }
    asm volatile("s_waitcnt lgkmcnt(0)" : "+v"(kf[0]), "+v"(kf[1]), "+v"(kf[2]), "+v"(kf[3]), "+v"(kf[4]), "+v"(kf[5]), "+v"(kf[6]), "+v"(kf[7]),
                 "+v"(kf[8]), "+v"(kf[9]), "+v"(kf[10]), "+v"(kf[11]), "+v"(kf[12]), "+v"(kf[13]), "+v"(kf[14]), "+v"(kf[15]), "+v"(scale));
#define CV_STEP(s)                                                                                         \
    {                                                                                                      \
        constexpr int a = (s) >> 4, g = (s)&15;                                                            \
        if constexpr ((s) + 3 < 64) {                                                                      \
            CV_XRD(((s) + 3) & 63)                                                                         \
            asm volatile("s_waitcnt lgkmcnt(6)" : "+v"(xf[(s)&3]));                                        \
        } else if constexpr ((s) == 61) {                                                                  \
            asm volatile("s_waitcnt lgkmcnt(0)" : "+v"(xf[1]), "+v"(xf[2]), "+v"(xf[3]));                  \
        }                                                                                                  \
        _Pragma("unroll") for (int e = (g + 1) & 1; e + g <= 15; e += 2)                                   \
            acc[(e + g) >> 1] = mfma_h(kf[e], xf[(s)&3], g == 0 ? (f4v){0.f, 0.f, 0.f, 0.f} : acc[(e + g) >> 1]); \
        if constexpr (g == 15) {                                                                           \
            _Pragma("unroll") for (int t = 0; t < 8; ++t) _Pragma("unroll") for (int r = 0; r < 4; ++r)    \
                Y[t][r + a] = (a == 0 || r == 3) ? acc[t][r] : Y[t][r + a] + acc[t][r];                    \
        }                                                                                                  \
        if constexpr ((s) >= PF0 && ((s) - PF0) % PFD == 0 && ((s) - PF0) / PFD < 6) G2_LOAD((((s) - PF0) / PFD) % 6) \
        __builtin_amdgcn_sched_barrier(0);                                                                 \
    }
#define CV_S4(s) CV_STEP(s) CV_STEP((s) + 1) CV_STEP((s) + 2) CV_STEP((s) + 3)
#define CV_S16(s) CV_S4(s) CV_S4((s) + 4) CV_S4((s) + 8) CV_S4((s) + 12)
    CV_S16(0) CV_S16(16) CV_S16(32) CV_S16(48)
    float pc[3] = {0.f, 0.f, 0.f};
#pragma unroll
    for (int t = 0; t < 8; ++t) {
        float y[4];
#pragma unroll
        for (int r = 0; r < 3; ++r) {
            float cr = __builtin_bit_cast(float, __builtin_amdgcn_ds_bpermute(csrc, __builtin_bit_cast(int, Y[t][4 + r])));
            y[r] = Y[t][r] + (lane == 0 ? pc[r] : cr);
            pc[r] = cr;
        }
        y[3] = Y[t][3];
        *(u2v*)(img + t * 512 + (16 * l15 + 4 * q4) * 2) = (u2v){pkbf(y[0] * scale, y[1] * scale), pkbf(y[2] * scale, y[3] * scale)};
    }
    char* dst = p.ws + WS_YT + (size_t)d * 4096 + lane * 16;
#pragma unroll
    for (int i = 0; i < 4; ++i) {
        u4v o = *(const u4v*)(img + i * 1024 + lane * 16);
        asm volatile("global_store_dwordx4 %0, %1, off offset:%2 sc1\n\ts_nop 1" ::"v"(dst), "v"(o), "i"(i * 1024) : "memory");
    }
    asm volatile("s_waitcnt vmcnt(0)" ::: "memory");
    if (lane == 0) __hip_atomic_fetch_add((u32*)(p.ws + WS_CNT + (d / 80) * 256), 1u, __ATOMIC_RELAXED, __HIP_MEMORY_SCOPE_AGENT);
}

__global__ __launch_bounds__(256, 1) NOPK void k2(Params p) {
    extern __shared__ __attribute__((aligned(16))) char smem[];
    const int b = blockIdx.x;
    const int w = __builtin_amdgcn_readfirstlane(threadIdx.x >> 6);
    TLB()
    const int xcd = b & 7, j = b >> 3;
    const int mb = 8 * (xcd >> 1) + (j & 7), nb = 4 * (xcd & 1) + (j >> 3);
    const int nch = (j & 1) ? 2 : 3;
    const int ch0 = xcd * 80 + (j >> 1) * 5 + ((j & 1) ? 3 : 0);
    const int nr = (j & 1) ? (w < 2 ? 1 : w + 1) : (w < 3 ? 1 : 6);
    const int r0 = ((j & 1) && w == 3) ? 5 : w;
    const char* ab = p.ws + WS_ACT + ((size_t)mb * KS2 + r0 * 6) * 4096;
    const char* wb = p.ws + WS_W2P + ((size_t)nb * KS2 + r0 * 6) * 5120;
    u4v ra = {(u32)(size_t)ab, (u32)((size_t)ab >> 32), (u32)nr * 6u * 4096u, 0x00020000u};
    u4v rw = {(u32)(size_t)wb, (u32)((size_t)wb >> 32), (u32)nr * 6u * 5120u, 0x00020000u};
    const u32 lo16 = (threadIdx.x & 63) * 16;
    const u32 lb = lds_addr(smem);
    const u32 ao[6] = {lo16 + (w & 3) * 1024, lo16 + ((w + 1) & 3) * 1024, lo16 + ((w + 2) & 3) * 1024, lo16 + ((w + 3) & 3) * 1024, lo16, lo16 + 4096};
    SGDECL SG(0)
    if (w < nch) {
        const char* src = p.ws + WS_REC + (size_t)(ch0 + w) * REC;
#pragma unroll
        for (int i = 0; i < 9; ++i) GLDS16(lb + w * CV_WAVE + i * 1024, lo16, src + i * 1024);
    }
    u4v af[6][4], wf[6][5];
    SG(1)
    if (w < nch) {
        conv_wave(p, ch0 + w, lb + w * CV_WAVE, smem + w * CV_WAVE, af, wf, ra, rw, ao);
    } else {
        G2_LOAD(0) G2_LOAD(1) G2_LOAD(2) G2_LOAD(3) G2_LOAD(4) G2_LOAD(5)
    }
    SG(2)
    f4v acc[5][4];
#pragma unroll
    for (int dt = 0; dt < 5; ++dt)
#pragma unroll
        for (int tt = 0; tt < 4; ++tt) acc[dt][tt] = (f4v){0.f, 0.f, 0.f, 0.f};
#define G2_MF(d, w_, a_) "v_mfma_f32_16x16x32_bf16 %" #d ", %" #w_ ", %" #a_ ", %" #d "\n\t"
#define G2_M4(dt, i, VO, OFF)                                                                           \
    asm volatile(G2_MF(0, 4, 5) G2_MF(1, 4, 6) G2_MF(2, 4, 7) G2_MF(3, 4, 8)                            \
                 "buffer_load_dwordx4 %4, %9, %10, %11 offen offset:" #OFF                              \
                 : "+a"(acc[dt][0]), "+a"(acc[dt][1]), "+a"(acc[dt][2]), "+a"(acc[dt][3]), "+a"(wf[i][dt]) \
                 : "v"(af[i][0]), "v"(af[i][1]), "v"(af[i][2]), "v"(af[i][3]), "v"(ao[VO]), "s"(rw), "s"(sw) : "memory");
#define G2_STEP(i)                                                                                      \
    {                                                                                                   \
        asm volatile("s_waitcnt vmcnt(45)" : G2_BUF("+", i)::"memory");                                 \
        G2_M4(0, i, 4, 0) G2_M4(1, i, 4, 1024) G2_M4(2, i, 4, 2048) G2_M4(3, i, 4, 3072) G2_M4(4, i, 5, 0) \
        asm volatile("buffer_load_dwordx4 %0, %4, %8, %9 offen\n\tbuffer_load_dwordx4 %1, %5, %8, %9 offen\n\t" \
                     "buffer_load_dwordx4 %2, %6, %8, %9 offen\n\tbuffer_load_dwordx4 %3, %7, %8, %9 offen" \
                     : "+v"(af[i][0]), "+v"(af[i][1]), "+v"(af[i][2]), "+v"(af[i][3])                   \
                     : "v"(ao[0]), "v"(ao[1]), "v"(ao[2]), "v"(ao[3]), "s"(ra), "s"(sa) : "memory");    \
        sa += 4096; sw += 5120;                                                                         \
    }
    u32 sa = 6 * 4096, sw = 6 * 5120;
    asm volatile("s_nop 1" : "+a"(acc[0][0]), "+a"(acc[0][1]), "+a"(acc[0][2]), "+a"(acc[0][3]));
#pragma nounroll
    for (int r = 0; r < nr; ++r) { G2_STEP(0) G2_STEP(1) G2_STEP(2) G2_STEP(3) G2_STEP(4) G2_STEP(5) }
    asm volatile("s_waitcnt vmcnt(0)" : G2_BUF("+", 0), G2_BUF("+", 1), G2_BUF("+", 2)::"memory");
    asm volatile("" : G2_BUF("+", 3), G2_BUF("+", 4), G2_BUF("+", 5)::"memory");
    asm volatile("s_nop 7\n\ts_nop 7" : "+a"(acc[4][0]), "+a"(acc[4][1]), "+a"(acc[4][2]), "+a"(acc[4][3]) :: "memory");
    SG(3)
    int lane = threadIdx.x & 63;
    asm volatile("" : "+v"(lane));
    const int l15 = lane & 15, q4 = lane >> 4;
    const int t = mb * 64 + w * 16 + l15;
    const int dbase = nb * 80 + 4 * q4;
    u2v xv[5];
#pragma unroll
    for (int dt = 0; dt < 5; ++dt) xv[dt] = *(const u2v*)(p.x + (size_t)t * D + dbase + dt * 16);
    char* red = smem + K2_RED;
#pragma unroll
    for (int tt = 1; tt < 4; ++tt) {
        char* dst = red + (((tt + w) & 3) * 3 + tt - 1) * 5120 + lane * 16;
#pragma unroll
        for (int dt = 0; dt < 5; ++dt) *(f4v*)(dst + dt * 1024) = acc[dt][tt];
    }
    if (w == 0) {
        const u32* cnt = (const u32*)(p.ws + WS_CNT + nb * 256);
        unsigned spins = 0;
        while (__hip_atomic_load(cnt, __ATOMIC_RELAXED, __HIP_MEMORY_SCOPE_AGENT) < 80u && ++spins < (1u << 22)) __builtin_amdgcn_s_sleep(8);
        const char* yb = p.ws + WS_YT + (size_t)(nb * 80) * 4096 + (size_t)mb * 128;
        const u32 yo = (lane >> 3) * 4096 + (lane & 7) * 16;
#pragma unroll
        for (int i = 0; i < 10; ++i)
            asm volatile("s_mov_b32 m0, %0\n\ts_nop 0\n\tglobal_load_lds_dwordx4 %1, %2 sc1" ::"s"(lb + K2_YS + i * 1024), "v"(yo), "s"(yb + i * 32768) : "memory");
        asm volatile("s_waitcnt vmcnt(0)" ::: "memory");
    }
    RAWBAR()
    SG(4)
    const char* ys = smem + K2_YS + (4 * q4 + (l15 >> 2)) * 128 + w * 32 + (lane & 3) * 8;
    const char* rsrc = red + w * 15360 + lane * 16;
#pragma unroll
    for (int dt = 0; dt < 5; ++dt) {
        const u2v sv = tr_read(ys + dt * 2048);
        const f4v g = (acc[dt][0] + *(const f4v*)(rsrc + dt * 1024)) + (*(const f4v*)(rsrc + 5120 + dt * 1024) + *(const f4v*)(rsrc + 10240 + dt * 1024));
        u2v o;
#pragma unroll
        for (int r2 = 0; r2 < 2; ++r2) {
            const float p0 = g[2 * r2], p1 = g[2 * r2 + 1];
            const float g0 = p0 * __builtin_amdgcn_rcpf(1.0f + __builtin_amdgcn_exp2f(-1.44269504f * p0));
            const float g1 = p1 * __builtin_amdgcn_rcpf(1.0f + __builtin_amdgcn_exp2f(-1.44269504f * p1));
            o[r2] = pkbf(fmaf(bflo(sv[r2]), g0, bflo(xv[dt][r2])), fmaf(bfhi(sv[r2]), g1, bfhi(xv[dt][r2])));
        }
        *(u2v*)(p.out + (size_t)t * D + dbase + dt * 16) = o;
    }
    SG(5) SGDUMP(6)
    TLE(1 << 20)
}

extern "C" void kernel_launch(void* const* d_in, const int* in_sizes, int n_in, void* d_out, int out_size, void* d_ws,
                              size_t ws_size, hipStream_t stream) {
    Params p;
    p.x = (const u16*)d_in[0];
    p.Are = (const u16*)d_in[1]; p.Aim = (const u16*)d_in[2]; p.Bre = (const u16*)d_in[3]; p.Bim = (const u16*)d_in[4];
    p.Cre = (const u16*)d_in[5]; p.Cim = (const u16*)d_in[6]; p.logdt = (const u16*)d_in[7]; p.Dskip = (const u16*)d_in[8];
    p.w1 = (const u16*)d_in[10]; p.b1 = (const u16*)d_in[11]; p.w2 = (const u16*)d_in[12];
    p.out = (u16*)d_out;
    p.ws = (char*)d_ws;
    (void)hipFuncSetAttribute((const void*)k1, hipFuncAttributeMaxDynamicSharedMemorySize, LDS_BYTES);
    (void)hipFuncSetAttribute((const void*)k2, hipFuncAttributeMaxDynamicSharedMemorySize, K2_LDS);
    k1<<<504, 512, LDS_BYTES, stream>>>(p);
    k2<<<256, 256, K2_LDS, stream>>>(p);
}
```

```cpp
#include <hip/hip_runtime.h>
#include <stdint.h>

#if defined(__HIP_DEVICE_COMPILE__) && !defined(ALLOWPK)
#define NOPK __attribute__((target("no-packed-fp32-ops")))
#else
#define NOPK
#endif
typedef unsigned short u16;
typedef unsigned int u32;
typedef short s4v __attribute__((ext_vector_type(4)));
typedef u32 u2v __attribute__((ext_vector_type(2)));
typedef u32 u4v __attribute__((ext_vector_type(4)));
typedef float f2v __attribute__((ext_vector_type(2)));
typedef float f4v __attribute__((ext_vector_type(4)));
typedef _Float16 h2v __attribute__((ext_vector_type(2)));
typedef _Float16 h8v __attribute__((ext_vector_type(8)));
typedef __bf16 b2v __attribute__((ext_vector_type(2)));
typedef __bf16 b8v __attribute__((ext_vector_type(8)));
#define LDSP(T, p) ((__attribute__((address_space(3))) T*)(p))
#define GLB(T, p) ((__attribute__((address_space(1))) T*)(p))

static constexpr int L = 2048, D = 640, NS = 128, F = 1728;
static constexpr int KS2 = F / 32;

static constexpr size_t WS_ACT = 0;
static constexpr size_t WS_W2P = WS_ACT + (size_t)L * F * 2;
static constexpr int REC_K = 32, REC_X = 4160 + 512, REC = 8768;
static constexpr size_t WS_REC = WS_W2P + (size_t)F * D * 2;
static constexpr size_t WS_YT = WS_REC + (size_t)641 * REC + 192;
static constexpr size_t WS_CNT = WS_YT + (size_t)L * D * 2;
static_assert(WS_REC % 256 == 0 && WS_YT % 256 == 0, "alignment");

static constexpr int LDS_BYTES = 80 * 1024;

__device__ __forceinline__ NOPK u32 f2u(float v) { return __builtin_bit_cast(u32, v); }
__device__ __forceinline__ NOPK float bf2f(u32 v) { return __builtin_bit_cast(float, (u32)(v << 16)); }
__device__ __forceinline__ NOPK float bflo(u32 v) { return __builtin_bit_cast(float, (u32)(v << 16)); }
__device__ __forceinline__ NOPK float bfhi(u32 v) { return __builtin_bit_cast(float, (u32)(v & 0xffff0000u)); }
__device__ __forceinline__ NOPK u32 pkbf(float a, float b) {
    f2v v = {a, b};
    return __builtin_bit_cast(u32, __builtin_convertvector(v, b2v));
}
__device__ __forceinline__ NOPK u32 pkh(float a, float b) {
    f2v v = {a, b};
    return __builtin_bit_cast(u32, __builtin_convertvector(v, h2v));
}
__device__ __forceinline__ NOPK float rbf(float a) { return bflo(pkbf(a, a)); }
__device__ __forceinline__ NOPK f4v mfma_bf(u4v a, u4v b, f4v c) {
    return __builtin_amdgcn_mfma_f32_16x16x32_bf16(__builtin_bit_cast(b8v, a), __builtin_bit_cast(b8v, b), c, 0, 0, 0);
}
__device__ __forceinline__ NOPK f4v mfma_h(u4v a, u4v b, f4v c) {
    return __builtin_amdgcn_mfma_f32_16x16x32_f16(__builtin_bit_cast(h8v, a), __builtin_bit_cast(h8v, b), c, 0, 0, 0);
}
__device__ __forceinline__ NOPK u2v tr_read(const char* p) {
    return __builtin_bit_cast(u2v, __builtin_amdgcn_ds_read_tr16_b64_v4i16(LDSP(s4v, p)));
}
__device__ __forceinline__ NOPK float dot2bf(u32 a, u32 b, float c) {
    return __builtin_amdgcn_fdot2_f32_bf16(__builtin_bit_cast(b2v, a), __builtin_bit_cast(b2v, b), c, false);
}
__device__ __forceinline__ NOPK void glds16(const void* g, void* l) {
    __builtin_amdgcn_global_load_lds((const __attribute__((address_space(1))) void*)g,
                                     (__attribute__((address_space(3))) void*)l, 16, 0, 0);
}
__device__ __forceinline__ NOPK u32 lds_addr(const void* l) { return (u32)(size_t)(__attribute__((address_space(3))) const char*)l; }
#define GLDS16(ldsa, voff, sbase) \
    asm volatile("s_mov_b32 m0, %0\n\ts_nop 0\n\tglobal_load_lds_dwordx4 %1, %2" ::"s"(ldsa), "v"(voff), "s"(sbase) : "memory")
#define GLDS4(ldsa, voff, sbase) \
    asm volatile("s_mov_b32 m0, %0\n\ts_nop 0\n\tglobal_load_lds_dword %1, %2" ::"s"(ldsa), "v"(voff), "s"(sbase) : "memory")
template <int CTRL> __device__ __forceinline__ NOPK float dpp(float v) {
    return __builtin_bit_cast(float, __builtin_amdgcn_update_dpp(0, __builtin_bit_cast(int, v), CTRL, 0xf, 0xf, true));
}
__device__ __forceinline__ NOPK float swap32(float v) {
    const u32 b = __builtin_bit_cast(u32, v);
    const auto r = __builtin_amdgcn_permlane32_swap(b, b, false, false);
    return __builtin_bit_cast(float, (threadIdx.x & 32) ? r[0] : r[1]);
}
__device__ __forceinline__ NOPK float red16(float v) {
    v += dpp<0xB1>(v); v += dpp<0x4E>(v); v += dpp<0x141>(v); v += dpp<0x140>(v);
    return v;
}
__device__ __forceinline__ NOPK float max64(float v) {
    v = fmaxf(v, dpp<0xB1>(v)); v = fmaxf(v, dpp<0x4E>(v)); v = fmaxf(v, dpp<0x141>(v)); v = fmaxf(v, dpp<0x140>(v));
    const int b = __builtin_bit_cast(int, v);
    float a0 = __builtin_bit_cast(float, __builtin_amdgcn_readlane(b, 0)), a1 = __builtin_bit_cast(float, __builtin_amdgcn_readlane(b, 16));
    float a2 = __builtin_bit_cast(float, __builtin_amdgcn_readlane(b, 32)), a3 = __builtin_bit_cast(float, __builtin_amdgcn_readlane(b, 48));
    return fmaxf(fmaxf(a0, a1), fmaxf(a2, a3));
}
__device__ __forceinline__ NOPK void st16(void* ptr, u4v v) {
    asm volatile("global_store_dwordx4 %0, %1, off sc1\n\ts_nop 1" ::"v"(ptr), "v"(v) : "memory");
}
__device__ __forceinline__ NOPK void ln_stats(float s1, float s2, float& mb, float& den) {
    float mean = s1 * (1.0f / D);
    float var = fmaxf(s2 * (1.0f / D) - mean * mean, 0.f);
    mb = rbf(mean);
    den = rbf(__builtin_amdgcn_sqrtf(rbf(rbf(var) + 1e-5f)));
}
__device__ __forceinline__ NOPK float ln_apply(float x, float mb, float rden) { return rbf(rbf(x - mb) * rden); }

#define SG(i) {}
#define SGDECL
#define SGDUMP(n)
#define TLB()
#define TLE(off)
struct Params {
    const u16 *x, *Are, *Aim, *Bre, *Bim, *Cre, *Cim, *logdt, *Dskip, *w1, *b1, *w2;
    u16* out;
    char* ws;
};

typedef float f16v __attribute__((ext_vector_type(16)));
__device__ __forceinline__ NOPK f16v mfma32_bf(u4v a, u4v b, f16v c) {
    return __builtin_amdgcn_mfma_f32_32x32x16_bf16(__builtin_bit_cast(b8v, a), __builtin_bit_cast(b8v, b), c, 0, 0, 0);
}
#define STATV 0
#define VMWAIT(N) asm volatile("s_waitcnt vmcnt(" #N ") lgkmcnt(0)" ::: "memory")

#define RAWBAR() { asm volatile("s_waitcnt lgkmcnt(0)" ::: "memory"); __builtin_amdgcn_s_barrier(); asm volatile("" ::: "memory"); }

__device__ __forceinline__ NOPK void gemm1_role(const Params& p, int b, char* smem) {
    const int tid = threadIdx.x, lane = tid & 63;
    const int w = __builtin_amdgcn_readfirstlane(tid >> 6);
    const int wt = w >> 2, wf = w & 3;
    const int xcd = b & 7, j = b >> 3;
    const int mb = 4 * (xcd >> 1) + (j & 3), nt = 7 * (xcd & 1) + (j >> 2);
    const int rot = 0;
    const int l31 = lane & 31, hh = lane >> 5;
    const bool half = (nt == 13);
    SGDECL
#define STOFF 0
    SG(0)
    u2v bia[4];
    {
        const u32 bo = (u32)min(nt * 128 + 32 * wf + 4 * hh, F - 28) * 2u;
        asm volatile("global_load_dwordx2 %0, %4, %5\n\tglobal_load_dwordx2 %1, %4, %5 offset:16\n\t"
                     "global_load_dwordx2 %2, %4, %5 offset:32\n\tglobal_load_dwordx2 %3, %4, %5 offset:48"
                     : "=&v"(bia[0]), "=&v"(bia[1]), "=&v"(bia[2]), "=&v"(bia[3]) : "v"(bo), "s"(p.b1) : "memory");
    }
    u32 xo[2], wo;
#pragma unroll
    for (int i = 0; i < 2; ++i) {
        int P = (2 * w + i) * 64 + lane, row = P >> 3, ch = (P & 7) ^ ((row >> 1) & 7);
        xo[i] = (u32)(row * (D * 2) + ch * 16);
    }
    {
        int P = w * 64 + lane, k = P >> 4, ch = (P & 15) ^ ((k & 3) << 2);
        if (half) ch &= 7;
        wo = (u32)(k * (F * 2) + ch * 16);
    }
    const char* xb = (const char*)p.x + (size_t)mb * 128 * (D * 2);
    const char* wb = (const char*)p.w1 + nt * 256;
    const u32 lx = lds_addr(smem) + w * 2048, lw = lds_addr(smem) + 49152 + w * 1024;
#define SROT(st) (((st) + rot >= 10) ? (st) + rot - 10 : (st) + rot)
#define G1_X(st) { const char* s_ = xb + SROT(st) * 128; GLDS16(lx + ((st) % 3) * 16384, xo[0], s_); GLDS16(lx + ((st) % 3) * 16384 + 1024, xo[1], s_); }
#define G1_W(h) { const char* s_ = wb + (size_t)(SROT((h) >> 1) * 64 + ((h)&1) * 32) * (F * 2); GLDS16(lw + ((h)&3) * 8192, wo, s_); }
    G1_X(0) G1_W(0) G1_W(1) G1_X(1) G1_W(2) G1_W(3) G1_X(2)

    const int q = (lane & 15) >> 2, pp = lane & 3, blk = (lane >> 4) & 1;
    const int ro = (wf & 1) ^ wt, ri = wf >> 1;
    const char* xa[4][2];
    const char* wa[2];
#pragma unroll
    for (int u = 0; u < 4; ++u)
#pragma unroll
        for (int i = 0; i < 2; ++i)
            xa[u][i] = smem + (64 * wt + 32 * (i ^ ri) + l31) * 128 + (((2 * (u ^ ro) + hh) ^ ((l31 >> 1) & 7)) << 4);
#pragma unroll
    for (int o = 0; o < 2; ++o)
        wa[o] = smem + 49152 + (16 * (o ^ ro) + 8 * hh + q) * 256 + (((4 * wf + 2 * blk + (pp >> 1)) ^ (q << 2)) << 4) + 8 * (pp & 1);

    f16v acc[2];
#pragma unroll
    for (int i = 0; i < 16; ++i) acc[0][i] = acc[1][i] = 0.f;
    u4v xf[2][2][2], wfr[2][2];
    float s1a = 0.f, s1b = 0.f, s2a = 0.f, s2b = 0.f, csa = 0.f, csb = 0.f;
    SG(1)
#define G1_READ(h, o)                                                                                    \
    {                                                                                                    \
        u2v lo = tr_read(wa[o] + ((h)&3) * 8192), hi = tr_read(wa[o] + ((h)&3) * 8192 + 1024);           \
        wfr[(h)&1][o] = (u4v){lo[0], lo[1], hi[0], hi[1]};                                               \
        xf[(h)&1][o][0] = *(const u4v*)(xa[((h)&1) * 2 + o][0] + (((h) >> 1) % 3) * 16384);              \
        xf[(h)&1][o][1] = *(const u4v*)(xa[((h)&1) * 2 + o][1] + (((h) >> 1) % 3) * 16384);              \
    }
#define G1_M(h, o, i)                                                                                    \
    if ((h) >= 0) {                                                                                      \
        acc[i] = mfma32_bf(wfr[(h)&1][o], xf[(h)&1][o][i], acc[i]);                                      \
        asm volatile("" : "+v"(acc[i]));                                                                 \
    }                                                                                                    \
    __builtin_amdgcn_sched_barrier(0);
#define G1_STAT(h, e)                                                                                    \
    if ((h) >= 0) {                                                                                      \
        const u32 xv_ = xf[(h)&1][0][0][e];                                                              \
        s1a = dot2bf(xv_, 0x3f803f80u, s1a); s2a = dot2bf(xv_, xv_, s2a);                                \
        csa = dot2bf(wfr[(h)&1][0][e], 0x3f803f80u, csa);                                                \
    }
#define G1_PIN() asm volatile("" : "+v"(s1a), "+v"(s1b), "+v"(s2a), "+v"(s2b), "+v"(csa), "+v"(csb)); __builtin_amdgcn_sched_barrier(0);
#define G1_IT(h, NW)                                                                                     \
    {                                                                                                    \
        VMWAIT(NW);                                                                                      \
        __builtin_amdgcn_s_barrier();                                                                    \
        G1_M(h, 0, 0)                                                                                    \
        G1_READ((h) + 1, 0) G1_STAT(h, 0) G1_PIN()                                                       \
        G1_M(h, 0, 1)                                                                                    \
        G1_READ((h) + 1, 1) G1_STAT(h, 1) G1_PIN()                                                       \
        G1_M(h, 1, 0)                                                                                    \
        if ((h) >= 0 && (h) + 4 <= 19) G1_W((h) + 4)                                                     \
        G1_STAT(h, 2) G1_PIN()                                                                           \
        G1_M(h, 1, 1)                                                                                    \
        if ((h) >= 1 && ((h)&1) && ((h) - 1) / 2 + 3 <= 9) G1_X(((h) - 1) / 2 + 3)                       \
        G1_STAT(h, 3) G1_PIN()                                                                           \
    }
    G1_IT(-1, 7) G1_IT(0, 6) G1_IT(1, 4) G1_IT(2, 6) G1_IT(3, 4) SG(2) G1_IT(4, 6) G1_IT(5, 4) G1_IT(6, 6) G1_IT(7, 4)
    G1_IT(8, 6) SG(3) G1_IT(9, 4) G1_IT(10, 6) G1_IT(11, 4) G1_IT(12, 6) G1_IT(13, 4) SG(4) G1_IT(14, 6) G1_IT(15, 4)
    G1_IT(16, 4) G1_IT(17, 1) G1_IT(18, 0)
    G1_M(19, 0, 0) G1_M(19, 0, 1) G1_M(19, 1, 0) G1_M(19, 1, 1) G1_STAT(19, 0) G1_STAT(19, 1) G1_STAT(19, 2) G1_STAT(19, 3)
    s1a += s1b; s2a += s2b; csa += csb;
    SG(5)
    asm volatile("" : "+v"(bia[0]), "+v"(bia[1]), "+v"(bia[2]), "+v"(bia[3]));
    char* rsb = smem + 16384;
    char* csl = smem + 16384 + 2048;
    {
        const float a1 = s1a + swap32(s1a), a2 = s2a + swap32(s2a), ac = csa + swap32(csa);
        if (hh == 0) *(f2v*)(rsb + ((64 * wt + 32 * ri + l31) * 2 + (wf & 1)) * 8) = (f2v){a1, a2};
        else *(float*)(csl + ((32 * wf + l31) * 2 + wt) * 4) = ac;
    }
    RAWBAR()
    SG(6)
    if (!(half && wf >= 2)) {
        float csv[16], bsv[16];
#pragma unroll
        for (int g = 0; g < 4; ++g) {
            const f4v c0 = *(const f4v*)(csl + (32 * wf + 8 * g + 4 * hh) * 8), c1 = *(const f4v*)(csl + (32 * wf + 8 * g + 4 * hh) * 8 + 16);
            csv[4 * g] = c0[0] + c0[1]; csv[4 * g + 1] = c0[2] + c0[3]; csv[4 * g + 2] = c1[0] + c1[1]; csv[4 * g + 3] = c1[2] + c1[3];
            bsv[4 * g] = bflo(bia[g][0]) * 1.44269504f; bsv[4 * g + 1] = bfhi(bia[g][0]) * 1.44269504f;
            bsv[4 * g + 2] = bflo(bia[g][1]) * 1.44269504f; bsv[4 * g + 3] = bfhi(bia[g][1]) * 1.44269504f;
        }
        char* actb = p.ws + WS_ACT + ((size_t)(mb * 2 + wt) * KS2 + nt * 4 + wf) * 4096 + (size_t)((l31 >> 4) * 1024 + ((l31 & 15) + 16 * hh) * 16);
#pragma unroll
        for (int i = 0; i < 2; ++i) {
            const f4v u0 = *(const f4v*)(rsb + (64 * wt + 32 * (i ^ ri) + l31) * 16);
            float mean, den;
            ln_stats(u0[0] + u0[2], u0[1] + u0[3], mean, den);
            const float rdl = __builtin_amdgcn_rcpf(den) * 1.44269504f;
#pragma unroll
            for (int c = 0; c < 2; ++c) {
                u4v o;
#pragma unroll
                for (int e = 0; e < 4; ++e) {
                    float r[2];
#pragma unroll
                    for (int z = 0; z < 2; ++z) {
                        const int ix = 8 * c + 2 * e + z;
                        const float u = fmaf(fmaf(-mean, csv[ix], acc[i][ix]), rdl, bsv[ix]);
                        const float e1 = __builtin_amdgcn_exp2f(u) + 1.0f;
                        r[z] = u * fmaf(__builtin_amdgcn_rcpf(fmaf(e1, e1, 1.0f)), -1.38629436f, 0.69314718f);
                    }
                    o[e] = pkbf(r[0], r[1]);
                }
                st16(actb + (i ^ ri) * 2048 + c * 512, o);
            }
        }
    }
    SG(7)
    SGDUMP(8)
}

__device__ __forceinline__ NOPK void ln_role(const Params& p, int rb, char* smem) {
    const int tid = threadIdx.x, lane = tid & 63;
    const int w = __builtin_amdgcn_readfirstlane(tid >> 6);
    const int rl = 4 * w + (lane >> 4), t = rb * 32 + rl, c15 = lane & 15;
    u4v v[5];
#pragma unroll
    for (int i = 0; i < 5; ++i) v[i] = *(const u4v*)(p.x + (size_t)t * D + (c15 + 16 * i) * 8);
    float s = 0.f;
#pragma unroll
    for (int i = 0; i < 5; ++i)
#pragma unroll
        for (int e = 0; e < 4; ++e) s += bflo(v[i][e]) + bfhi(v[i][e]);
    const float mean = red16(s) * (1.0f / D);
    float qv = 0.f;
#pragma unroll
    for (int i = 0; i < 5; ++i)
#pragma unroll
        for (int e = 0; e < 4; ++e) {
            float a = bflo(v[i][e]) - mean, c = bfhi(v[i][e]) - mean;
            qv = fmaf(a, a, qv); qv = fmaf(c, c, qv);
        }
    const float var = red16(qv) * (1.0f / D);
    const float mbf = rbf(mean);
    const float rd = 1.0f / rbf(__builtin_sqrtf(rbf(rbf(var) + 1e-5f)));
#pragma unroll
    for (int i = 0; i < 5; ++i) {
        u2v o0, o1;
        o0[0] = pkh(ln_apply(bflo(v[i][0]), mbf, rd), ln_apply(bfhi(v[i][0]), mbf, rd));
        o0[1] = pkh(ln_apply(bflo(v[i][1]), mbf, rd), ln_apply(bfhi(v[i][1]), mbf, rd));
        o1[0] = pkh(ln_apply(bflo(v[i][2]), mbf, rd), ln_apply(bfhi(v[i][2]), mbf, rd));
        o1[1] = pkh(ln_apply(bflo(v[i][3]), mbf, rd), ln_apply(bfhi(v[i][3]), mbf, rd));
        *(u2v*)(smem + rl * 1288 + (c15 + 16 * i) * 16) = o0;
        *(u2v*)(smem + rl * 1288 + (c15 + 16 * i) * 16 + 8) = o1;
    }
    RAWBAR()
    const int G = lane >> 4, q = c15 >> 2, pp = lane & 3;
    const char* src = smem + (16 * (G >> 1) + 4 * q + 2 * (G & 1)) * 1288 + pp * 8;
    char* dst = p.ws + WS_REC + (size_t)c15 * REC + REC_X + (size_t)rb * 64 + (G ^ ((rb >> 2) & 1)) * 16;
#pragma unroll
    for (int it = 0; it < 5; ++it) {
        const int cg = w * 5 + it;
        u2v lo = tr_read(src + cg * 32), hi = tr_read(src + cg * 32 + 1288);
        st16(dst + (size_t)cg * 16 * REC, (u4v){lo[0], lo[1], hi[0], hi[1]});
    }
}

struct cplx2 { f2v r, i; };
__device__ __forceinline__ NOPK cplx2 cmul(cplx2 a, cplx2 b) {
    cplx2 o;
    o.r = a.r * b.r - a.i * b.i;
    o.i = a.r * b.i + a.i * b.r;
    return o;
}
struct cplxh { h2v r, i; };
__device__ __forceinline__ NOPK cplxh toh(cplx2 a) { return {__builtin_convertvector(a.r, h2v), __builtin_convertvector(a.i, h2v)}; }
__device__ __forceinline__ NOPK cplx2 cexp2(f2v zr, f2v zi, float c) {
    cplx2 o;
#pragma unroll
    for (int m = 0; m < 2; ++m) {
        float mag = __builtin_amdgcn_exp2f(zr[m] * c * 1.44269504089f);
        float ph = zi[m] * c;
        const float CH = 0.15915494f, CL = 6.4206382e-9f;
        float hi = ph * CH;
        float lo = fmaf(ph, CH, -hi) + ph * CL;
        float fr = __builtin_amdgcn_fractf(hi) + lo;
        o.r[m] = mag * __builtin_amdgcn_cosf(fr);
        o.i[m] = mag * __builtin_amdgcn_sinf(fr);
    }
    return o;
}
static constexpr int KG_ROW = 544;

__device__ __forceinline__ NOPK void kgen_role(const Params& p, int d, int part, char* sw  ) {
    const int lane = threadIdx.x & 63, l15 = lane & 15, q4 = lane >> 4;
    const int o = d * NS + 2 * lane;
    u32 are = *(const u32*)(p.Are + o), aim = *(const u32*)(p.Aim + o), bre = *(const u32*)(p.Bre + o);
    u32 bim = *(const u32*)(p.Bim + o), cre = *(const u32*)(p.Cre + o), cim = *(const u32*)(p.Cim + o);
    float dt = rbf(expf(bf2f(p.logdt[d])));
    f2v zr = {dt * bflo(are), dt * bfhi(are)}, zi = {dt * bflo(aim), dt * bfhi(aim)};
    cplx2 dB = {{dt * bflo(bre), dt * bfhi(bre)}, {dt * bflo(bim), dt * bfhi(bim)}};
    cplx2 C = {{bflo(cre), bfhi(cre)}, {bflo(cim), bfhi(cim)}};
    cplx2 W = cmul(C, dB);
    float wm = fmaxf(fmaxf(fabsf(W.r[0]), fabsf(W.r[1])), fmaxf(fabsf(W.i[0]), fabsf(W.i[1])));
    const float dsk = bf2f(p.Dskip[d]);
    wm = fmaxf(max64(wm), fabsf(dsk) * (1.0f / 128.0f));
    int eU = 14 - __builtin_amdgcn_frexp_expf(wm);
    eU = min(max(eU, -100), 100);
    const float sU = __builtin_amdgcn_ldexpf(1.0f, eU);
    W.r *= sU; W.i *= sU;
    char* rec = p.ws + WS_REC + (size_t)d * REC;
    if (part == 0 && lane < 34) {
        u4v z = {0u, 0u, 0u, 0u};
        if (lane == 0) z[0] = f2u(__builtin_amdgcn_ldexpf(1.0f, 8 - eU));
        *(u4v*)(rec + (lane < 2 ? lane * 16 : 4160 + (lane - 2) * 16)) = z;
    }
    const float KSC = 2.384185791015625e-07f;

    char* r0 = sw;
    char* kc = sw + 16 * KG_ROW;
#define KG_PUT(row, v) *(u2v*)(r0 + (row)*KG_ROW + lane * 8) = (u2v){pkh((v).r[0], (v).r[1]), pkh((v).i[0], (v).i[1])}
#define KG_PUTH(row, T_, E_)                                                                            \
    *(u2v*)(r0 + (row)*KG_ROW + lane * 8) = (u2v){__builtin_bit_cast(u32, __builtin_elementwise_fma(T_.r, E_.r, -(T_.i * E_.i))), \
                                                  __builtin_bit_cast(u32, __builtin_elementwise_fma(T_.r, E_.i, T_.i * E_.r))}
#define KG_MMA(A)                                         \
    A = (f4v){0.f, 0.f, 0.f, 0.f};                        \
    _Pragma("unroll") for (int ks = 0; ks < 8; ++ks) A = mfma_h(*(const u4v*)(r0 + l15 * KG_ROW + ks * 64 + q4 * 16), vf[ks], A);
    const cplx2 G = cexp2(zr, zi, 128.f);
    u16* kch = (u16*)kc;
    char* kx = rec + REC_K;
    u4v vf[8];
    if (part == 0) {
        {
            cplx2 v1 = cexp2(zr, zi, 8.f);
            cplx2 v = {{16384.f, 16384.f}, {0.f, 0.f}};
#pragma unroll
            for (int jj = 0; jj < 16; ++jj) {
                cplx2 t = {v.r, -v.i};
                KG_PUT(jj, t);
                if (jj < 15) v = cmul(v, v1);
            }
        }
        RAWBAR()
#pragma unroll
        for (int ks = 0; ks < 8; ++ks) vf[ks] = *(const u4v*)(r0 + l15 * KG_ROW + ks * 64 + q4 * 16);
        cplx2 E[8];
        E[1] = cexp2(zr, zi, 1.f);
        E[2] = cmul(E[1], E[1]); E[3] = cmul(E[2], E[1]); E[4] = cmul(E[2], E[2]);
        E[5] = cmul(E[4], E[1]); E[6] = cmul(E[4], E[2]); E[7] = cmul(E[4], E[3]);
        const cplxh T0 = toh(W), T1 = toh(cmul(W, G));
        RAWBAR()
        *(u2v*)(r0 + lane * 8) = (u2v){__builtin_bit_cast(u32, T0.r), __builtin_bit_cast(u32, T0.i)};
        *(u2v*)(r0 + 8 * KG_ROW + lane * 8) = (u2v){__builtin_bit_cast(u32, T1.r), __builtin_bit_cast(u32, T1.i)};
#pragma unroll
        for (int c = 1; c < 8; ++c) {
            const cplxh Eh = toh(E[c]);
            KG_PUTH(c, T0, Eh);
            KG_PUTH(8 + c, T1, Eh);
        }
        f4v acc;
        KG_MMA(acc)
        if (lane == 0) acc[0] = fmaf(dsk * sU, 16384.f, acc[0]);
        *(u2v*)(kx + (128 * (q4 >> 1) + 4 * (q4 & 1) + 8 * l15) * 2) = (u2v){pkh(acc[0] * KSC, acc[1] * KSC), pkh(acc[2] * KSC, acc[3] * KSC)};
    } else {
        const cplx2 E2 = cexp2(zr, zi, 2.f);
        const cplx2 E4 = cmul(E2, E2);
        const cplxh E2h = toh(E2), E4h = toh(E4), E6h = toh(cmul(E4, E2));
        cplx2 T = cmul(W, cexp2(zr, zi, 256.f));
#pragma unroll
        for (int a = 0; a < 6; ++a) {
            const cplxh Th = toh(T);
            const int row = a < 2 ? 4 * a : 4 + 2 * a;
            *(u2v*)(r0 + row * KG_ROW + lane * 8) = (u2v){__builtin_bit_cast(u32, Th.r), __builtin_bit_cast(u32, Th.i)};
            if (a < 2) { KG_PUTH(row + 1, Th, E2h); KG_PUTH(row + 2, Th, E4h); KG_PUTH(row + 3, Th, E6h); }
            else KG_PUTH(row + 1, Th, E4h);
            if (a < 5) T = cmul(T, G);
        }
        RAWBAR()
#pragma unroll
        for (int ks = 0; ks < 8; ++ks) vf[ks] = *(const u4v*)(r0 - 10240 + l15 * KG_ROW + ks * 64 + q4 * 16);
        RAWBAR()
        f4v acc1, acc2;
        KG_MMA(acc1)
        T = cmul(W, cexp2(zr, zi, 1024.f));
#pragma unroll
        for (int a = 0; a < 9; ++a) {
            KG_PUT(a, T);
            if (a < 8) T = cmul(T, G);
        }
        KG_MMA(acc2)
        {
            u32 h01 = pkh(acc1[0] * KSC, acc1[1] * KSC), h23 = pkh(acc1[2] * KSC, acc1[3] * KSC);
            if (q4 < 2) {
                *(u2v*)(kc + (64 * q4 + 4 * l15) * 2) = (u2v){h01, h23};
            } else {
                *(u32*)(kc + (128 + 64 * (q4 - 2) + 2 * l15) * 2) = h01;
                *(u32*)(kc + (128 + 64 * (q4 - 2) + 32 + 2 * l15) * 2) = h23;
            }
            u32 g01 = pkh(acc2[0] * KSC, acc2[1] * KSC), g23 = pkh(acc2[2] * KSC, acc2[3] * KSC);
            if (q4 < 2) {
                kch[256 + 64 * q4 + l15] = (u16)g01;
                kch[256 + 64 * q4 + 16 + l15] = (u16)(g01 >> 16);
                kch[256 + 64 * q4 + 32 + l15] = (u16)g23;
                kch[256 + 64 * q4 + 48 + l15] = (u16)(g23 >> 16);
            } else if (lane == 32) {
                kch[384] = (u16)g01;
            }
        }
        if (lane < 32) {
            u2v s = *(const u2v*)(kc + lane * 8);
            u32 s4 = kch[lane * 4 + 4];
            u4v b2;
            b2[0] = (s[0] & 0xffffu) | (s[0] << 16);
            b2[1] = (s[0] >> 16) | (s[1] << 16);
            b2[2] = (s[1] & 0xffffu) | (s[1] << 16);
            b2[3] = (s[1] >> 16) | (s4 << 16);
            st16(kx + 512 + lane * 16, b2);
        }
        {
            u32 s = *(const u32*)(kc + 256 + lane * 4);
            u32 c = kch[128 + lane * 2 + 2];
            u4v v;
            v[0] = (s & 0xffffu) | (s << 16);
            v[1] = s;
            v[2] = (s >> 16) | (s & 0xffff0000u);
            v[3] = c | (c << 16);
            st16(kx + 1024 + lane * 16, v);
        }
#pragma unroll
        for (int it = 0; it < 2; ++it) {
            int oc = lane + 64 * it;
            u32 a = kch[256 + oc], c = kch[257 + oc];
            u4v v;
            v[0] = a | (a << 16);
            v[1] = v[0];
            v[2] = ((oc & 1) ? c : a) | (c << 16);
            v[3] = c | (c << 16);
            st16(kx + 2048 + oc * 16, v);
        }
    }
}

__device__ __forceinline__ NOPK void w2pack_role(const Params& p, int unit, char* sw) {
    const int lane = threadIdx.x & 63, l15 = lane & 15, q4 = lane >> 4;
    const int nb = unit / KS2, ks = unit - nb * KS2;
#pragma unroll
    for (int i = 0; i < 5; ++i) {
        int c = lane + 64 * i, row = c / 10, ch = c - row * 10;
        *(u4v*)(sw + c * 16) = *(const u4v*)(p.w2 + (size_t)(ks * 32 + row) * D + nb * 80 + ch * 8);
    }
    const int q = l15 >> 2, pp = lane & 3;
    char* dst = p.ws + WS_W2P + (size_t)unit * 5120 + lane * 16;
    const char* src = sw + (16 * (q4 >> 1) + 4 * (q4 & 1) + q) * 160 + pp * 8;
#pragma unroll
    for (int dt = 0; dt < 5; ++dt) {
        u2v lo = tr_read(src + dt * 32);
        u2v hi = tr_read(src + 8 * 160 + dt * 32);
        st16(dst + dt * 1024, (u4v){lo[0], lo[1], hi[0], hi[1]});
    }
}

#define CODE_WARM(entry, piece, ldsa, lo16_) GLDS16(ldsa, lo16_, (const char*)(entry) + (piece)*1024)

__global__ __launch_bounds__(512, 4) NOPK void k1(Params p) {
    extern __shared__ __attribute__((aligned(16))) char smem[];
    const int b = blockIdx.x;
    const int w = __builtin_amdgcn_readfirstlane(threadIdx.x >> 6);
    TLB()
    if (b < 224) {
        gemm1_role(p, b, smem);
    } else if (b < 438) {
        const int d = (b - 224) * 3 + (w >> 1);
        if (w < 6 && d < D) {
            kgen_role(p, d, w & 1, smem + w * 10240);
        } else {
            if (w >= 6) {
                const int unit = (b - 224) * 2 + (w - 6);
                w2pack_role(p, unit, smem + w * 10240);
                if (unit < 4) w2pack_role(p, 428 + unit, smem + w * 10240);
                if (b == 437 && (threadIdx.x & 63) < 8 && w == 7) *(u32*)(p.ws + WS_CNT + (threadIdx.x & 63) * 256) = 0u;
            }
            __builtin_amdgcn_s_barrier();
            __builtin_amdgcn_s_barrier();
        }
    } else {
        ln_role(p, b - 438, smem);
    }
    TLE(0)
}

#define PF0 16
#define PFD 8
static constexpr int CV_WAVE = 9216;
static constexpr int K2_RED = 3 * CV_WAVE;
static constexpr int K2_YS = K2_RED + 61440;
static constexpr int K2_LDS = K2_YS + 10240;

#define G2_BUF(C, i) C "v"(af[i][0]), C "v"(af[i][1]), C "v"(af[i][2]), C "v"(af[i][3]), C "a"(wf[i][0]), C "a"(wf[i][1]), C "a"(wf[i][2]), C "a"(wf[i][3]), C "a"(wf[i][4])
#define G2_LOAD(i)                                                                                                   \
    asm volatile("buffer_load_dwordx4 %0, %9, %15, %17 offen\n\tbuffer_load_dwordx4 %4, %13, %16, %18 offen\n\t"     \
                 "buffer_load_dwordx4 %1, %10, %15, %17 offen\n\tbuffer_load_dwordx4 %2, %11, %15, %17 offen\n\t"    \
                 "buffer_load_dwordx4 %3, %12, %15, %17 offen\n\tbuffer_load_dwordx4 %5, %13, %16, %18 offen offset:1024\n\t" \
                 "buffer_load_dwordx4 %6, %13, %16, %18 offen offset:2048\n\tbuffer_load_dwordx4 %7, %13, %16, %18 offen offset:3072\n\t" \
                 "buffer_load_dwordx4 %8, %14, %16, %18 offen"                                                       \
                 : G2_BUF("=&", i) : "v"(ao[0]), "v"(ao[1]), "v"(ao[2]), "v"(ao[3]), "v"(ao[4]), "v"(ao[5]), "s"(ra), "s"(rw), \
                   "s"((i)*4096), "s"((i)*5120) : "memory");
#define G2_MF(d, w_, a_) "v_mfma_f32_16x16x32_bf16 %" #d ", %" #w_ ", %" #a_ ", %" #d "\n\t"
#define G2_M4(dt, i, VO, OFF)                                                                           \
    asm volatile(G2_MF(0, 4, 5) G2_MF(1, 4, 6) G2_MF(2, 4, 7) G2_MF(3, 4, 8)                            \
                 "buffer_load_dwordx4 %4, %9, %10, %11 offen offset:" #OFF                              \
                 : "+a"(acc2[dt][0]), "+a"(acc2[dt][1]), "+a"(acc2[dt][2]), "+a"(acc2[dt][3]), "+a"(wf[i][dt]) \
                 : "v"(af[i][0]), "v"(af[i][1]), "v"(af[i][2]), "v"(af[i][3]), "v"(ao[VO]), "s"(rw), "s"(sw) : "memory");
#define G2_BODY(i)                                                                                      \
    {                                                                                                   \
        G2_M4(0, i, 4, 0) G2_M4(1, i, 4, 1024) G2_M4(2, i, 4, 2048) G2_M4(3, i, 4, 3072) G2_M4(4, i, 5, 0) \
        asm volatile("buffer_load_dwordx4 %0, %4, %8, %9 offen\n\tbuffer_load_dwordx4 %1, %5, %8, %9 offen\n\t" \
                     "buffer_load_dwordx4 %2, %6, %8, %9 offen\n\tbuffer_load_dwordx4 %3, %7, %8, %9 offen" \
                     : "+v"(af[i][0]), "+v"(af[i][1]), "+v"(af[i][2]), "+v"(af[i][3])                   \
                     : "v"(ao[0]), "v"(ao[1]), "v"(ao[2]), "v"(ao[3]), "s"(ra), "s"(sa) : "memory");    \
    }
#define G2_STEP(i) { asm volatile("s_waitcnt vmcnt(45)" : G2_BUF("+", i)::"memory"); G2_BODY(i) sa += 4096; sw += 5120; }
#define G2_M4N(dt, i)                                                                                   \
    asm volatile(G2_MF(0, 4, 5) G2_MF(1, 4, 6) G2_MF(2, 4, 7) G2_MF(3, 4, 8) "s_nop 0"                  \
                 : "+a"(acc2[dt][0]), "+a"(acc2[dt][1]), "+a"(acc2[dt][2]), "+a"(acc2[dt][3])           \
                 : "a"(wf[i][dt]), "v"(af[i][0]), "v"(af[i][1]), "v"(af[i][2]), "v"(af[i][3]));
#define G2_SSTEP(k, NW)                                                                                 \
    {                                                                                                   \
        asm volatile("s_waitcnt vmcnt(" #NW ")" : G2_BUF("+", (k) % 6)::"memory");                      \
        if constexpr ((k) < 2) {                                                                        \
            const u32 sa = ((k) + 6) * 4096, sw = ((k) + 6) * 5120;                                     \
            G2_BODY((k) % 6)                                                                            \
        } else {                                                                                        \
            G2_M4N(0, (k) % 6) G2_M4N(1, (k) % 6) G2_M4N(2, (k) % 6) G2_M4N(3, (k) % 6) G2_M4N(4, (k) % 6) \
        }                                                                                               \
        __builtin_amdgcn_sched_barrier(0);                                                              \
    }
#define G2_ACC(a0) "+a"(acc2[a0][0]), "+a"(acc2[a0][1]), "+a"(acc2[a0][2]), "+a"(acc2[a0][3])
#define G2_ZERO()                                                                                       \
    _Pragma("unroll") for (int dt = 0; dt < 5; ++dt) _Pragma("unroll") for (int tt = 0; tt < 4; ++tt) acc2[dt][tt] = (f4v){0.f, 0.f, 0.f, 0.f}; \
    asm volatile("s_nop 1" : G2_ACC(0), G2_ACC(1), G2_ACC(2), G2_ACC(3), G2_ACC(4));
#undef STOFF
#define STOFF (4 << 20)
__device__ __forceinline__ NOPK void conv_wave(const Params& p, int d, u32 lb, char* img, u4v (&af)[6][4], u4v (&wf)[6][5],
                                                f4v (&acc2)[5][4], u4v ra, u4v rw, const u32 (&ao)[6], u32 svc, const u32* cnt,
                                                const char* yb, u32 ysl) {
    const int lane = threadIdx.x & 63, l15 = lane & 15, q4 = lane >> 4;
    const int q = l15 >> 2, pp = lane & 3;
    const u32 kra = lb + REC_K + (32 * q4 - 4 * q + 4 * pp) * 2;
    u32 xra[2][2][2];
#pragma unroll
    for (int ap = 0; ap < 2; ++ap)
#pragma unroll
        for (int h = 0; h < 2; ++h)
#pragma unroll
            for (int g = 0; g < 2; ++g) {
                int G = l15 + 8 * g - 2 * q4 - h + 8;
                xra[ap][h][g] = lb + 4160 + G * 32 + ((ap ^ ((G >> 3) & 1)) * 16);
            }
    const u32 csrc = (lane >= 16 ? lane - 16 : (lane == 0 ? 63 : lane + 47)) * 4;
    asm volatile("s_waitcnt vmcnt(0)" ::: "memory");
    u4v kf[16], xf[4];
    f4v acc[8];
    float Y[8][7];
    float scale;
    asm volatile("ds_read_b32 %0, %1" : "=v"(scale) : "v"(lb));
#define CV_XRD(s)                                                                                          \
    {                                                                                                      \
        u2v v0, v1;                                                                                        \
        asm volatile("ds_read_b64 %0, %2 offset:%4\n\tds_read_b64 %1, %3 offset:%4" : "=&v"(v0), "=&v"(v1) \
                     : "v"(xra[(s) >> 5][0][(s)&1]), "v"(xra[(s) >> 5][1][(s)&1]), "i"((((s)&15) >> 1) * 512 + (((s) >> 4) & 1) * 8)); \
        xf[(s)&3] = (u4v){v0[0], v0[1], v1[0], v1[1]};                                                     \
    }
    CV_XRD(0) CV_XRD(1) CV_XRD(2)
#pragma unroll
    for (int e = 0; e < 16; ++e) {
        u2v lo, hi;
        asm volatile("ds_read_b64_tr_b16 %0, %2 offset:%3\n\tds_read_b64_tr_b16 %1, %2 offset:%4" : "=&v"(lo), "=&v"(hi)
                     : "v"(kra), "i"(e * 256), "i"(e * 256 + 32));
        kf[e] = (u4v){lo[0], lo[1], hi[0], hi[1]};
    }
    asm volatile("s_waitcnt lgkmcnt(0)" : "+v"(kf[0]), "+v"(kf[1]), "+v"(kf[2]), "+v"(kf[3]), "+v"(kf[4]), "+v"(kf[5]), "+v"(kf[6]), "+v"(kf[7]),
                 "+v"(kf[8]), "+v"(kf[9]), "+v"(kf[10]), "+v"(kf[11]), "+v"(kf[12]), "+v"(kf[13]), "+v"(kf[14]), "+v"(kf[15]), "+v"(scale));
#define CV_STEP(s)                                                                                         \
    {                                                                                                      \
        constexpr int a = (s) >> 4, g = (s)&15;                                                            \
        if constexpr ((s) + 3 < 64) {                                                                      \
            CV_XRD(((s) + 3) & 63)                                                                         \
            asm volatile("s_waitcnt lgkmcnt(6)" : "+v"(xf[(s)&3]));                                        \
        } else if constexpr ((s) == 61) {                                                                  \
            asm volatile("s_waitcnt lgkmcnt(0)" : "+v"(xf[1]), "+v"(xf[2]), "+v"(xf[3]));                  \
        }                                                                                                  \
        _Pragma("unroll") for (int e = (g + 1) & 1; e + g <= 15; e += 2)                                   \
            acc[(e + g) >> 1] = mfma_h(kf[e], xf[(s)&3], g == 0 ? (f4v){0.f, 0.f, 0.f, 0.f} : acc[(e + g) >> 1]); \
        if constexpr (g == 15) {                                                                           \
            _Pragma("unroll") for (int t = 0; t < 8; ++t) _Pragma("unroll") for (int r = 0; r < 4; ++r)    \
                Y[t][r + a] = (a == 0 || r == 3) ? acc[t][r] : Y[t][r + a] + acc[t][r];                    \
        }                                                                                                  \
        if constexpr ((s) >= PF0 && ((s) - PF0) % PFD == 0 && ((s) - PF0) / PFD < 6) G2_LOAD((((s) - PF0) / PFD) % 6) \
        __builtin_amdgcn_sched_barrier(0);                                                                 \
    }
#define CV_S4(s) CV_STEP(s) CV_STEP((s) + 1) CV_STEP((s) + 2) CV_STEP((s) + 3)
#define CV_S16(s) CV_S4(s) CV_S4((s) + 4) CV_S4((s) + 8) CV_S4((s) + 12)
    CV_S16(0) CV_S16(16) CV_S16(32) CV_S16(48)
    float cr[8][3];
#pragma unroll
    for (int t = 0; t < 8; ++t)
#pragma unroll
        for (int r = 0; r < 3; ++r)
            cr[t][r] = __builtin_bit_cast(float, __builtin_amdgcn_ds_bpermute(csrc, __builtin_bit_cast(int, Y[t][4 + r])));
    __builtin_amdgcn_sched_barrier(0);
#pragma unroll
    for (int t = 0; t < 8; ++t) {
        float y[4];
#pragma unroll
        for (int r = 0; r < 3; ++r) y[r] = Y[t][r] + (lane == 0 ? (t ? cr[t - (t > 0)][r] : 0.f) : cr[t][r]);
        y[3] = Y[t][3];
        *(u2v*)(img + t * 512 + (16 * l15 + 4 * q4) * 2) = (u2v){pkbf(y[0] * scale, y[1] * scale), pkbf(y[2] * scale, y[3] * scale)};
    }
    char* dst = p.ws + WS_YT + (size_t)d * 4096 + lane * 16;
    {
        u4v o[4];
#pragma unroll
        for (int i = 0; i < 4; ++i) o[i] = *(const u4v*)(img + i * 1024 + lane * 16);
#pragma unroll
        for (int i = 0; i < 4; ++i)
            asm volatile("global_store_dwordx4 %0, %1, off offset:%2 sc1\n\ts_nop 1" ::"v"(dst), "v"(o[i]), "i"(i * 1024) : "memory");
    }
    G2_ZERO()
    asm volatile("s_waitcnt vmcnt(0)\n\ts_mov_b64 exec, 1\n\tglobal_atomic_add %0, %1, %2\n\ts_mov_b64 exec, -1"
                 ::"v"(0u), "v"(1u), "s"(p.ws + WS_CNT + (d / 80) * 256) : "memory");
    G2_SSTEP(0, 63) G2_SSTEP(1, 63) G2_SSTEP(2, 63) G2_SSTEP(3, 63)
    u32 cv = 0;
    asm volatile("s_cmp_lg_u32 %1, 0\n\ts_cbranch_scc1 .Lsv%=\n\tglobal_load_dword %0, %0, %2 sc1\n.Lsv%=:" : "+v"(cv) : "s"(svc), "s"(cnt) : "memory", "scc");
    G2_SSTEP(4, 63) G2_SSTEP(5, 63)
    asm volatile("s_waitcnt vmcnt(0)" : "+v"(cv), G2_BUF("+", 0), G2_BUF("+", 1)::"memory");
    if (svc == 0) {
        unsigned spins = 0;
        while (__builtin_amdgcn_readfirstlane(cv) < 80u && ++spins < (1u << 22)) {
            __builtin_amdgcn_s_sleep(4);
            cv = __hip_atomic_load(cnt, __ATOMIC_RELAXED, __HIP_MEMORY_SCOPE_AGENT);
        }
        const u32 yo = (lane >> 3) * 4096 + (lane & 7) * 16;
#pragma unroll
        for (int i = 0; i < 10; ++i)
            asm volatile("s_mov_b32 m0, %0\n\ts_nop 0\n\tglobal_load_lds_dwordx4 %1, %2 sc1" ::"s"(ysl + i * 1024), "v"(yo), "s"(yb + i * 32768) : "memory");
    }
    G2_SSTEP(6, 63) G2_SSTEP(7, 63)
    asm volatile("s_waitcnt vmcnt(0)\n\ts_nop 7\n\ts_nop 7" : G2_ACC(0), G2_ACC(1), G2_ACC(2), G2_ACC(3), G2_ACC(4)::"memory");
}

__global__ __launch_bounds__(256, 1) NOPK void k2(Params p) {
    extern __shared__ __attribute__((aligned(16))) char smem[];
    const int b = blockIdx.x;
    const int w = __builtin_amdgcn_readfirstlane(threadIdx.x >> 6);
    TLB()
    const int xcd = b & 7, j = b >> 3;
    const int mb = 8 * (xcd >> 1) + (j & 7), nb = 4 * (xcd & 1) + (j >> 3);
    const int nch = (j & 1) ? 2 : 3;
    const int ch0 = xcd * 80 + (j >> 1) * 5 + ((j & 1) ? 3 : 0);
    const int k0 = ((j & 1) && w == 3) ? 34 : 8 * w;
    const int nk = w < nch ? 8 : ((j & 1) ? 2 * w + 14 : 30);
    const char* ab = p.ws + WS_ACT + ((size_t)mb * KS2 + k0) * 4096;
    const char* wb = p.ws + WS_W2P + ((size_t)nb * KS2 + k0) * 5120;
    u4v ra = {(u32)(size_t)ab, (u32)((size_t)ab >> 32), (u32)nk * 4096u, 0x00020000u};
    u4v rw = {(u32)(size_t)wb, (u32)((size_t)wb >> 32), (u32)nk * 5120u, 0x00020000u};
    const u32 lo16 = (threadIdx.x & 63) * 16;
    const u32 lb = lds_addr(smem);
    const u32 ao[6] = {lo16 + (w & 3) * 1024, lo16 + ((w + 1) & 3) * 1024, lo16 + ((w + 2) & 3) * 1024, lo16 + ((w + 3) & 3) * 1024, lo16, lo16 + 4096};
    SGDECL SG(0)
    if (w < nch) {
        const char* src = p.ws + WS_REC + (size_t)(ch0 + w) * REC;
#pragma unroll
        for (int i = 0; i < 9; ++i) GLDS16(lb + w * CV_WAVE + i * 1024, lo16, src + i * 1024);
    }
    u4v af[6][4], wf[6][5];
    SG(1)
    f4v acc2[5][4];
    if (w < nch) {
        conv_wave(p, ch0 + w, lb + w * CV_WAVE, smem + w * CV_WAVE, af, wf, acc2, ra, rw, ao, (u32)w,
                  (const u32*)(p.ws + WS_CNT + nb * 256), p.ws + WS_YT + (size_t)(nb * 80) * 4096 + (size_t)mb * 128, lb + K2_YS);
    } else {
        G2_LOAD(0) G2_LOAD(1) G2_LOAD(2) G2_LOAD(3) G2_LOAD(4) G2_LOAD(5)
        G2_ZERO()
        u32 sa = 6 * 4096, sw = 6 * 5120;
#pragma nounroll
        for (int r = 0; r < nk; r += 6) { G2_STEP(0) G2_STEP(1) G2_STEP(2) G2_STEP(3) G2_STEP(4) G2_STEP(5) }
        asm volatile("s_waitcnt vmcnt(0)" : G2_BUF("+", 0), G2_BUF("+", 1), G2_BUF("+", 2)::"memory");
        asm volatile("" : G2_BUF("+", 3), G2_BUF("+", 4), G2_BUF("+", 5)::"memory");
        asm volatile("s_nop 7\n\ts_nop 7" : G2_ACC(0), G2_ACC(1), G2_ACC(2), G2_ACC(3), G2_ACC(4)::"memory");
    }
    SG(2)
    SG(3)
    int lane = threadIdx.x & 63;
    asm volatile("" : "+v"(lane));
    const int l15 = lane & 15, q4 = lane >> 4;
    const int t = mb * 64 + w * 16 + l15;
    const int dbase = nb * 80 + 4 * q4;
    u2v xv[5];
#pragma unroll
    for (int dt = 0; dt < 5; ++dt) xv[dt] = *(const u2v*)(p.x + (size_t)t * D + dbase + dt * 16);
    char* red = smem + K2_RED;
#pragma unroll
    for (int tt = 1; tt < 4; ++tt) {
        char* dst = red + (((tt + w) & 3) * 3 + tt - 1) * 5120 + lane * 16;
#pragma unroll
        for (int dt = 0; dt < 5; ++dt) *(f4v*)(dst + dt * 1024) = acc2[dt][tt];
    }
    RAWBAR()
    SG(4)
    const char* ys = smem + K2_YS + (4 * q4 + (l15 >> 2)) * 128 + w * 32 + (lane & 3) * 8;
    const char* rsrc = red + w * 15360 + lane * 16;
#pragma unroll
    for (int dt = 0; dt < 5; ++dt) {
        const u2v sv = tr_read(ys + dt * 2048);
        const f4v g = (acc2[dt][0] + *(const f4v*)(rsrc + dt * 1024)) + (*(const f4v*)(rsrc + 5120 + dt * 1024) + *(const f4v*)(rsrc + 10240 + dt * 1024));
        u2v o;
#pragma unroll
        for (int r2 = 0; r2 < 2; ++r2) {
            const float p0 = g[2 * r2], p1 = g[2 * r2 + 1];
            const float g0 = p0 * __builtin_amdgcn_rcpf(1.0f + __builtin_amdgcn_exp2f(-1.44269504f * p0));
            const float g1 = p1 * __builtin_amdgcn_rcpf(1.0f + __builtin_amdgcn_exp2f(-1.44269504f * p1));
            o[r2] = pkbf(fmaf(bflo(sv[r2]), g0, bflo(xv[dt][r2])), fmaf(bfhi(sv[r2]), g1, bfhi(xv[dt][r2])));
        }
        *(u2v*)(p.out + (size_t)t * D + dbase + dt * 16) = o;
    }
    SG(5) SGDUMP(6)
    TLE(1 << 20)
}

extern "C" void kernel_launch(void* const* d_in, const int* in_sizes, int n_in, void* d_out, int out_size, void* d_ws,
                              size_t ws_size, hipStream_t stream) {
    Params p;
    p.x = (const u16*)d_in[0];
    p.Are = (const u16*)d_in[1]; p.Aim = (const u16*)d_in[2]; p.Bre = (const u16*)d_in[3]; p.Bim = (const u16*)d_in[4];
    p.Cre = (const u16*)d_in[5]; p.Cim = (const u16*)d_in[6]; p.logdt = (const u16*)d_in[7]; p.Dskip = (const u16*)d_in[8];
    p.w1 = (const u16*)d_in[10]; p.b1 = (const u16*)d_in[11]; p.w2 = (const u16*)d_in[12];
    p.out = (u16*)d_out;
    p.ws = (char*)d_ws;
    (void)hipFuncSetAttribute((const void*)k1, hipFuncAttributeMaxDynamicSharedMemorySize, LDS_BYTES);
    (void)hipFuncSetAttribute((const void*)k2, hipFuncAttributeMaxDynamicSharedMemorySize, K2_LDS);
    k1<<<502, 512, LDS_BYTES, stream>>>(p);
    k2<<<256, 256, K2_LDS, stream>>>(p);
}
```

```cpp
#include <hip/hip_runtime.h>
#include <stdint.h>

#if defined(__HIP_DEVICE_COMPILE__) && !defined(ALLOWPK)
#define NOPK __attribute__((target("no-packed-fp32-ops")))
#else
#define NOPK
#endif
typedef unsigned short u16;
typedef unsigned int u32;
typedef short s4v __attribute__((ext_vector_type(4)));
typedef u32 u2v __attribute__((ext_vector_type(2)));
typedef u32 u4v __attribute__((ext_vector_type(4)));
typedef float f2v __attribute__((ext_vector_type(2)));
typedef float f4v __attribute__((ext_vector_type(4)));
typedef _Float16 h2v __attribute__((ext_vector_type(2)));
typedef _Float16 h8v __attribute__((ext_vector_type(8)));
typedef __bf16 b2v __attribute__((ext_vector_type(2)));
typedef __bf16 b8v __attribute__((ext_vector_type(8)));
#define LDSP(T, p) ((__attribute__((address_space(3))) T*)(p))
#define GLB(T, p) ((__attribute__((address_space(1))) T*)(p))

static constexpr int L = 2048, D = 640, NS = 128, F = 1728;
static constexpr int KS2 = F / 32;

static constexpr size_t WS_ACT = 0;
static constexpr size_t WS_W2P = WS_ACT + (size_t)L * F * 2;
static constexpr int REC_K = 32, REC_X = 4160 + 512, REC = 8768;
static constexpr size_t WS_REC = WS_W2P + (size_t)F * D * 2;
static constexpr size_t WS_YT = WS_REC + (size_t)641 * REC + 192;
static constexpr size_t WS_CNT = WS_YT + (size_t)L * D * 2;
static_assert(WS_REC % 256 == 0 && WS_YT % 256 == 0, "alignment");

static constexpr int LDS_BYTES = 80 * 1024;

__device__ __forceinline__ NOPK u32 f2u(float v) { return __builtin_bit_cast(u32, v); }
__device__ __forceinline__ NOPK float bf2f(u32 v) { return __builtin_bit_cast(float, (u32)(v << 16)); }
__device__ __forceinline__ NOPK float bflo(u32 v) { return __builtin_bit_cast(float, (u32)(v << 16)); }
__device__ __forceinline__ NOPK float bfhi(u32 v) { return __builtin_bit_cast(float, (u32)(v & 0xffff0000u)); }
__device__ __forceinline__ NOPK u32 pkbf(float a, float b) {
    f2v v = {a, b};
    return __builtin_bit_cast(u32, __builtin_convertvector(v, b2v));
}
__device__ __forceinline__ NOPK u32 pkh(float a, float b) {
    f2v v = {a, b};
    return __builtin_bit_cast(u32, __builtin_convertvector(v, h2v));
}
__device__ __forceinline__ NOPK float rbf(float a) { return bflo(pkbf(a, a)); }
__device__ __forceinline__ NOPK f4v mfma_bf(u4v a, u4v b, f4v c) {
    return __builtin_amdgcn_mfma_f32_16x16x32_bf16(__builtin_bit_cast(b8v, a), __builtin_bit_cast(b8v, b), c, 0, 0, 0);
}
__device__ __forceinline__ NOPK f4v mfma_h(u4v a, u4v b, f4v c) {
    return __builtin_amdgcn_mfma_f32_16x16x32_f16(__builtin_bit_cast(h8v, a), __builtin_bit_cast(h8v, b), c, 0, 0, 0);
}
__device__ __forceinline__ NOPK u2v tr_read(const char* p) {
    return __builtin_bit_cast(u2v, __builtin_amdgcn_ds_read_tr16_b64_v4i16(LDSP(s4v, p)));
}
__device__ __forceinline__ NOPK float dot2bf(u32 a, u32 b, float c) {
    return __builtin_amdgcn_fdot2_f32_bf16(__builtin_bit_cast(b2v, a), __builtin_bit_cast(b2v, b), c, false);
}
__device__ __forceinline__ NOPK void glds16(const void* g, void* l) {
    __builtin_amdgcn_global_load_lds((const __attribute__((address_space(1))) void*)g,
                                     (__attribute__((address_space(3))) void*)l, 16, 0, 0);
}
__device__ __forceinline__ NOPK u32 lds_addr(const void* l) { return (u32)(size_t)(__attribute__((address_space(3))) const char*)l; }
#define GLDS16(ldsa, voff, sbase) \
    asm volatile("s_mov_b32 m0, %0\n\ts_nop 0\n\tglobal_load_lds_dwordx4 %1, %2" ::"s"(ldsa), "v"(voff), "s"(sbase) : "memory")
#define GLDS4(ldsa, voff, sbase) \
    asm volatile("s_mov_b32 m0, %0\n\ts_nop 0\n\tglobal_load_lds_dword %1, %2" ::"s"(ldsa), "v"(voff), "s"(sbase) : "memory")
template <int CTRL> __device__ __forceinline__ NOPK float dpp(float v) {
    return __builtin_bit_cast(float, __builtin_amdgcn_update_dpp(0, __builtin_bit_cast(int, v), CTRL, 0xf, 0xf, true));
}
__device__ __forceinline__ NOPK float swap32(float v) {
    const u32 b = __builtin_bit_cast(u32, v);
    const auto r = __builtin_amdgcn_permlane32_swap(b, b, false, false);
    return __builtin_bit_cast(float, (threadIdx.x & 32) ? r[0] : r[1]);
}
__device__ __forceinline__ NOPK float red16(float v) {
    v += dpp<0xB1>(v); v += dpp<0x4E>(v); v += dpp<0x141>(v); v += dpp<0x140>(v);
    return v;
}
__device__ __forceinline__ NOPK float max64(float v) {
    v = fmaxf(v, dpp<0xB1>(v)); v = fmaxf(v, dpp<0x4E>(v)); v = fmaxf(v, dpp<0x141>(v)); v = fmaxf(v, dpp<0x140>(v));
    const int b = __builtin_bit_cast(int, v);
    float a0 = __builtin_bit_cast(float, __builtin_amdgcn_readlane(b, 0)), a1 = __builtin_bit_cast(float, __builtin_amdgcn_readlane(b, 16));
    float a2 = __builtin_bit_cast(float, __builtin_amdgcn_readlane(b, 32)), a3 = __builtin_bit_cast(float, __builtin_amdgcn_readlane(b, 48));
    return fmaxf(fmaxf(a0, a1), fmaxf(a2, a3));
}
__device__ __forceinline__ NOPK void st16(void* ptr, u4v v) {
    asm volatile("global_store_dwordx4 %0, %1, off sc1\n\ts_nop 1" ::"v"(ptr), "v"(v) : "memory");
}
__device__ __forceinline__ NOPK void ln_stats(float s1, float s2, float& mb, float& den) {
    float mean = s1 * (1.0f / D);
    float var = fmaxf(s2 * (1.0f / D) - mean * mean, 0.f);
    mb = rbf(mean);
    den = rbf(__builtin_amdgcn_sqrtf(rbf(rbf(var) + 1e-5f)));
}
__device__ __forceinline__ NOPK float ln_apply(float x, float mb, float rden) { return rbf(rbf(x - mb) * rden); }

#define SG(i) {}
#define SGDECL
#define SGDUMP(n)
#define TLB()
#define TLE(off)
struct Params {
    const u16 *x, *Are, *Aim, *Bre, *Bim, *Cre, *Cim, *logdt, *Dskip, *w1, *b1, *w2;
    u16* out;
    char* ws;
};

typedef float f16v __attribute__((ext_vector_type(16)));
__device__ __forceinline__ NOPK f16v mfma32_bf(u4v a, u4v b, f16v c) {
    return __builtin_amdgcn_mfma_f32_32x32x16_bf16(__builtin_bit_cast(b8v, a), __builtin_bit_cast(b8v, b), c, 0, 0, 0);
}
#define STATV 0
#define VMWAIT(N) asm volatile("s_waitcnt vmcnt(" #N ") lgkmcnt(0)" ::: "memory")

#define RAWBAR() { asm volatile("s_waitcnt lgkmcnt(0)" ::: "memory"); __builtin_amdgcn_s_barrier(); asm volatile("" ::: "memory"); }

__device__ __forceinline__ NOPK void gemm1_role(const Params& p, int b, char* smem) {
    const int tid = threadIdx.x, lane = tid & 63;
    const int w = __builtin_amdgcn_readfirstlane(tid >> 6);
    const int wt = w >> 2, wf = w & 3;
    const int xcd = b & 7, j = b >> 3;
    const int mb = 4 * (xcd >> 1) + (j & 3), nt = 7 * (xcd & 1) + (j >> 2);
    const int rot = 0;
    const int l31 = lane & 31, hh = lane >> 5;
    const bool half = (nt == 13);
    SGDECL
#define STOFF 0
    SG(0)
    u32 tj1 = 0;
    u2v bia[4];
    {
        const u32 bo = (u32)min(nt * 128 + 32 * wf + 4 * hh, F - 28) * 2u;
        asm volatile("global_load_dwordx2 %0, %4, %5\n\tglobal_load_dwordx2 %1, %4, %5 offset:16\n\t"
                     "global_load_dwordx2 %2, %4, %5 offset:32\n\tglobal_load_dwordx2 %3, %4, %5 offset:48"
                     : "=&v"(bia[0]), "=&v"(bia[1]), "=&v"(bia[2]), "=&v"(bia[3]) : "v"(bo), "s"(p.b1) : "memory");
    }
    {
        const u32 id = min((u32)((j * 8 + w) * 64 + lane), 14079u);
        const u32 wi = id - 5120u, wr = (wi * 4682u) >> 16, wc = wi - wr * 14u;
        const char* tp = id < 5120u ? (const char*)p.x + (size_t)(xcd >> 1) * (512 * D * 2) + id * 128u
                                    : (const char*)p.w1 + wr * (F * 2) + min((xcd & 1) * 1792u + wc * 128u, (u32)(F * 2 - 128));
        u32 junk;
        asm volatile("global_load_dword %0, %1, off" : "=v"(junk) : "v"(tp) : "memory");
        tj1 = junk;
    }
    u32 xo[2], wo;
#pragma unroll
    for (int i = 0; i < 2; ++i) {
        int P = (2 * w + i) * 64 + lane, row = P >> 3, ch = (P & 7) ^ ((row >> 1) & 7);
        xo[i] = (u32)(row * (D * 2) + ch * 16);
    }
    {
        int P = w * 64 + lane, k = P >> 4, ch = (P & 15) ^ ((k & 3) << 2);
        if (half) ch &= 7;
        wo = (u32)(k * (F * 2) + ch * 16);
    }
    const char* xb = (const char*)p.x + (size_t)mb * 128 * (D * 2);
    const char* wb = (const char*)p.w1 + nt * 256;
    const u32 lx = lds_addr(smem) + w * 2048, lw = lds_addr(smem) + 49152 + w * 1024;
#define SROT(st) (((st) + rot >= 10) ? (st) + rot - 10 : (st) + rot)
#define G1_X(st) { const char* s_ = xb + SROT(st) * 128; GLDS16(lx + ((st) % 3) * 16384, xo[0], s_); GLDS16(lx + ((st) % 3) * 16384 + 1024, xo[1], s_); }
#define G1_W(h) { const char* s_ = wb + (size_t)(SROT((h) >> 1) * 64 + ((h)&1) * 32) * (F * 2); GLDS16(lw + ((h)&3) * 8192, wo, s_); }
    G1_X(0) G1_W(0) G1_W(1) G1_X(1) G1_W(2) G1_W(3) G1_X(2)

    const int q = (lane & 15) >> 2, pp = lane & 3, blk = (lane >> 4) & 1;
    const int ro = (wf & 1) ^ wt, ri = wf >> 1;
    const char* xa[4][2];
    const char* wa[2];
#pragma unroll
    for (int u = 0; u < 4; ++u)
#pragma unroll
        for (int i = 0; i < 2; ++i)
            xa[u][i] = smem + (64 * wt + 32 * (i ^ ri) + l31) * 128 + (((2 * (u ^ ro) + hh) ^ ((l31 >> 1) & 7)) << 4);
#pragma unroll
    for (int o = 0; o < 2; ++o)
        wa[o] = smem + 49152 + (16 * (o ^ ro) + 8 * hh + q) * 256 + (((4 * wf + 2 * blk + (pp >> 1)) ^ (q << 2)) << 4) + 8 * (pp & 1);

    f16v acc[2];
#pragma unroll
    for (int i = 0; i < 16; ++i) acc[0][i] = acc[1][i] = 0.f;
    u4v xf[2][2][2], wfr[2][2];
    float s1a = 0.f, s1b = 0.f, s2a = 0.f, s2b = 0.f, csa = 0.f, csb = 0.f;
    SG(1)
#define G1_READ(h, o)                                                                                    \
    {                                                                                                    \
        u2v lo = tr_read(wa[o] + ((h)&3) * 8192), hi = tr_read(wa[o] + ((h)&3) * 8192 + 1024);           \
        wfr[(h)&1][o] = (u4v){lo[0], lo[1], hi[0], hi[1]};                                               \
        xf[(h)&1][o][0] = *(const u4v*)(xa[((h)&1) * 2 + o][0] + (((h) >> 1) % 3) * 16384);              \
        xf[(h)&1][o][1] = *(const u4v*)(xa[((h)&1) * 2 + o][1] + (((h) >> 1) % 3) * 16384);              \
    }
#define G1_M(h, o, i)                                                                                    \
    if ((h) >= 0) {                                                                                      \
        acc[i] = mfma32_bf(wfr[(h)&1][o], xf[(h)&1][o][i], acc[i]);                                      \
        asm volatile("" : "+v"(acc[i]));                                                                 \
    }                                                                                                    \
    __builtin_amdgcn_sched_barrier(0);
#define G1_STAT(h, e)                                                                                    \
    if ((h) >= 0) {                                                                                      \
        const u32 xv_ = xf[(h)&1][0][0][e];                                                              \
        s1a = dot2bf(xv_, 0x3f803f80u, s1a); s2a = dot2bf(xv_, xv_, s2a);                                \
        csa = dot2bf(wfr[(h)&1][0][e], 0x3f803f80u, csa);                                                \
    }
#define G1_PIN() asm volatile("" : "+v"(s1a), "+v"(s1b), "+v"(s2a), "+v"(s2b), "+v"(csa), "+v"(csb)); __builtin_amdgcn_sched_barrier(0);
#define G1_IT(h, NW)                                                                                     \
    {                                                                                                    \
        VMWAIT(NW);                                                                                      \
        __builtin_amdgcn_s_barrier();                                                                    \
        G1_M(h, 0, 0)                                                                                    \
        G1_READ((h) + 1, 0) G1_STAT(h, 0) G1_PIN()                                                       \
        G1_M(h, 0, 1)                                                                                    \
        G1_READ((h) + 1, 1) G1_STAT(h, 1) G1_PIN()                                                       \
        G1_M(h, 1, 0)                                                                                    \
        if ((h) >= 0 && (h) + 4 <= 19) G1_W((h) + 4)                                                     \
        G1_STAT(h, 2) G1_PIN()                                                                           \
        G1_M(h, 1, 1)                                                                                    \
        if ((h) >= 1 && ((h)&1) && ((h) - 1) / 2 + 3 <= 9) G1_X(((h) - 1) / 2 + 3)                       \
        G1_STAT(h, 3) G1_PIN()                                                                           \
    }
    G1_IT(-1, 7) G1_IT(0, 6) G1_IT(1, 4) G1_IT(2, 6) G1_IT(3, 4) SG(2) G1_IT(4, 6) G1_IT(5, 4) G1_IT(6, 6) G1_IT(7, 4)
    G1_IT(8, 6) SG(3) G1_IT(9, 4) G1_IT(10, 6) G1_IT(11, 4) G1_IT(12, 6) G1_IT(13, 4) SG(4) G1_IT(14, 6) G1_IT(15, 4)
    G1_IT(16, 4) G1_IT(17, 1) G1_IT(18, 0)
    G1_M(19, 0, 0) G1_M(19, 0, 1) G1_M(19, 1, 0) G1_M(19, 1, 1) G1_STAT(19, 0) G1_STAT(19, 1) G1_STAT(19, 2) G1_STAT(19, 3)
    s1a += s1b; s2a += s2b; csa += csb;
    SG(5)
    asm volatile("" : "+v"(bia[0]), "+v"(bia[1]), "+v"(bia[2]), "+v"(bia[3]), "+v"(tj1));
    char* rsb = smem + 16384;
    char* csl = smem + 16384 + 2048;
    {
        const float a1 = s1a + swap32(s1a), a2 = s2a + swap32(s2a), ac = csa + swap32(csa);
        if (hh == 0) *(f2v*)(rsb + ((64 * wt + 32 * ri + l31) * 2 + (wf & 1)) * 8) = (f2v){a1, a2};
        else *(float*)(csl + ((32 * wf + l31) * 2 + wt) * 4) = ac;
    }
    RAWBAR()
    SG(6)
    if (!(half && wf >= 2)) {
        float csv[16], bsv[16];
#pragma unroll
        for (int g = 0; g < 4; ++g) {
            const f4v c0 = *(const f4v*)(csl + (32 * wf + 8 * g + 4 * hh) * 8), c1 = *(const f4v*)(csl + (32 * wf + 8 * g + 4 * hh) * 8 + 16);
            csv[4 * g] = c0[0] + c0[1]; csv[4 * g + 1] = c0[2] + c0[3]; csv[4 * g + 2] = c1[0] + c1[1]; csv[4 * g + 3] = c1[2] + c1[3];
            bsv[4 * g] = bflo(bia[g][0]) * 1.44269504f; bsv[4 * g + 1] = bfhi(bia[g][0]) * 1.44269504f;
            bsv[4 * g + 2] = bflo(bia[g][1]) * 1.44269504f; bsv[4 * g + 3] = bfhi(bia[g][1]) * 1.44269504f;
        }
        char* actb = p.ws + WS_ACT + ((size_t)(mb * 2 + wt) * KS2 + nt * 4 + wf) * 4096 + (size_t)((l31 >> 4) * 1024 + ((l31 & 15) + 16 * hh) * 16);
#pragma unroll
        for (int i = 0; i < 2; ++i) {
            const f4v u0 = *(const f4v*)(rsb + (64 * wt + 32 * (i ^ ri) + l31) * 16);
            float mean, den;
            ln_stats(u0[0] + u0[2], u0[1] + u0[3], mean, den);
            const float rdl = __builtin_amdgcn_rcpf(den) * 1.44269504f;
#pragma unroll
            for (int c = 0; c < 2; ++c) {
                u4v o;
#pragma unroll
                for (int e = 0; e < 4; ++e) {
                    float r[2];
#pragma unroll
                    for (int z = 0; z < 2; ++z) {
                        const int ix = 8 * c + 2 * e + z;
                        const float u = fmaf(fmaf(-mean, csv[ix], acc[i][ix]), rdl, bsv[ix]);
                        const float e1 = __builtin_amdgcn_exp2f(u) + 1.0f;
                        r[z] = u * fmaf(__builtin_amdgcn_rcpf(fmaf(e1, e1, 1.0f)), -1.38629436f, 0.69314718f);
                    }
                    o[e] = pkbf(r[0], r[1]);
                }
                st16(actb + (i ^ ri) * 2048 + c * 512, o);
            }
        }
    }
    SG(7)
    SGDUMP(8)
}

__device__ __forceinline__ NOPK void ln_role(const Params& p, int rb, char* smem) {
    const int tid = threadIdx.x, lane = tid & 63;
    const int w = __builtin_amdgcn_readfirstlane(tid >> 6);
    const int rl = 4 * w + (lane >> 4), t = rb * 32 + rl, c15 = lane & 15;
    u4v v[5];
#pragma unroll
    for (int i = 0; i < 5; ++i) v[i] = *(const u4v*)(p.x + (size_t)t * D + (c15 + 16 * i) * 8);
    float s = 0.f;
#pragma unroll
    for (int i = 0; i < 5; ++i)
#pragma unroll
        for (int e = 0; e < 4; ++e) s += bflo(v[i][e]) + bfhi(v[i][e]);
    const float mean = red16(s) * (1.0f / D);
    float qv = 0.f;
#pragma unroll
    for (int i = 0; i < 5; ++i)
#pragma unroll
        for (int e = 0; e < 4; ++e) {
            float a = bflo(v[i][e]) - mean, c = bfhi(v[i][e]) - mean;
            qv = fmaf(a, a, qv); qv = fmaf(c, c, qv);
        }
    const float var = red16(qv) * (1.0f / D);
    const float mbf = rbf(mean);
    const float rd = 1.0f / rbf(__builtin_sqrtf(rbf(rbf(var) + 1e-5f)));
#pragma unroll
    for (int i = 0; i < 5; ++i) {
        u2v o0, o1;
        o0[0] = pkh(ln_apply(bflo(v[i][0]), mbf, rd), ln_apply(bfhi(v[i][0]), mbf, rd));
        o0[1] = pkh(ln_apply(bflo(v[i][1]), mbf, rd), ln_apply(bfhi(v[i][1]), mbf, rd));
        o1[0] = pkh(ln_apply(bflo(v[i][2]), mbf, rd), ln_apply(bfhi(v[i][2]), mbf, rd));
        o1[1] = pkh(ln_apply(bflo(v[i][3]), mbf, rd), ln_apply(bfhi(v[i][3]), mbf, rd));
        *(u2v*)(smem + rl * 1288 + (c15 + 16 * i) * 16) = o0;
        *(u2v*)(smem + rl * 1288 + (c15 + 16 * i) * 16 + 8) = o1;
    }
    RAWBAR()
    const int G = lane >> 4, q = c15 >> 2, pp = lane & 3;
    const char* src = smem + (16 * (G >> 1) + 4 * q + 2 * (G & 1)) * 1288 + pp * 8;
    char* dst = p.ws + WS_REC + (size_t)c15 * REC + REC_X + (size_t)rb * 64 + (G ^ ((rb >> 2) & 1)) * 16;
#pragma unroll
    for (int it = 0; it < 5; ++it) {
        const int cg = w * 5 + it;
        u2v lo = tr_read(src + cg * 32), hi = tr_read(src + cg * 32 + 1288);
        st16(dst + (size_t)cg * 16 * REC, (u4v){lo[0], lo[1], hi[0], hi[1]});
    }
}

struct cplx2 { f2v r, i; };
__device__ __forceinline__ NOPK cplx2 cmul(cplx2 a, cplx2 b) {
    cplx2 o;
    o.r = a.r * b.r - a.i * b.i;
    o.i = a.r * b.i + a.i * b.r;
    return o;
}
struct cplxh { h2v r, i; };
__device__ __forceinline__ NOPK cplxh toh(cplx2 a) { return {__builtin_convertvector(a.r, h2v), __builtin_convertvector(a.i, h2v)}; }
__device__ __forceinline__ NOPK cplx2 cexp2(f2v zr, f2v zi, float c) {
    cplx2 o;
#pragma unroll
    for (int m = 0; m < 2; ++m) {
        float mag = __builtin_amdgcn_exp2f(zr[m] * c * 1.44269504089f);
        float ph = zi[m] * c;
        const float CH = 0.15915494f, CL = 6.4206382e-9f;
        float hi = ph * CH;
        float lo = fmaf(ph, CH, -hi) + ph * CL;
        float fr = __builtin_amdgcn_fractf(hi) + lo;
        o.r[m] = mag * __builtin_amdgcn_cosf(fr);
        o.i[m] = mag * __builtin_amdgcn_sinf(fr);
    }
    return o;
}
static constexpr int KG_ROW = 544;

__device__ __forceinline__ NOPK void kgen_role(const Params& p, int d, int part, char* sw  ) {
    const int lane = threadIdx.x & 63, l15 = lane & 15, q4 = lane >> 4;
    const int o = d * NS + 2 * lane;
    u32 are = *(const u32*)(p.Are + o), aim = *(const u32*)(p.Aim + o), bre = *(const u32*)(p.Bre + o);
    u32 bim = *(const u32*)(p.Bim + o), cre = *(const u32*)(p.Cre + o), cim = *(const u32*)(p.Cim + o);
    float dt = rbf(expf(bf2f(p.logdt[d])));
    f2v zr = {dt * bflo(are), dt * bfhi(are)}, zi = {dt * bflo(aim), dt * bfhi(aim)};
    cplx2 dB = {{dt * bflo(bre), dt * bfhi(bre)}, {dt * bflo(bim), dt * bfhi(bim)}};
    cplx2 C = {{bflo(cre), bfhi(cre)}, {bflo(cim), bfhi(cim)}};
    cplx2 W = cmul(C, dB);
    float wm = fmaxf(fmaxf(fabsf(W.r[0]), fabsf(W.r[1])), fmaxf(fabsf(W.i[0]), fabsf(W.i[1])));
    const float dsk = bf2f(p.Dskip[d]);
    wm = fmaxf(max64(wm), fabsf(dsk) * (1.0f / 128.0f));
    int eU = 14 - __builtin_amdgcn_frexp_expf(wm);
    eU = min(max(eU, -100), 100);
    const float sU = __builtin_amdgcn_ldexpf(1.0f, eU);
    W.r *= sU; W.i *= sU;
    char* rec = p.ws + WS_REC + (size_t)d * REC;
    if (part == 0 && lane < 34) {
        u4v z = {0u, 0u, 0u, 0u};
        if (lane == 0) z[0] = f2u(__builtin_amdgcn_ldexpf(1.0f, 8 - eU));
        *(u4v*)(rec + (lane < 2 ? lane * 16 : 4160 + (lane - 2) * 16)) = z;
    }
    const float KSC = 2.384185791015625e-07f;

    char* r0 = sw;
    char* kc = sw + 16 * KG_ROW;
#define KG_PUT(row, v) *(u2v*)(r0 + (row)*KG_ROW + lane * 8) = (u2v){pkh((v).r[0], (v).r[1]), pkh((v).i[0], (v).i[1])}
#define KG_PUTH(row, T_, E_)                                                                            \
    *(u2v*)(r0 + (row)*KG_ROW + lane * 8) = (u2v){__builtin_bit_cast(u32, __builtin_elementwise_fma(T_.r, E_.r, -(T_.i * E_.i))), \
                                                  __builtin_bit_cast(u32, __builtin_elementwise_fma(T_.r, E_.i, T_.i * E_.r))}
#define KG_MMA(A)                                         \
    A = (f4v){0.f, 0.f, 0.f, 0.f};                        \
    _Pragma("unroll") for (int ks = 0; ks < 8; ++ks) A = mfma_h(*(const u4v*)(r0 + l15 * KG_ROW + ks * 64 + q4 * 16), vf[ks], A);
    const cplx2 G = cexp2(zr, zi, 128.f);
    u16* kch = (u16*)kc;
    char* kx = rec + REC_K;
    u4v vf[8];
    if (part == 0) {
        {
            cplx2 v1 = cexp2(zr, zi, 8.f);
            cplx2 v = {{16384.f, 16384.f}, {0.f, 0.f}};
#pragma unroll
            for (int jj = 0; jj < 16; ++jj) {
                cplx2 t = {v.r, -v.i};
                KG_PUT(jj, t);
                if (jj < 15) v = cmul(v, v1);
            }
        }
        RAWBAR()
#pragma unroll
        for (int ks = 0; ks < 8; ++ks) vf[ks] = *(const u4v*)(r0 + l15 * KG_ROW + ks * 64 + q4 * 16);
        cplx2 E[8];
        E[1] = cexp2(zr, zi, 1.f);
        E[2] = cmul(E[1], E[1]); E[3] = cmul(E[2], E[1]); E[4] = cmul(E[2], E[2]);
        E[5] = cmul(E[4], E[1]); E[6] = cmul(E[4], E[2]); E[7] = cmul(E[4], E[3]);
        const cplxh T0 = toh(W), T1 = toh(cmul(W, G));
        RAWBAR()
        *(u2v*)(r0 + lane * 8) = (u2v){__builtin_bit_cast(u32, T0.r), __builtin_bit_cast(u32, T0.i)};
        *(u2v*)(r0 + 8 * KG_ROW + lane * 8) = (u2v){__builtin_bit_cast(u32, T1.r), __builtin_bit_cast(u32, T1.i)};
#pragma unroll
        for (int c = 1; c < 8; ++c) {
            const cplxh Eh = toh(E[c]);
            KG_PUTH(c, T0, Eh);
            KG_PUTH(8 + c, T1, Eh);
        }
        f4v acc;
        KG_MMA(acc)
        if (lane == 0) acc[0] = fmaf(dsk * sU, 16384.f, acc[0]);
        *(u2v*)(kx + (128 * (q4 >> 1) + 4 * (q4 & 1) + 8 * l15) * 2) = (u2v){pkh(acc[0] * KSC, acc[1] * KSC), pkh(acc[2] * KSC, acc[3] * KSC)};
    } else {
        const cplx2 E2 = cexp2(zr, zi, 2.f);
        const cplx2 E4 = cmul(E2, E2);
        const cplxh E2h = toh(E2), E4h = toh(E4), E6h = toh(cmul(E4, E2));
        cplx2 T = cmul(W, cexp2(zr, zi, 256.f));
#pragma unroll
        for (int a = 0; a < 6; ++a) {
            const cplxh Th = toh(T);
            const int row = a < 2 ? 4 * a : 4 + 2 * a;
            *(u2v*)(r0 + row * KG_ROW + lane * 8) = (u2v){__builtin_bit_cast(u32, Th.r), __builtin_bit_cast(u32, Th.i)};
            if (a < 2) { KG_PUTH(row + 1, Th, E2h); KG_PUTH(row + 2, Th, E4h); KG_PUTH(row + 3, Th, E6h); }
            else KG_PUTH(row + 1, Th, E4h);
            if (a < 5) T = cmul(T, G);
        }
        RAWBAR()
#pragma unroll
        for (int ks = 0; ks < 8; ++ks) vf[ks] = *(const u4v*)(r0 - 10240 + l15 * KG_ROW + ks * 64 + q4 * 16);
        RAWBAR()
        f4v acc1, acc2;
        KG_MMA(acc1)
        T = cmul(W, cexp2(zr, zi, 1024.f));
#pragma unroll
        for (int a = 0; a < 9; ++a) {
            KG_PUT(a, T);
            if (a < 8) T = cmul(T, G);
        }
        KG_MMA(acc2)
        {
            u32 h01 = pkh(acc1[0] * KSC, acc1[1] * KSC), h23 = pkh(acc1[2] * KSC, acc1[3] * KSC);
            if (q4 < 2) {
                *(u2v*)(kc + (64 * q4 + 4 * l15) * 2) = (u2v){h01, h23};
            } else {
                *(u32*)(kc + (128 + 64 * (q4 - 2) + 2 * l15) * 2) = h01;
                *(u32*)(kc + (128 + 64 * (q4 - 2) + 32 + 2 * l15) * 2) = h23;
            }
            u32 g01 = pkh(acc2[0] * KSC, acc2[1] * KSC), g23 = pkh(acc2[2] * KSC, acc2[3] * KSC);
            if (q4 < 2) {
                kch[256 + 64 * q4 + l15] = (u16)g01;
                kch[256 + 64 * q4 + 16 + l15] = (u16)(g01 >> 16);
                kch[256 + 64 * q4 + 32 + l15] = (u16)g23;
                kch[256 + 64 * q4 + 48 + l15] = (u16)(g23 >> 16);
            } else if (lane == 32) {
                kch[384] = (u16)g01;
            }
        }
        if (lane < 32) {
            u2v s = *(const u2v*)(kc + lane * 8);
            u32 s4 = kch[lane * 4 + 4];
            u4v b2;
            b2[0] = (s[0] & 0xffffu) | (s[0] << 16);
            b2[1] = (s[0] >> 16) | (s[1] << 16);
            b2[2] = (s[1] & 0xffffu) | (s[1] << 16);
            b2[3] = (s[1] >> 16) | (s4 << 16);
            st16(kx + 512 + lane * 16, b2);
        }
        {
            u32 s = *(const u32*)(kc + 256 + lane * 4);
            u32 c = kch[128 + lane * 2 + 2];
            u4v v;
            v[0] = (s & 0xffffu) | (s << 16);
            v[1] = s;
            v[2] = (s >> 16) | (s & 0xffff0000u);
            v[3] = c | (c << 16);
            st16(kx + 1024 + lane * 16, v);
        }
#pragma unroll
        for (int it = 0; it < 2; ++it) {
            int oc = lane + 64 * it;
            u32 a = kch[256 + oc], c = kch[257 + oc];
            u4v v;
            v[0] = a | (a << 16);
            v[1] = v[0];
            v[2] = ((oc & 1) ? c : a) | (c << 16);
            v[3] = c | (c << 16);
            st16(kx + 2048 + oc * 16, v);
        }
    }
}

__device__ __forceinline__ NOPK void w2pack_role(const Params& p, int unit, char* sw) {
    const int lane = threadIdx.x & 63, l15 = lane & 15, q4 = lane >> 4;
    const int nb = unit / KS2, ks = unit - nb * KS2;
#pragma unroll
    for (int i = 0; i < 5; ++i) {
        int c = lane + 64 * i, row = c / 10, ch = c - row * 10;
        *(u4v*)(sw + c * 16) = *(const u4v*)(p.w2 + (size_t)(ks * 32 + row) * D + nb * 80 + ch * 8);
    }
    const int q = l15 >> 2, pp = lane & 3;
    char* dst = p.ws + WS_W2P + (size_t)unit * 5120 + lane * 16;
    const char* src = sw + (16 * (q4 >> 1) + 4 * (q4 & 1) + q) * 160 + pp * 8;
#pragma unroll
    for (int dt = 0; dt < 5; ++dt) {
        u2v lo = tr_read(src + dt * 32);
        u2v hi = tr_read(src + 8 * 160 + dt * 32);
        st16(dst + dt * 1024, (u4v){lo[0], lo[1], hi[0], hi[1]});
    }
}

#define CODE_WARM(entry, piece, ldsa, lo16_) GLDS16(ldsa, lo16_, (const char*)(entry) + (piece)*1024)

__global__ __launch_bounds__(512, 4) NOPK void k1(Params p) {
    extern __shared__ __attribute__((aligned(16))) char smem[];
    const int b = blockIdx.x;
    const int w = __builtin_amdgcn_readfirstlane(threadIdx.x >> 6);
    TLB()
    if (b < 224) {
        gemm1_role(p, b, smem);
    } else if (b < 438) {
        const int d = (b - 224) * 3 + (w >> 1);
        if (w < 6 && d < D) {
            kgen_role(p, d, w & 1, smem + w * 10240);
        } else {
            if (w >= 6) {
                const int unit = (b - 224) * 2 + (w - 6);
                w2pack_role(p, unit, smem + w * 10240);
                if (unit < 4) w2pack_role(p, 428 + unit, smem + w * 10240);
                if (b == 437 && (threadIdx.x & 63) < 8 && w == 7) *(u32*)(p.ws + WS_CNT + (threadIdx.x & 63) * 256) = 0u;
            }
            __builtin_amdgcn_s_barrier();
            __builtin_amdgcn_s_barrier();
        }
    } else {
        ln_role(p, b - 438, smem);
    }
    TLE(0)
}

#define PF0 16
#define PFD 8
static constexpr int CV_WAVE = 9216;
static constexpr int K2_RED = 3 * CV_WAVE;
static constexpr int K2_YS = K2_RED + 61440;
static constexpr int K2_LDS = K2_YS + 10240;

#define G2_BUF(C, i) C "v"(af[i][0]), C "v"(af[i][1]), C "v"(af[i][2]), C "v"(af[i][3]), C "a"(wf[i][0]), C "a"(wf[i][1]), C "a"(wf[i][2]), C "a"(wf[i][3]), C "a"(wf[i][4])
#define G2_LOAD(i)                                                                                                   \
    asm volatile("buffer_load_dwordx4 %0, %9, %15, %17 offen\n\tbuffer_load_dwordx4 %4, %13, %16, %18 offen\n\t"     \
                 "buffer_load_dwordx4 %1, %10, %15, %17 offen\n\tbuffer_load_dwordx4 %2, %11, %15, %17 offen\n\t"    \
                 "buffer_load_dwordx4 %3, %12, %15, %17 offen\n\tbuffer_load_dwordx4 %5, %13, %16, %18 offen offset:1024\n\t" \
                 "buffer_load_dwordx4 %6, %13, %16, %18 offen offset:2048\n\tbuffer_load_dwordx4 %7, %13, %16, %18 offen offset:3072\n\t" \
                 "buffer_load_dwordx4 %8, %14, %16, %18 offen"                                                       \
                 : G2_BUF("=&", i) : "v"(ao[0]), "v"(ao[1]), "v"(ao[2]), "v"(ao[3]), "v"(ao[4]), "v"(ao[5]), "s"(ra), "s"(rw), \
                   "s"((i)*4096), "s"((i)*5120) : "memory");
#define G2_MF(d, w_, a_) "v_mfma_f32_16x16x32_bf16 %" #d ", %" #w_ ", %" #a_ ", %" #d "\n\t"
#define G2_M4(dt, i, VO, OFF)                                                                           \
    asm volatile(G2_MF(0, 4, 5) G2_MF(1, 4, 6) G2_MF(2, 4, 7) G2_MF(3, 4, 8)                            \
                 "buffer_load_dwordx4 %4, %9, %10, %11 offen offset:" #OFF                              \
                 : "+a"(acc2[dt][0]), "+a"(acc2[dt][1]), "+a"(acc2[dt][2]), "+a"(acc2[dt][3]), "+a"(wf[i][dt]) \
                 : "v"(af[i][0]), "v"(af[i][1]), "v"(af[i][2]), "v"(af[i][3]), "v"(ao[VO]), "s"(rw), "s"(sw) : "memory");
#define G2_BODY(i)                                                                                      \
    {                                                                                                   \
        G2_M4(0, i, 4, 0) G2_M4(1, i, 4, 1024) G2_M4(2, i, 4, 2048) G2_M4(3, i, 4, 3072) G2_M4(4, i, 5, 0) \
        asm volatile("buffer_load_dwordx4 %0, %4, %8, %9 offen\n\tbuffer_load_dwordx4 %1, %5, %8, %9 offen\n\t" \
                     "buffer_load_dwordx4 %2, %6, %8, %9 offen\n\tbuffer_load_dwordx4 %3, %7, %8, %9 offen" \
                     : "+v"(af[i][0]), "+v"(af[i][1]), "+v"(af[i][2]), "+v"(af[i][3])                   \
                     : "v"(ao[0]), "v"(ao[1]), "v"(ao[2]), "v"(ao[3]), "s"(ra), "s"(sa) : "memory");    \
    }
#define G2_STEP(i) { asm volatile("s_waitcnt vmcnt(45)" : G2_BUF("+", i)::"memory"); G2_BODY(i) sa += 4096; sw += 5120; }
#define G2_M4N(dt, i)                                                                                   \
    asm volatile(G2_MF(0, 4, 5) G2_MF(1, 4, 6) G2_MF(2, 4, 7) G2_MF(3, 4, 8) "s_nop 0"                  \
                 : "+a"(acc2[dt][0]), "+a"(acc2[dt][1]), "+a"(acc2[dt][2]), "+a"(acc2[dt][3])           \
                 : "a"(wf[i][dt]), "v"(af[i][0]), "v"(af[i][1]), "v"(af[i][2]), "v"(af[i][3]));
#define G2_SSTEP(k, NW)                                                                                 \
    {                                                                                                   \
        asm volatile("s_waitcnt vmcnt(" #NW ")" : G2_BUF("+", (k) % 6)::"memory");                      \
        if constexpr ((k) < 2) {                                                                        \
            const u32 sa = ((k) + 6) * 4096, sw = ((k) + 6) * 5120;                                     \
            G2_BODY((k) % 6)                                                                            \
        } else {                                                                                        \
            G2_M4N(0, (k) % 6) G2_M4N(1, (k) % 6) G2_M4N(2, (k) % 6) G2_M4N(3, (k) % 6) G2_M4N(4, (k) % 6) \
        }                                                                                               \
        __builtin_amdgcn_sched_barrier(0);                                                              \
    }
#define G2_ACC(a0) "+a"(acc2[a0][0]), "+a"(acc2[a0][1]), "+a"(acc2[a0][2]), "+a"(acc2[a0][3])
#define G2_ZERO()                                                                                       \
    _Pragma("unroll") for (int dt = 0; dt < 5; ++dt) _Pragma("unroll") for (int tt = 0; tt < 4; ++tt) acc2[dt][tt] = (f4v){0.f, 0.f, 0.f, 0.f}; \
    asm volatile("s_nop 1" : G2_ACC(0), G2_ACC(1), G2_ACC(2), G2_ACC(3), G2_ACC(4));
#undef STOFF
#define STOFF (4 << 20)
__device__ __forceinline__ NOPK void conv_wave(const Params& p, int d, u32 lb, char* img, u4v (&af)[6][4], u4v (&wf)[6][5],
                                                f4v (&acc2)[5][4], u4v ra, u4v rw, const u32 (&ao)[6], u32 svc, const u32* cnt,
                                                const char* yb, u32 ysl) {
    const int lane = threadIdx.x & 63, l15 = lane & 15, q4 = lane >> 4;
    const int q = l15 >> 2, pp = lane & 3;
    const u32 kra = lb + REC_K + (32 * q4 - 4 * q + 4 * pp) * 2;
    u32 xra[2][2][2];
#pragma unroll
    for (int ap = 0; ap < 2; ++ap)
#pragma unroll
        for (int h = 0; h < 2; ++h)
#pragma unroll
            for (int g = 0; g < 2; ++g) {
                int G = l15 + 8 * g - 2 * q4 - h + 8;
                xra[ap][h][g] = lb + 4160 + G * 32 + ((ap ^ ((G >> 3) & 1)) * 16);
            }
    const u32 csrc = (lane >= 16 ? lane - 16 : (lane == 0 ? 63 : lane + 47)) * 4;
    asm volatile("s_waitcnt vmcnt(0)" ::: "memory");
    u4v kf[16], xf[4];
    f4v acc[8];
    float Y[8][7];
    float scale;
    asm volatile("ds_read_b32 %0, %1" : "=v"(scale) : "v"(lb));
#define CV_XRD(s)                                                                                          \
    {                                                                                                      \
        u2v v0, v1;                                                                                        \
        asm volatile("ds_read_b64 %0, %2 offset:%4\n\tds_read_b64 %1, %3 offset:%4" : "=&v"(v0), "=&v"(v1) \
                     : "v"(xra[(s) >> 5][0][(s)&1]), "v"(xra[(s) >> 5][1][(s)&1]), "i"((((s)&15) >> 1) * 512 + (((s) >> 4) & 1) * 8)); \
        xf[(s)&3] = (u4v){v0[0], v0[1], v1[0], v1[1]};                                                     \
    }
    CV_XRD(0) CV_XRD(1) CV_XRD(2)
#pragma unroll
    for (int e = 0; e < 16; ++e) {
        u2v lo, hi;
        asm volatile("ds_read_b64_tr_b16 %0, %2 offset:%3\n\tds_read_b64_tr_b16 %1, %2 offset:%4" : "=&v"(lo), "=&v"(hi)
                     : "v"(kra), "i"(e * 256), "i"(e * 256 + 32));
        kf[e] = (u4v){lo[0], lo[1], hi[0], hi[1]};
    }
    asm volatile("s_waitcnt lgkmcnt(0)" : "+v"(kf[0]), "+v"(kf[1]), "+v"(kf[2]), "+v"(kf[3]), "+v"(kf[4]), "+v"(kf[5]), "+v"(kf[6]), "+v"(kf[7]),
                 "+v"(kf[8]), "+v"(kf[9]), "+v"(kf[10]), "+v"(kf[11]), "+v"(kf[12]), "+v"(kf[13]), "+v"(kf[14]), "+v"(kf[15]), "+v"(scale));
#define CV_STEP(s)                                                                                         \
    {                                                                                                      \
        constexpr int a = (s) >> 4, g = (s)&15;                                                            \
        if constexpr ((s) + 3 < 64) {                                                                      \
            CV_XRD(((s) + 3) & 63)                                                                         \
            asm volatile("s_waitcnt lgkmcnt(6)" : "+v"(xf[(s)&3]));                                        \
        } else if constexpr ((s) == 61) {                                                                  \
            asm volatile("s_waitcnt lgkmcnt(0)" : "+v"(xf[1]), "+v"(xf[2]), "+v"(xf[3]));                  \
        }                                                                                                  \
        _Pragma("unroll") for (int e = (g + 1) & 1; e + g <= 15; e += 2)                                   \
            acc[(e + g) >> 1] = mfma_h(kf[e], xf[(s)&3], g == 0 ? (f4v){0.f, 0.f, 0.f, 0.f} : acc[(e + g) >> 1]); \
        if constexpr (g == 15) {                                                                           \
            _Pragma("unroll") for (int t = 0; t < 8; ++t) _Pragma("unroll") for (int r = 0; r < 4; ++r)    \
                Y[t][r + a] = (a == 0 || r == 3) ? acc[t][r] : Y[t][r + a] + acc[t][r];                    \
        }                                                                                                  \
        if constexpr ((s) >= PF0 && ((s) - PF0) % PFD == 0 && ((s) - PF0) / PFD < 6) G2_LOAD((((s) - PF0) / PFD) % 6) \
        __builtin_amdgcn_sched_barrier(0);                                                                 \
    }
#define CV_S4(s) CV_STEP(s) CV_STEP((s) + 1) CV_STEP((s) + 2) CV_STEP((s) + 3)
#define CV_S16(s) CV_S4(s) CV_S4((s) + 4) CV_S4((s) + 8) CV_S4((s) + 12)
    CV_S16(0) CV_S16(16) CV_S16(32) CV_S16(48)
    float cr[8][3];
#pragma unroll
    for (int t = 0; t < 8; ++t)
#pragma unroll
        for (int r = 0; r < 3; ++r)
            cr[t][r] = __builtin_bit_cast(float, __builtin_amdgcn_ds_bpermute(csrc, __builtin_bit_cast(int, Y[t][4 + r])));
    __builtin_amdgcn_sched_barrier(0);
#pragma unroll
    for (int t = 0; t < 8; ++t) {
        float y[4];
#pragma unroll
        for (int r = 0; r < 3; ++r) y[r] = Y[t][r] + (lane == 0 ? (t ? cr[t - (t > 0)][r] : 0.f) : cr[t][r]);
        y[3] = Y[t][3];
        *(u2v*)(img + t * 512 + (16 * l15 + 4 * q4) * 2) = (u2v){pkbf(y[0] * scale, y[1] * scale), pkbf(y[2] * scale, y[3] * scale)};
    }
    char* dst = p.ws + WS_YT + (size_t)d * 4096 + lane * 16;
    {
        u4v o[4];
#pragma unroll
        for (int i = 0; i < 4; ++i) o[i] = *(const u4v*)(img + i * 1024 + lane * 16);
#pragma unroll
        for (int i = 0; i < 4; ++i)
            asm volatile("global_store_dwordx4 %0, %1, off offset:%2 sc1\n\ts_nop 1" ::"v"(dst), "v"(o[i]), "i"(i * 1024) : "memory");
    }
    G2_ZERO()
    asm volatile("s_waitcnt vmcnt(0)\n\ts_mov_b64 exec, 1\n\tglobal_atomic_add %0, %1, %2\n\ts_mov_b64 exec, -1"
                 ::"v"(0u), "v"(1u), "s"(p.ws + WS_CNT + (d / 80) * 256) : "memory");
    G2_SSTEP(0, 63) G2_SSTEP(1, 63) G2_SSTEP(2, 63) G2_SSTEP(3, 63)
    u32 cv = 0;
    asm volatile("s_cmp_lg_u32 %1, 0\n\ts_cbranch_scc1 .Lsv%=\n\tglobal_load_dword %0, %0, %2 sc1\n.Lsv%=:" : "+v"(cv) : "s"(svc), "s"(cnt) : "memory", "scc");
    G2_SSTEP(4, 63) G2_SSTEP(5, 63)
    asm volatile("s_waitcnt vmcnt(0)" : "+v"(cv), G2_BUF("+", 0), G2_BUF("+", 1)::"memory");
    if (svc == 0) {
        unsigned spins = 0;
        while (__builtin_amdgcn_readfirstlane(cv) < 80u && ++spins < (1u << 22)) {
            __builtin_amdgcn_s_sleep(4);
            cv = __hip_atomic_load(cnt, __ATOMIC_RELAXED, __HIP_MEMORY_SCOPE_AGENT);
        }
        const u32 yo = (lane >> 3) * 4096 + (lane & 7) * 16;
#pragma unroll
        for (int i = 0; i < 10; ++i)
            asm volatile("s_mov_b32 m0, %0\n\ts_nop 0\n\tglobal_load_lds_dwordx4 %1, %2 sc1" ::"s"(ysl + i * 1024), "v"(yo), "s"(yb + i * 32768) : "memory");
    }
    G2_SSTEP(6, 63) G2_SSTEP(7, 63)
    asm volatile("s_waitcnt vmcnt(0)\n\ts_nop 7\n\ts_nop 7" : G2_ACC(0), G2_ACC(1), G2_ACC(2), G2_ACC(3), G2_ACC(4)::"memory");
}

__global__ __launch_bounds__(256, 1) NOPK void k2(Params p) {
    extern __shared__ __attribute__((aligned(16))) char smem[];
    const int b = blockIdx.x;
    const int w = __builtin_amdgcn_readfirstlane(threadIdx.x >> 6);
    TLB()
    const int xcd = b & 7, j = b >> 3;
    const int mb = 8 * (xcd >> 1) + (j & 7), nb = 4 * (xcd & 1) + (j >> 3);
    const int nch = (j & 1) ? 2 : 3;
    const int ch0 = xcd * 80 + (j >> 1) * 5 + ((j & 1) ? 3 : 0);
    const int k0 = ((j & 1) && w == 3) ? 34 : 8 * w;
    const int nk = w < nch ? 8 : ((j & 1) ? 2 * w + 14 : 30);
    const char* ab = p.ws + WS_ACT + ((size_t)mb * KS2 + k0) * 4096;
    const char* wb = p.ws + WS_W2P + ((size_t)nb * KS2 + k0) * 5120;
    u4v ra = {(u32)(size_t)ab, (u32)((size_t)ab >> 32), (u32)nk * 4096u, 0x00020000u};
    u4v rw = {(u32)(size_t)wb, (u32)((size_t)wb >> 32), (u32)nk * 5120u, 0x00020000u};
    const u32 lo16 = (threadIdx.x & 63) * 16;
    const u32 lb = lds_addr(smem);
    const u32 ao[6] = {lo16 + (w & 3) * 1024, lo16 + ((w + 1) & 3) * 1024, lo16 + ((w + 2) & 3) * 1024, lo16 + ((w + 3) & 3) * 1024, lo16, lo16 + 4096};
    SGDECL SG(0)
    if (w < nch) {
        const char* src = p.ws + WS_REC + (size_t)(ch0 + w) * REC;
#pragma unroll
        for (int i = 0; i < 9; ++i) GLDS16(lb + w * CV_WAVE + i * 1024, lo16, src + i * 1024);
    }
    u4v af[6][4], wf[6][5];
    SG(1)
    f4v acc2[5][4];
    if (w < nch) {
        conv_wave(p, ch0 + w, lb + w * CV_WAVE, smem + w * CV_WAVE, af, wf, acc2, ra, rw, ao, (u32)w,
                  (const u32*)(p.ws + WS_CNT + nb * 256), p.ws + WS_YT + (size_t)(nb * 80) * 4096 + (size_t)mb * 128, lb + K2_YS);
    } else {
        G2_LOAD(0) G2_LOAD(1) G2_LOAD(2) G2_LOAD(3) G2_LOAD(4) G2_LOAD(5)
        G2_ZERO()
        u32 sa = 6 * 4096, sw = 6 * 5120;
#pragma nounroll
        for (int r = 0; r < nk; r += 6) { G2_STEP(0) G2_STEP(1) G2_STEP(2) G2_STEP(3) G2_STEP(4) G2_STEP(5) }
        asm volatile("s_waitcnt vmcnt(0)" : G2_BUF("+", 0), G2_BUF("+", 1), G2_BUF("+", 2)::"memory");
        asm volatile("" : G2_BUF("+", 3), G2_BUF("+", 4), G2_BUF("+", 5)::"memory");
        asm volatile("s_nop 7\n\ts_nop 7" : G2_ACC(0), G2_ACC(1), G2_ACC(2), G2_ACC(3), G2_ACC(4)::"memory");
    }
    SG(2)
    SG(3)
    int lane = threadIdx.x & 63;
    asm volatile("" : "+v"(lane));
    const int l15 = lane & 15, q4 = lane >> 4;
    const int t = mb * 64 + w * 16 + l15;
    const int dbase = nb * 80 + 4 * q4;
    u2v xv[5];
#pragma unroll
    for (int dt = 0; dt < 5; ++dt) xv[dt] = *(const u2v*)(p.x + (size_t)t * D + dbase + dt * 16);
    char* red = smem + K2_RED;
#pragma unroll
    for (int tt = 1; tt < 4; ++tt) {
        char* dst = red + (((tt + w) & 3) * 3 + tt - 1) * 5120 + lane * 16;
#pragma unroll
        for (int dt = 0; dt < 5; ++dt) *(f4v*)(dst + dt * 1024) = acc2[dt][tt];
    }
    RAWBAR()
    SG(4)
    const char* ys = smem + K2_YS + (4 * q4 + (l15 >> 2)) * 128 + w * 32 + (lane & 3) * 8;
    const char* rsrc = red + w * 15360 + lane * 16;
#pragma unroll
    for (int dt = 0; dt < 5; ++dt) {
        const u2v sv = tr_read(ys + dt * 2048);
        const f4v g = (acc2[dt][0] + *(const f4v*)(rsrc + dt * 1024)) + (*(const f4v*)(rsrc + 5120 + dt * 1024) + *(const f4v*)(rsrc + 10240 + dt * 1024));
        u2v o;
#pragma unroll
        for (int r2 = 0; r2 < 2; ++r2) {
            const float p0 = g[2 * r2], p1 = g[2 * r2 + 1];
            const float g0 = p0 * __builtin_amdgcn_rcpf(1.0f + __builtin_amdgcn_exp2f(-1.44269504f * p0));
            const float g1 = p1 * __builtin_amdgcn_rcpf(1.0f + __builtin_amdgcn_exp2f(-1.44269504f * p1));
            o[r2] = pkbf(fmaf(bflo(sv[r2]), g0, bflo(xv[dt][r2])), fmaf(bfhi(sv[r2]), g1, bfhi(xv[dt][r2])));
        }
        *(u2v*)(p.out + (size_t)t * D + dbase + dt * 16) = o;
    }
    SG(5) SGDUMP(6)
    TLE(1 << 20)
}

extern "C" void kernel_launch(void* const* d_in, const int* in_sizes, int n_in, void* d_out, int out_size, void* d_ws,
                              size_t ws_size, hipStream_t stream) {
    Params p;
    p.x = (const u16*)d_in[0];
    p.Are = (const u16*)d_in[1]; p.Aim = (const u16*)d_in[2]; p.Bre = (const u16*)d_in[3]; p.Bim = (const u16*)d_in[4];
    p.Cre = (const u16*)d_in[5]; p.Cim = (const u16*)d_in[6]; p.logdt = (const u16*)d_in[7]; p.Dskip = (const u16*)d_in[8];
    p.w1 = (const u16*)d_in[10]; p.b1 = (const u16*)d_in[11]; p.w2 = (const u16*)d_in[12];
    p.out = (u16*)d_out;
    p.ws = (char*)d_ws;
    (void)hipFuncSetAttribute((const void*)k1, hipFuncAttributeMaxDynamicSharedMemorySize, LDS_BYTES);
    (void)hipFuncSetAttribute((const void*)k2, hipFuncAttributeMaxDynamicSharedMemorySize, K2_LDS);
    k1<<<502, 512, LDS_BYTES, stream>>>(p);
    k2<<<256, 256, K2_LDS, stream>>>(p);
}
```

```cpp
#include <hip/hip_runtime.h>
#include <stdint.h>

#if defined(__HIP_DEVICE_COMPILE__) && !defined(ALLOWPK)
#define NOPK __attribute__((target("no-packed-fp32-ops")))
#else
#define NOPK
#endif
typedef unsigned short u16;
typedef unsigned int u32;
typedef short s4v __attribute__((ext_vector_type(4)));
typedef u32 u2v __attribute__((ext_vector_type(2)));
typedef u32 u4v __attribute__((ext_vector_type(4)));
typedef float f2v __attribute__((ext_vector_type(2)));
typedef float f4v __attribute__((ext_vector_type(4)));
typedef _Float16 h2v __attribute__((ext_vector_type(2)));
typedef _Float16 h8v __attribute__((ext_vector_type(8)));
typedef __bf16 b2v __attribute__((ext_vector_type(2)));
typedef __bf16 b8v __attribute__((ext_vector_type(8)));
#define LDSP(T, p) ((__attribute__((address_space(3))) T*)(p))
#define GLB(T, p) ((__attribute__((address_space(1))) T*)(p))

static constexpr int L = 2048, D = 640, NS = 128, F = 1728;
static constexpr int KS2 = F / 32;

static constexpr size_t WS_ACT = 0;
static constexpr size_t WS_W2P = WS_ACT + (size_t)L * F * 2;
static constexpr int REC_K = 32, REC_X = 4160 + 512, REC = 8768;
static constexpr size_t WS_REC = WS_W2P + (size_t)F * D * 2;
static constexpr size_t WS_YT = WS_REC + (size_t)641 * REC + 192;
static constexpr size_t WS_CNT = WS_YT + (size_t)L * D * 2;
static_assert(WS_REC % 256 == 0 && WS_YT % 256 == 0, "alignment");

static constexpr int LDS_BYTES = 80 * 1024;

__device__ __forceinline__ NOPK u32 f2u(float v) { return __builtin_bit_cast(u32, v); }
__device__ __forceinline__ NOPK float bf2f(u32 v) { return __builtin_bit_cast(float, (u32)(v << 16)); }
__device__ __forceinline__ NOPK float bflo(u32 v) { return __builtin_bit_cast(float, (u32)(v << 16)); }
__device__ __forceinline__ NOPK float bfhi(u32 v) { return __builtin_bit_cast(float, (u32)(v & 0xffff0000u)); }
__device__ __forceinline__ NOPK u32 pkbf(float a, float b) {
    f2v v = {a, b};
    return __builtin_bit_cast(u32, __builtin_convertvector(v, b2v));
}
__device__ __forceinline__ NOPK u32 pkh(float a, float b) {
    f2v v = {a, b};
    return __builtin_bit_cast(u32, __builtin_convertvector(v, h2v));
}
__device__ __forceinline__ NOPK float rbf(float a) { return bflo(pkbf(a, a)); }
__device__ __forceinline__ NOPK f4v mfma_bf(u4v a, u4v b, f4v c) {
    return __builtin_amdgcn_mfma_f32_16x16x32_bf16(__builtin_bit_cast(b8v, a), __builtin_bit_cast(b8v, b), c, 0, 0, 0);
}
__device__ __forceinline__ NOPK f4v mfma_h(u4v a, u4v b, f4v c) {
    return __builtin_amdgcn_mfma_f32_16x16x32_f16(__builtin_bit_cast(h8v, a), __builtin_bit_cast(h8v, b), c, 0, 0, 0);
}
__device__ __forceinline__ NOPK u2v tr_read(const char* p) {
    return __builtin_bit_cast(u2v, __builtin_amdgcn_ds_read_tr16_b64_v4i16(LDSP(s4v, p)));
}
__device__ __forceinline__ NOPK float dot2bf(u32 a, u32 b, float c) {
    return __builtin_amdgcn_fdot2_f32_bf16(__builtin_bit_cast(b2v, a), __builtin_bit_cast(b2v, b), c, false);
}
__device__ __forceinline__ NOPK void glds16(const void* g, void* l) {
    __builtin_amdgcn_global_load_lds((const __attribute__((address_space(1))) void*)g,
                                     (__attribute__((address_space(3))) void*)l, 16, 0, 0);
}
__device__ __forceinline__ NOPK u32 lds_addr(const void* l) { return (u32)(size_t)(__attribute__((address_space(3))) const char*)l; }
#define GLDS16(ldsa, voff, sbase) \
    asm volatile("s_mov_b32 m0, %0\n\ts_nop 0\n\tglobal_load_lds_dwordx4 %1, %2" ::"s"(ldsa), "v"(voff), "s"(sbase) : "memory")
#define GLDS4(ldsa, voff, sbase) \
    asm volatile("s_mov_b32 m0, %0\n\ts_nop 0\n\tglobal_load_lds_dword %1, %2" ::"s"(ldsa), "v"(voff), "s"(sbase) : "memory")
template <int CTRL> __device__ __forceinline__ NOPK float dpp(float v) {
    return __builtin_bit_cast(float, __builtin_amdgcn_update_dpp(0, __builtin_bit_cast(int, v), CTRL, 0xf, 0xf, true));
}
__device__ __forceinline__ NOPK float swap32(float v) {
    const u32 b = __builtin_bit_cast(u32, v);
    const auto r = __builtin_amdgcn_permlane32_swap(b, b, false, false);
    return __builtin_bit_cast(float, (threadIdx.x & 32) ? r[0] : r[1]);
}
__device__ __forceinline__ NOPK float red16(float v) {
    v += dpp<0xB1>(v); v += dpp<0x4E>(v); v += dpp<0x141>(v); v += dpp<0x140>(v);
    return v;
}
__device__ __forceinline__ NOPK float max64(float v) {
    v = fmaxf(v, dpp<0xB1>(v)); v = fmaxf(v, dpp<0x4E>(v)); v = fmaxf(v, dpp<0x141>(v)); v = fmaxf(v, dpp<0x140>(v));
    const int b = __builtin_bit_cast(int, v);
    float a0 = __builtin_bit_cast(float, __builtin_amdgcn_readlane(b, 0)), a1 = __builtin_bit_cast(float, __builtin_amdgcn_readlane(b, 16));
    float a2 = __builtin_bit_cast(float, __builtin_amdgcn_readlane(b, 32)), a3 = __builtin_bit_cast(float, __builtin_amdgcn_readlane(b, 48));
    return fmaxf(fmaxf(a0, a1), fmaxf(a2, a3));
}
__device__ __forceinline__ NOPK void st16(void* ptr, u4v v) {
    asm volatile("global_store_dwordx4 %0, %1, off sc1\n\ts_nop 1" ::"v"(ptr), "v"(v) : "memory");
}
__device__ __forceinline__ NOPK void ln_stats(float s1, float s2, float& mb, float& den) {
    float mean = s1 * (1.0f / D);
    float var = fmaxf(s2 * (1.0f / D) - mean * mean, 0.f);
    mb = rbf(mean);
    den = rbf(__builtin_amdgcn_sqrtf(rbf(rbf(var) + 1e-5f)));
}
__device__ __forceinline__ NOPK float ln_apply(float x, float mb, float rden) { return rbf(rbf(x - mb) * rden); }

#define SG(i) {}
#define SGDECL
#define SGDUMP(n)
#define TLB()
#define TLE(off)
struct Params {
    const u16 *x, *Are, *Aim, *Bre, *Bim, *Cre, *Cim, *logdt, *Dskip, *w1, *b1, *w2;
    u16* out;
    char* ws;
};

typedef float f16v __attribute__((ext_vector_type(16)));
__device__ __forceinline__ NOPK f16v mfma32_bf(u4v a, u4v b, f16v c) {
    return __builtin_amdgcn_mfma_f32_32x32x16_bf16(__builtin_bit_cast(b8v, a), __builtin_bit_cast(b8v, b), c, 0, 0, 0);
}
#define STATV 0
#define VMWAIT(N) asm volatile("s_waitcnt vmcnt(" #N ") lgkmcnt(0)" ::: "memory")

#define RAWBAR() { asm volatile("s_waitcnt lgkmcnt(0)" ::: "memory"); __builtin_amdgcn_s_barrier(); asm volatile("" ::: "memory"); }

__device__ __forceinline__ NOPK void gemm1_role(const Params& p, int b, char* smem) {
    const int tid = threadIdx.x, lane = tid & 63;
    const int w = __builtin_amdgcn_readfirstlane(tid >> 6);
    const int wt = w >> 2, wf = w & 3;
    const int xcd = b & 7, j = b >> 3;
    const int mb = 4 * (xcd >> 1) + (j & 3), nt = 7 * (xcd & 1) + (j >> 2);
    const int l31 = lane & 31, hh = lane >> 5;
    const bool half = (nt == 13);
    SGDECL
#define STOFF 0
    SG(0)
    u32 xo[2], wo;
#pragma unroll
    for (int i = 0; i < 2; ++i) {
        int P = (2 * w + i) * 64 + lane, row = P >> 3, ch = (P & 7) ^ ((row >> 1) & 7);
        xo[i] = (u32)(row * (D * 2) + ch * 16);
    }
    {
        int P = w * 64 + lane, k = P >> 4, ch = (P & 15) ^ ((k & 3) << 2);
        if (half) ch &= 7;
        wo = (u32)(k * (F * 2) + ch * 16);
    }
    const char* xb = (const char*)p.x + (size_t)mb * 128 * (D * 2);
    const char* wb = (const char*)p.w1 + nt * 256;
    const u32 lx = lds_addr(smem) + w * 2048, lw = lds_addr(smem) + 49152 + w * 1024;
#define SROT(st) (st)
#define G1_X(st) { const char* s_ = xb + SROT(st) * 128; GLDS16(lx + ((st) % 3) * 16384, xo[0], s_); GLDS16(lx + ((st) % 3) * 16384 + 1024, xo[1], s_); }
#define G1_W(h) { const char* s_ = wb + (size_t)(SROT((h) >> 1) * 64 + ((h)&1) * 32) * (F * 2); GLDS16(lw + ((h)&3) * 8192, wo, s_); }
    G1_W(0) G1_X(0)
    u2v bia[4];
    {
        const u32 bo = (u32)min(nt * 128 + 32 * wf + 4 * hh, F - 28) * 2u;
        asm volatile("global_load_dwordx2 %0, %4, %5\n\tglobal_load_dwordx2 %1, %4, %5 offset:16\n\t"
                     "global_load_dwordx2 %2, %4, %5 offset:32\n\tglobal_load_dwordx2 %3, %4, %5 offset:48"
                     : "=&v"(bia[0]), "=&v"(bia[1]), "=&v"(bia[2]), "=&v"(bia[3]) : "v"(bo), "s"(p.b1) : "memory");
    }
    G1_W(1) G1_X(1) G1_W(2) G1_W(3) G1_X(2)

    const int q = (lane & 15) >> 2, pp = lane & 3, blk = (lane >> 4) & 1;
    const int ro = (wf & 1) ^ wt, ri = wf >> 1;
    const char* xa[4][2];
    const char* wa[2];
#pragma unroll
    for (int u = 0; u < 4; ++u)
#pragma unroll
        for (int i = 0; i < 2; ++i)
            xa[u][i] = smem + (64 * wt + 32 * (i ^ ri) + l31) * 128 + (((2 * (u ^ ro) + hh) ^ ((l31 >> 1) & 7)) << 4);
#pragma unroll
    for (int o = 0; o < 2; ++o)
        wa[o] = smem + 49152 + (16 * (o ^ ro) + 8 * hh + q) * 256 + (((4 * wf + 2 * blk + (pp >> 1)) ^ (q << 2)) << 4) + 8 * (pp & 1);

    f16v acc[2];
#pragma unroll
    for (int i = 0; i < 16; ++i) acc[0][i] = acc[1][i] = 0.f;
    u4v xf[2][2][2], wfr[2][2];
    float s1a = 0.f, s1b = 0.f, s2a = 0.f, s2b = 0.f, csa = 0.f, csb = 0.f;
    SG(1)
#define G1_READ(h, o)                                                                                    \
    {                                                                                                    \
        u2v lo = tr_read(wa[o] + ((h)&3) * 8192), hi = tr_read(wa[o] + ((h)&3) * 8192 + 1024);           \
        wfr[(h)&1][o] = (u4v){lo[0], lo[1], hi[0], hi[1]};                                               \
        xf[(h)&1][o][0] = *(const u4v*)(xa[((h)&1) * 2 + o][0] + (((h) >> 1) % 3) * 16384);              \
        xf[(h)&1][o][1] = *(const u4v*)(xa[((h)&1) * 2 + o][1] + (((h) >> 1) % 3) * 16384);              \
    }
#define G1_M(h, o, i)                                                                                    \
    if ((h) >= 0) {                                                                                      \
        acc[i] = mfma32_bf(wfr[(h)&1][o], xf[(h)&1][o][i], acc[i]);                                      \
        asm volatile("" : "+v"(acc[i]));                                                                 \
    }                                                                                                    \
    __builtin_amdgcn_sched_barrier(0);
#define G1_STAT(h, e)                                                                                    \
    if ((h) >= 0) {                                                                                      \
        const u32 xv_ = xf[(h)&1][0][0][e];                                                              \
        s1a = dot2bf(xv_, 0x3f803f80u, s1a); s2a = dot2bf(xv_, xv_, s2a);                                \
        csa = dot2bf(wfr[(h)&1][0][e], 0x3f803f80u, csa);                                                \
    }
#define G1_PIN() asm volatile("" : "+v"(s1a), "+v"(s1b), "+v"(s2a), "+v"(s2b), "+v"(csa), "+v"(csb)); __builtin_amdgcn_sched_barrier(0);
#define G1_IT(h, NW)                                                                                     \
    {                                                                                                    \
        VMWAIT(NW);                                                                                      \
        __builtin_amdgcn_s_barrier();                                                                    \
        G1_M(h, 0, 0)                                                                                    \
        G1_READ((h) + 1, 0) G1_STAT(h, 0) G1_PIN()                                                       \
        G1_M(h, 0, 1)                                                                                    \
        G1_READ((h) + 1, 1) G1_STAT(h, 1) G1_PIN()                                                       \
        G1_M(h, 1, 0)                                                                                    \
        if ((h) >= 0 && (h) + 4 <= 19) G1_W((h) + 4)                                                     \
        G1_STAT(h, 2) G1_PIN()                                                                           \
        G1_M(h, 1, 1)                                                                                    \
        if ((h) >= 1 && ((h)&1) && ((h) - 1) / 2 + 3 <= 9) G1_X(((h) - 1) / 2 + 3)                       \
        G1_STAT(h, 3) G1_PIN()                                                                           \
    }
    G1_IT(-1, 11) G1_IT(0, 6) G1_IT(1, 4) G1_IT(2, 6) G1_IT(3, 4) SG(2) G1_IT(4, 6) G1_IT(5, 4) G1_IT(6, 6) G1_IT(7, 4)
    G1_IT(8, 6) SG(3) G1_IT(9, 4) G1_IT(10, 6) G1_IT(11, 4) G1_IT(12, 6) G1_IT(13, 4) SG(4) G1_IT(14, 6) G1_IT(15, 4)
    G1_IT(16, 4) G1_IT(17, 1) G1_IT(18, 0)
    G1_M(19, 0, 0) G1_M(19, 0, 1) G1_M(19, 1, 0) G1_M(19, 1, 1) G1_STAT(19, 0) G1_STAT(19, 1) G1_STAT(19, 2) G1_STAT(19, 3)
    s1a += s1b; s2a += s2b; csa += csb;
    SG(5)
    asm volatile("" : "+v"(bia[0]), "+v"(bia[1]), "+v"(bia[2]), "+v"(bia[3]));
    char* rsb = smem + 16384;
    char* csl = smem + 16384 + 2048;
    {
        const float a1 = s1a + swap32(s1a), a2 = s2a + swap32(s2a), ac = csa + swap32(csa);
        if (hh == 0) *(f2v*)(rsb + ((64 * wt + 32 * ri + l31) * 2 + (wf & 1)) * 8) = (f2v){a1, a2};
        else *(float*)(csl + ((32 * wf + l31) * 2 + wt) * 4) = ac;
    }
    RAWBAR()
    SG(6)
    if (!(half && wf >= 2)) {
        float csv[16], bsv[16];
#pragma unroll
        for (int g = 0; g < 4; ++g) {
            const f4v c0 = *(const f4v*)(csl + (32 * wf + 8 * g + 4 * hh) * 8), c1 = *(const f4v*)(csl + (32 * wf + 8 * g + 4 * hh) * 8 + 16);
            csv[4 * g] = c0[0] + c0[1]; csv[4 * g + 1] = c0[2] + c0[3]; csv[4 * g + 2] = c1[0] + c1[1]; csv[4 * g + 3] = c1[2] + c1[3];
            bsv[4 * g] = bflo(bia[g][0]) * 1.44269504f; bsv[4 * g + 1] = bfhi(bia[g][0]) * 1.44269504f;
            bsv[4 * g + 2] = bflo(bia[g][1]) * 1.44269504f; bsv[4 * g + 3] = bfhi(bia[g][1]) * 1.44269504f;
        }
        char* actb = p.ws + WS_ACT + ((size_t)(mb * 2 + wt) * KS2 + nt * 4 + wf) * 4096 + (size_t)((l31 >> 4) * 1024 + ((l31 & 15) + 16 * hh) * 16);
#pragma unroll
        for (int i = 0; i < 2; ++i) {
            const f4v u0 = *(const f4v*)(rsb + (64 * wt + 32 * (i ^ ri) + l31) * 16);
            float mean, den;
            ln_stats(u0[0] + u0[2], u0[1] + u0[3], mean, den);
            const float rdl = __builtin_amdgcn_rcpf(den) * 1.44269504f;
#pragma unroll
            for (int c = 0; c < 2; ++c) {
                u4v o;
#pragma unroll
                for (int e = 0; e < 4; ++e) {
                    float r[2];
#pragma unroll
                    for (int z = 0; z < 2; ++z) {
                        const int ix = 8 * c + 2 * e + z;
                        const float u = fmaf(fmaf(-mean, csv[ix], acc[i][ix]), rdl, bsv[ix]);
                        const float e1 = __builtin_amdgcn_exp2f(u) + 1.0f;
                        r[z] = u * fmaf(__builtin_amdgcn_rcpf(fmaf(e1, e1, 1.0f)), -1.38629436f, 0.69314718f);
                    }
                    o[e] = pkbf(r[0], r[1]);
                }
                st16(actb + (i ^ ri) * 2048 + c * 512, o);
            }
        }
    }
    SG(7)
    SGDUMP(8)
}

__device__ __forceinline__ NOPK void ln_role(const Params& p, int rb, char* smem) {
    const int tid = threadIdx.x, lane = tid & 63;
    const int w = __builtin_amdgcn_readfirstlane(tid >> 6);
    const int rl = 4 * w + (lane >> 4), t = rb * 32 + rl, c15 = lane & 15;
    u4v v[5];
#pragma unroll
    for (int i = 0; i < 5; ++i) v[i] = *(const u4v*)(p.x + (size_t)t * D + (c15 + 16 * i) * 8);
    float s = 0.f;
#pragma unroll
    for (int i = 0; i < 5; ++i)
#pragma unroll
        for (int e = 0; e < 4; ++e) s += bflo(v[i][e]) + bfhi(v[i][e]);
    const float mean = red16(s) * (1.0f / D);
    float qv = 0.f;
#pragma unroll
    for (int i = 0; i < 5; ++i)
#pragma unroll
        for (int e = 0; e < 4; ++e) {
            float a = bflo(v[i][e]) - mean, c = bfhi(v[i][e]) - mean;
            qv = fmaf(a, a, qv); qv = fmaf(c, c, qv);
        }
    const float var = red16(qv) * (1.0f / D);
    const float mbf = rbf(mean);
    const float rd = 1.0f / rbf(__builtin_sqrtf(rbf(rbf(var) + 1e-5f)));
#pragma unroll
    for (int i = 0; i < 5; ++i) {
        u2v o0, o1;
        o0[0] = pkh(ln_apply(bflo(v[i][0]), mbf, rd), ln_apply(bfhi(v[i][0]), mbf, rd));
        o0[1] = pkh(ln_apply(bflo(v[i][1]), mbf, rd), ln_apply(bfhi(v[i][1]), mbf, rd));
        o1[0] = pkh(ln_apply(bflo(v[i][2]), mbf, rd), ln_apply(bfhi(v[i][2]), mbf, rd));
        o1[1] = pkh(ln_apply(bflo(v[i][3]), mbf, rd), ln_apply(bfhi(v[i][3]), mbf, rd));
        *(u2v*)(smem + rl * 1288 + (c15 + 16 * i) * 16) = o0;
        *(u2v*)(smem + rl * 1288 + (c15 + 16 * i) * 16 + 8) = o1;
    }
    RAWBAR()
    const int G = lane >> 4, q = c15 >> 2, pp = lane & 3;
    const char* src = smem + (16 * (G >> 1) + 4 * q + 2 * (G & 1)) * 1288 + pp * 8;
    char* dst = p.ws + WS_REC + (size_t)c15 * REC + REC_X + (size_t)rb * 64 + (G ^ ((rb >> 2) & 1)) * 16;
#pragma unroll
    for (int it = 0; it < 5; ++it) {
        const int cg = w * 5 + it;
        u2v lo = tr_read(src + cg * 32), hi = tr_read(src + cg * 32 + 1288);
        st16(dst + (size_t)cg * 16 * REC, (u4v){lo[0], lo[1], hi[0], hi[1]});
    }
}

struct cplx2 { f2v r, i; };
__device__ __forceinline__ NOPK cplx2 cmul(cplx2 a, cplx2 b) {
    cplx2 o;
    o.r = a.r * b.r - a.i * b.i;
    o.i = a.r * b.i + a.i * b.r;
    return o;
}
struct cplxh { h2v r, i; };
__device__ __forceinline__ NOPK cplxh toh(cplx2 a) { return {__builtin_convertvector(a.r, h2v), __builtin_convertvector(a.i, h2v)}; }
__device__ __forceinline__ NOPK cplx2 cexp2(f2v zr, f2v zi, float c) {
    cplx2 o;
#pragma unroll
    for (int m = 0; m < 2; ++m) {
        float mag = __builtin_amdgcn_exp2f(zr[m] * c * 1.44269504089f);
        float ph = zi[m] * c;
        const float CH = 0.15915494f, CL = 6.4206382e-9f;
        float hi = ph * CH;
        float lo = fmaf(ph, CH, -hi) + ph * CL;
        float fr = __builtin_amdgcn_fractf(hi) + lo;
        o.r[m] = mag * __builtin_amdgcn_cosf(fr);
        o.i[m] = mag * __builtin_amdgcn_sinf(fr);
    }
    return o;
}
static constexpr int KG_ROW = 544;

__device__ __forceinline__ NOPK void kgen_role(const Params& p, int d, int part, char* sw  ) {
    const int lane = threadIdx.x & 63, l15 = lane & 15, q4 = lane >> 4;
    const int o = d * NS + 2 * lane;
    u32 are = *(const u32*)(p.Are + o), aim = *(const u32*)(p.Aim + o), bre = *(const u32*)(p.Bre + o);
    u32 bim = *(const u32*)(p.Bim + o), cre = *(const u32*)(p.Cre + o), cim = *(const u32*)(p.Cim + o);
    float dt = rbf(expf(bf2f(p.logdt[d])));
    f2v zr = {dt * bflo(are), dt * bfhi(are)}, zi = {dt * bflo(aim), dt * bfhi(aim)};
    cplx2 dB = {{dt * bflo(bre), dt * bfhi(bre)}, {dt * bflo(bim), dt * bfhi(bim)}};
    cplx2 C = {{bflo(cre), bfhi(cre)}, {bflo(cim), bfhi(cim)}};
    cplx2 W = cmul(C, dB);
    float wm = fmaxf(fmaxf(fabsf(W.r[0]), fabsf(W.r[1])), fmaxf(fabsf(W.i[0]), fabsf(W.i[1])));
    const float dsk = bf2f(p.Dskip[d]);
    wm = fmaxf(max64(wm), fabsf(dsk) * (1.0f / 128.0f));
    int eU = 14 - __builtin_amdgcn_frexp_expf(wm);
    eU = min(max(eU, -100), 100);
    const float sU = __builtin_amdgcn_ldexpf(1.0f, eU);
    W.r *= sU; W.i *= sU;
    char* rec = p.ws + WS_REC + (size_t)d * REC;
    if (part == 0 && lane < 34) {
        u4v z = {0u, 0u, 0u, 0u};
        if (lane == 0) z[0] = f2u(__builtin_amdgcn_ldexpf(1.0f, 8 - eU));
        *(u4v*)(rec + (lane < 2 ? lane * 16 : 4160 + (lane - 2) * 16)) = z;
    }
    const float KSC = 2.384185791015625e-07f;

    char* r0 = sw;
    char* kc = sw + 16 * KG_ROW;
#define KG_PUT(row, v) *(u2v*)(r0 + (row)*KG_ROW + lane * 8) = (u2v){pkh((v).r[0], (v).r[1]), pkh((v).i[0], (v).i[1])}
#define KG_PUTH(row, T_, E_)                                                                            \
    *(u2v*)(r0 + (row)*KG_ROW + lane * 8) = (u2v){__builtin_bit_cast(u32, __builtin_elementwise_fma(T_.r, E_.r, -(T_.i * E_.i))), \
                                                  __builtin_bit_cast(u32, __builtin_elementwise_fma(T_.r, E_.i, T_.i * E_.r))}
#define KG_MMA(A)                                         \
    A = (f4v){0.f, 0.f, 0.f, 0.f};                        \
    _Pragma("unroll") for (int ks = 0; ks < 8; ++ks) A = mfma_h(*(const u4v*)(r0 + l15 * KG_ROW + ks * 64 + q4 * 16), vf[ks], A);
    const cplx2 G = cexp2(zr, zi, 128.f);
    u16* kch = (u16*)kc;
    char* kx = rec + REC_K;
    u4v vf[8];
    if (part == 0) {
        {
            cplx2 v1 = cexp2(zr, zi, 8.f);
            cplx2 v = {{16384.f, 16384.f}, {0.f, 0.f}};
#pragma unroll
            for (int jj = 0; jj < 16; ++jj) {
                cplx2 t = {v.r, -v.i};
                KG_PUT(jj, t);
                if (jj < 15) v = cmul(v, v1);
            }
        }
        RAWBAR()
#pragma unroll
        for (int ks = 0; ks < 8; ++ks) vf[ks] = *(const u4v*)(r0 + l15 * KG_ROW + ks * 64 + q4 * 16);
        cplx2 E[8];
        E[1] = cexp2(zr, zi, 1.f);
        E[2] = cmul(E[1], E[1]); E[3] = cmul(E[2], E[1]); E[4] = cmul(E[2], E[2]);
        E[5] = cmul(E[4], E[1]); E[6] = cmul(E[4], E[2]); E[7] = cmul(E[4], E[3]);
        const cplxh T0 = toh(W), T1 = toh(cmul(W, G));
        RAWBAR()
        *(u2v*)(r0 + lane * 8) = (u2v){__builtin_bit_cast(u32, T0.r), __builtin_bit_cast(u32, T0.i)};
        *(u2v*)(r0 + 8 * KG_ROW + lane * 8) = (u2v){__builtin_bit_cast(u32, T1.r), __builtin_bit_cast(u32, T1.i)};
#pragma unroll
        for (int c = 1; c < 8; ++c) {
            const cplxh Eh = toh(E[c]);
            KG_PUTH(c, T0, Eh);
            KG_PUTH(8 + c, T1, Eh);
        }
        f4v acc;
        KG_MMA(acc)
        if (lane == 0) acc[0] = fmaf(dsk * sU, 16384.f, acc[0]);
        *(u2v*)(kx + (128 * (q4 >> 1) + 4 * (q4 & 1) + 8 * l15) * 2) = (u2v){pkh(acc[0] * KSC, acc[1] * KSC), pkh(acc[2] * KSC, acc[3] * KSC)};
    } else {
        const cplx2 E2 = cexp2(zr, zi, 2.f);
        const cplx2 E4 = cmul(E2, E2);
        const cplxh E2h = toh(E2), E4h = toh(E4), E6h = toh(cmul(E4, E2));
        cplx2 T = cmul(W, cexp2(zr, zi, 256.f));
#pragma unroll
        for (int a = 0; a < 6; ++a) {
            const cplxh Th = toh(T);
            const int row = a < 2 ? 4 * a : 4 + 2 * a;
            *(u2v*)(r0 + row * KG_ROW + lane * 8) = (u2v){__builtin_bit_cast(u32, Th.r), __builtin_bit_cast(u32, Th.i)};
            if (a < 2) { KG_PUTH(row + 1, Th, E2h); KG_PUTH(row + 2, Th, E4h); KG_PUTH(row + 3, Th, E6h); }
            else KG_PUTH(row + 1, Th, E4h);
            if (a < 5) T = cmul(T, G);
        }
        RAWBAR()
#pragma unroll
        for (int ks = 0; ks < 8; ++ks) vf[ks] = *(const u4v*)(r0 - 10240 + l15 * KG_ROW + ks * 64 + q4 * 16);
        RAWBAR()
        f4v acc1, acc2;
        KG_MMA(acc1)
        T = cmul(W, cexp2(zr, zi, 1024.f));
#pragma unroll
        for (int a = 0; a < 9; ++a) {
            KG_PUT(a, T);
            if (a < 8) T = cmul(T, G);
        }
        KG_MMA(acc2)
        {
            u32 h01 = pkh(acc1[0] * KSC, acc1[1] * KSC), h23 = pkh(acc1[2] * KSC, acc1[3] * KSC);
            if (q4 < 2) {
                *(u2v*)(kc + (64 * q4 + 4 * l15) * 2) = (u2v){h01, h23};
            } else {
                *(u32*)(kc + (128 + 64 * (q4 - 2) + 2 * l15) * 2) = h01;
                *(u32*)(kc + (128 + 64 * (q4 - 2) + 32 + 2 * l15) * 2) = h23;
            }
            u32 g01 = pkh(acc2[0] * KSC, acc2[1] * KSC), g23 = pkh(acc2[2] * KSC, acc2[3] * KSC);
            if (q4 < 2) {
                kch[256 + 64 * q4 + l15] = (u16)g01;
                kch[256 + 64 * q4 + 16 + l15] = (u16)(g01 >> 16);
                kch[256 + 64 * q4 + 32 + l15] = (u16)g23;
                kch[256 + 64 * q4 + 48 + l15] = (u16)(g23 >> 16);
            } else if (lane == 32) {
                kch[384] = (u16)g01;
            }
        }
        if (lane < 32) {
            u2v s = *(const u2v*)(kc + lane * 8);
            u32 s4 = kch[lane * 4 + 4];
            u4v b2;
            b2[0] = (s[0] & 0xffffu) | (s[0] << 16);
            b2[1] = (s[0] >> 16) | (s[1] << 16);
            b2[2] = (s[1] & 0xffffu) | (s[1] << 16);
            b2[3] = (s[1] >> 16) | (s4 << 16);
            st16(kx + 512 + lane * 16, b2);
        }
        {
            u32 s = *(const u32*)(kc + 256 + lane * 4);
            u32 c = kch[128 + lane * 2 + 2];
            u4v v;
            v[0] = (s & 0xffffu) | (s << 16);
            v[1] = s;
            v[2] = (s >> 16) | (s & 0xffff0000u);
            v[3] = c | (c << 16);
            st16(kx + 1024 + lane * 16, v);
        }
#pragma unroll
        for (int it = 0; it < 2; ++it) {
            int oc = lane + 64 * it;
            u32 a = kch[256 + oc], c = kch[257 + oc];
            u4v v;
            v[0] = a | (a << 16);
            v[1] = v[0];
            v[2] = ((oc & 1) ? c : a) | (c << 16);
            v[3] = c | (c << 16);
            st16(kx + 2048 + oc * 16, v);
        }
    }
}

__device__ __forceinline__ NOPK void w2pack_role(const Params& p, int unit, char* sw) {
    const int lane = threadIdx.x & 63, l15 = lane & 15, q4 = lane >> 4;
    const int nb = unit / KS2, ks = unit - nb * KS2;
#pragma unroll
    for (int i = 0; i < 5; ++i) {
        int c = lane + 64 * i, row = c / 10, ch = c - row * 10;
        *(u4v*)(sw + c * 16) = *(const u4v*)(p.w2 + (size_t)(ks * 32 + row) * D + nb * 80 + ch * 8);
    }
    const int q = l15 >> 2, pp = lane & 3;
    char* dst = p.ws + WS_W2P + (size_t)unit * 5120 + lane * 16;
    const char* src = sw + (16 * (q4 >> 1) + 4 * (q4 & 1) + q) * 160 + pp * 8;
#pragma unroll
    for (int dt = 0; dt < 5; ++dt) {
        u2v lo = tr_read(src + dt * 32);
        u2v hi = tr_read(src + 8 * 160 + dt * 32);
        st16(dst + dt * 1024, (u4v){lo[0], lo[1], hi[0], hi[1]});
    }
}

__global__ __launch_bounds__(512, 4) NOPK void k1(Params p) {
    extern __shared__ __attribute__((aligned(16))) char smem[];
    const int b = blockIdx.x;
    const int w = __builtin_amdgcn_readfirstlane(threadIdx.x >> 6);
    TLB()
    if (b < 224) {
        gemm1_role(p, b, smem);
    } else if (b < 438) {
        const int d = (b - 224) * 3 + (w >> 1);
        if (w < 6 && d < D) {
            kgen_role(p, d, w & 1, smem + w * 10240);
        } else {
            if (w >= 6) {
                const int unit = (b - 224) * 2 + (w - 6);
                w2pack_role(p, unit, smem + w * 10240);
                if (unit < 4) w2pack_role(p, 428 + unit, smem + w * 10240);
                if (b == 437 && (threadIdx.x & 63) < 8 && w == 7) *(u32*)(p.ws + WS_CNT + (threadIdx.x & 63) * 256) = 0u;
            }
            __builtin_amdgcn_s_barrier();
            __builtin_amdgcn_s_barrier();
        }
    } else {
        ln_role(p, b - 438, smem);
    }
    TLE(0)
}

static constexpr int CV_WAVE = 9216;
static constexpr int K2_RED = 3 * CV_WAVE;
static constexpr int K2_YS = K2_RED + 61440;
static constexpr int K2_LDS = K2_YS + 10240;

#define AC0 "a"
#define AC1 "a"
#define AC2 "a"
#define AC3 "v"
#define AC4 "v"
#define AC5 "v"
#define G2_BUF(C, i) C AC##i(af[i][0]), C AC##i(af[i][1]), C AC##i(af[i][2]), C AC##i(af[i][3]), C "a"(wf[i][0]), C "a"(wf[i][1]), C "a"(wf[i][2]), C "a"(wf[i][3]), C "a"(wf[i][4])
#define G2_LOAD(i)                                                                                                   \
    asm volatile("buffer_load_dwordx4 %0, %9, %15, %17 offen\n\tbuffer_load_dwordx4 %4, %13, %16, %18 offen\n\t"     \
                 "buffer_load_dwordx4 %1, %10, %15, %17 offen\n\tbuffer_load_dwordx4 %2, %11, %15, %17 offen\n\t"    \
                 "buffer_load_dwordx4 %3, %12, %15, %17 offen\n\tbuffer_load_dwordx4 %5, %13, %16, %18 offen offset:1024\n\t" \
                 "buffer_load_dwordx4 %6, %13, %16, %18 offen offset:2048\n\tbuffer_load_dwordx4 %7, %13, %16, %18 offen offset:3072\n\t" \
                 "buffer_load_dwordx4 %8, %14, %16, %18 offen"                                                       \
                 : G2_BUF("=&", i) : "v"(ao[0]), "v"(ao[1]), "v"(ao[2]), "v"(ao[3]), "v"(ao[4]), "v"(ao[5]), "s"(ra), "s"(rw), \
                   "s"((i)*4096), "s"((i)*5120) : "memory");
#define G2_MF(d, w_, a_) "v_mfma_f32_16x16x32_bf16 %" #d ", %" #w_ ", %" #a_ ", %" #d "\n\t"
#define G2_M4(dt, i, VO, OFF)                                                                           \
    asm volatile(G2_MF(0, 4, 5) G2_MF(1, 4, 6) G2_MF(2, 4, 7) G2_MF(3, 4, 8)                            \
                 "buffer_load_dwordx4 %4, %9, %10, %11 offen offset:" #OFF                              \
                 : "+a"(acc2[dt][0]), "+a"(acc2[dt][1]), "+a"(acc2[dt][2]), "+a"(acc2[dt][3]), "+a"(wf[i][dt]) \
                 : AC##i(af[i][0]), AC##i(af[i][1]), AC##i(af[i][2]), AC##i(af[i][3]), "v"(ao[VO]), "s"(rw), "s"(sw) : "memory");
#define G2_BODY(i)                                                                                      \
    {                                                                                                   \
        G2_M4(0, i, 4, 0) G2_M4(1, i, 4, 1024) G2_M4(2, i, 4, 2048) G2_M4(3, i, 4, 3072) G2_M4(4, i, 5, 0) \
        asm volatile("buffer_load_dwordx4 %0, %4, %8, %9 offen\n\tbuffer_load_dwordx4 %1, %5, %8, %9 offen\n\t" \
                     "buffer_load_dwordx4 %2, %6, %8, %9 offen\n\tbuffer_load_dwordx4 %3, %7, %8, %9 offen" \
                     : "+" AC##i(af[i][0]), "+" AC##i(af[i][1]), "+" AC##i(af[i][2]), "+" AC##i(af[i][3])                   \
                     : "v"(ao[0]), "v"(ao[1]), "v"(ao[2]), "v"(ao[3]), "s"(ra), "s"(sa) : "memory");    \
    }
#define G2_STEP(i) { asm volatile("s_waitcnt vmcnt(45)" : G2_BUF("+", i)::"memory"); G2_BODY(i) sa += 4096; sw += 5120; }
#define G2_M4N(dt, i)                                                                                   \
    asm volatile(G2_MF(0, 4, 5) G2_MF(1, 4, 6) G2_MF(2, 4, 7) G2_MF(3, 4, 8) ""                         \
                 : "+a"(acc2[dt][0]), "+a"(acc2[dt][1]), "+a"(acc2[dt][2]), "+a"(acc2[dt][3])           \
                 : "a"(wf[i][dt]), AC##i(af[i][0]), AC##i(af[i][1]), AC##i(af[i][2]), AC##i(af[i][3]));
#define G2_SSTEP(k, i)                                                                                  \
    {                                                                                                   \
        if constexpr ((k) < 2) {                                                                        \
            const u32 sa = ((k) + 6) * 4096, sw = ((k) + 6) * 5120;                                     \
            G2_BODY(i)                                                                                  \
        } else {                                                                                        \
            G2_M4N(0, i) G2_M4N(1, i) G2_M4N(2, i) G2_M4N(3, i) G2_M4N(4, i)                            \
        }                                                                                               \
        __builtin_amdgcn_sched_barrier(0);                                                              \
    }
#define G2_ACC(a0) "+a"(acc2[a0][0]), "+a"(acc2[a0][1]), "+a"(acc2[a0][2]), "+a"(acc2[a0][3])
#define G2_ZERO()                                                                                       \
    _Pragma("unroll") for (int dt = 0; dt < 5; ++dt) _Pragma("unroll") for (int tt = 0; tt < 4; ++tt) acc2[dt][tt] = (f4v){0.f, 0.f, 0.f, 0.f}; \
    asm volatile("s_nop 1" : G2_ACC(0), G2_ACC(1), G2_ACC(2), G2_ACC(3), G2_ACC(4));
#undef STOFF
#define STOFF (4 << 20)
__device__ __forceinline__ NOPK void conv_wave(const Params& p, int d, u32 lb, char* img, u4v (&af)[6][4], u4v (&wf)[6][5],
                                                f4v (&acc2)[5][4], u4v ra, u4v rw, const u32 (&ao)[6], u32 svc, const u32* cnt,
                                                const char* yb, u32 ysl) {
    const int lane = threadIdx.x & 63, l15 = lane & 15, q4 = lane >> 4;
    const int q = l15 >> 2, pp = lane & 3;
    const u32 kra = lb + REC_K + (32 * q4 - 4 * q + 4 * pp) * 2;
    u32 xra[2][2][2];
#pragma unroll
    for (int ap = 0; ap < 2; ++ap)
#pragma unroll
        for (int h = 0; h < 2; ++h)
#pragma unroll
            for (int g = 0; g < 2; ++g) {
                int G = l15 + 8 * g - 2 * q4 - h + 8;
                xra[ap][h][g] = lb + 4160 + G * 32 + ((ap ^ ((G >> 3) & 1)) * 16);
            }
    const u32 csrc = (lane >= 16 ? lane - 16 : (lane == 0 ? 63 : lane + 47)) * 4;
    asm volatile("s_waitcnt vmcnt(0)" ::: "memory");
    u4v kf[16], xf[4];
    f4v acc[8];
    float Y[8][7];
    float scale;
    asm volatile("ds_read_b32 %0, %1" : "=v"(scale) : "v"(lb));
#define CV_XRD(s)                                                                                          \
    {                                                                                                      \
        u2v v0, v1;                                                                                        \
        asm volatile("ds_read_b64 %0, %2 offset:%4\n\tds_read_b64 %1, %3 offset:%4" : "=&v"(v0), "=&v"(v1) \
                     : "v"(xra[(s) >> 5][0][(s)&1]), "v"(xra[(s) >> 5][1][(s)&1]), "i"((((s)&15) >> 1) * 512 + (((s) >> 4) & 1) * 8)); \
        xf[(s)&3] = (u4v){v0[0], v0[1], v1[0], v1[1]};                                                     \
    }
    CV_XRD(0) CV_XRD(1) CV_XRD(2)
#pragma unroll
    for (int e = 0; e < 16; ++e) {
        u2v lo, hi;
        asm volatile("ds_read_b64_tr_b16 %0, %2 offset:%3\n\tds_read_b64_tr_b16 %1, %2 offset:%4" : "=&v"(lo), "=&v"(hi)
                     : "v"(kra), "i"(e * 256), "i"(e * 256 + 32));
        kf[e] = (u4v){lo[0], lo[1], hi[0], hi[1]};
    }
    asm volatile("s_waitcnt lgkmcnt(0)" : "+v"(kf[0]), "+v"(kf[1]), "+v"(kf[2]), "+v"(kf[3]), "+v"(kf[4]), "+v"(kf[5]), "+v"(kf[6]), "+v"(kf[7]),
                 "+v"(kf[8]), "+v"(kf[9]), "+v"(kf[10]), "+v"(kf[11]), "+v"(kf[12]), "+v"(kf[13]), "+v"(kf[14]), "+v"(kf[15]), "+v"(scale));
#define CV_STEP(s)                                                                                         \
    {                                                                                                      \
        constexpr int a = (s) >> 4, g = (s)&15;                                                            \
        if constexpr ((s) + 3 < 64) CV_XRD(((s) + 3) & 63)                                                 \
          \
        _Pragma("unroll") for (int e = (g + 1) & 1; e + g <= 15; e += 2) {                                 \
            if constexpr (g == 0) {                                                                        \
                if (e < 2) asm volatile("s_waitcnt lgkmcnt(6)\n\tv_mfma_f32_16x16x32_f16 %0, %1, %2, 0" : "=&v"(acc[(e + g) >> 1]) : "v"(kf[e]), "v"(xf[(s)&3])); \
                else asm volatile("v_mfma_f32_16x16x32_f16 %0, %1, %2, 0" : "=&v"(acc[(e + g) >> 1]) : "v"(kf[e]), "v"(xf[(s)&3])); \
            } else {                                                                                       \
                if (e < 2) asm volatile("s_waitcnt lgkmcnt(%3)\n\tv_mfma_f32_16x16x32_f16 %0, %1, %2, %0" : "+v"(acc[(e + g) >> 1]) : "v"(kf[e]), "v"(xf[(s)&3]), "i"((s) + 3 < 64 ? 6 : 2 * (63 - (s)))); \
                else asm volatile("v_mfma_f32_16x16x32_f16 %0, %1, %2, %0" : "+v"(acc[(e + g) >> 1]) : "v"(kf[e]), "v"(xf[(s)&3])); \
            }                                                                                              \
        }                                                                                                  \
        if constexpr (g == 15) {      \
            asm volatile("s_nop 7\n\ts_nop 3" : "+v"(acc[0]), "+v"(acc[1]), "+v"(acc[2]), "+v"(acc[3]), "+v"(acc[4]), "+v"(acc[5]), "+v"(acc[6]), "+v"(acc[7])); \
            _Pragma("unroll") for (int t = 0; t < 8; ++t) _Pragma("unroll") for (int r = 0; r < 4; ++r)    \
                Y[t][r + a] = (a == 0 || r == 3) ? acc[t][r] : Y[t][r + a] + acc[t][r];                    \
            __builtin_amdgcn_sched_barrier(0);                                                             \
        }                                                                                                  \
    }
#define CV_S4(s) CV_STEP(s) CV_STEP((s) + 1) CV_STEP((s) + 2) CV_STEP((s) + 3)
#define CV_S16(s) CV_S4(s) CV_S4((s) + 4) CV_S4((s) + 8) CV_S4((s) + 12)
#define CV_S8(s) CV_S4(s) CV_S4((s) + 4)
    CV_S16(0) G2_LOAD(0) CV_S8(16) G2_LOAD(1) CV_S8(24) G2_LOAD(2) CV_S8(32) G2_LOAD(3) CV_S8(40) G2_LOAD(4) CV_S8(48) G2_LOAD(5) CV_S8(56)
    float cr[8][3];
#pragma unroll
    for (int t = 0; t < 8; ++t)
#pragma unroll
        for (int r = 0; r < 3; ++r)
            cr[t][r] = __builtin_bit_cast(float, __builtin_amdgcn_ds_bpermute(csrc, __builtin_bit_cast(int, Y[t][4 + r])));
    __builtin_amdgcn_sched_barrier(0);
#pragma unroll
    for (int t = 0; t < 8; ++t) {
        float y[4];
#pragma unroll
        for (int r = 0; r < 3; ++r) y[r] = Y[t][r] + (lane == 0 ? (t ? cr[t - (t > 0)][r] : 0.f) : cr[t][r]);
        y[3] = Y[t][3];
        *(u2v*)(img + t * 512 + (16 * l15 + 4 * q4) * 2) = (u2v){pkbf(y[0] * scale, y[1] * scale), pkbf(y[2] * scale, y[3] * scale)};
    }
    char* dst = p.ws + WS_YT + (size_t)d * 4096 + lane * 16;
    {
        u4v o[4];
#pragma unroll
        for (int i = 0; i < 4; ++i) o[i] = *(const u4v*)(img + i * 1024 + lane * 16);
#pragma unroll
        for (int i = 0; i < 4; ++i)
            asm volatile("global_store_dwordx4 %0, %1, off offset:%2 sc1\n\ts_nop 1" ::"v"(dst), "v"(o[i]), "i"(i * 1024) : "memory");
    }
    G2_ZERO()
    asm volatile("s_waitcnt vmcnt(0)\n\ts_mov_b64 exec, 1\n\tglobal_atomic_add %0, %1, %2\n\ts_mov_b64 exec, -1"
                 ::"v"(0u), "v"(1u), "s"(p.ws + WS_CNT + (d / 80) * 256) : "memory");
    asm volatile("" : G2_BUF("+", 0), G2_BUF("+", 1), G2_BUF("+", 2)::"memory");
    asm volatile("" : G2_BUF("+", 3), G2_BUF("+", 4), G2_BUF("+", 5)::"memory");
    G2_SSTEP(0, 0) G2_SSTEP(1, 1) G2_SSTEP(2, 2) G2_SSTEP(3, 3)
    u32 cv = 0;
    asm volatile("s_cmp_lg_u32 %1, 0\n\ts_cbranch_scc1 .Lsv%=\n\tglobal_load_dword %0, %0, %2 sc1\n.Lsv%=:" : "+v"(cv) : "s"(svc), "s"(cnt) : "memory", "scc");
    G2_SSTEP(4, 4) G2_SSTEP(5, 5)
    asm volatile("s_waitcnt vmcnt(0)" : "+v"(cv), G2_BUF("+", 0), G2_BUF("+", 1)::"memory");
    if (svc == 0) {
        unsigned spins = 0;
        while (__builtin_amdgcn_readfirstlane(cv) < 80u && ++spins < (1u << 22)) {
            __builtin_amdgcn_s_sleep(4);
            cv = __hip_atomic_load(cnt, __ATOMIC_RELAXED, __HIP_MEMORY_SCOPE_AGENT);
        }
        const u32 yo = (lane >> 3) * 4096 + (lane & 7) * 16;
#pragma unroll
        for (int i = 0; i < 10; ++i)
            asm volatile("s_mov_b32 m0, %0\n\ts_nop 0\n\tglobal_load_lds_dwordx4 %1, %2 sc1" ::"s"(ysl + i * 1024), "v"(yo), "s"(yb + i * 32768) : "memory");
    }
    G2_SSTEP(6, 0) G2_SSTEP(7, 1)
    asm volatile("s_waitcnt vmcnt(0)\n\ts_nop 7\n\ts_nop 7" : G2_ACC(0), G2_ACC(1), G2_ACC(2), G2_ACC(3), G2_ACC(4)::"memory");
}

__global__ __launch_bounds__(256, 1) NOPK void k2(Params p) {
    extern __shared__ __attribute__((aligned(16))) char smem[];
    const int b = blockIdx.x;
    const int w = __builtin_amdgcn_readfirstlane(threadIdx.x >> 6);
    TLB()
    const int xcd = b & 7, j = b >> 3;
    const int mb = 8 * (xcd >> 1) + (j & 7), nb = 4 * (xcd & 1) + (j >> 3);
    const int nch = (j & 1) ? 2 : 3;
    const int ch0 = xcd * 80 + (j >> 1) * 5 + ((j & 1) ? 3 : 0);
    const int k0 = ((j & 1) && w == 3) ? 34 : 8 * w;
    const int nk = w < nch ? 8 : ((j & 1) ? 2 * w + 14 : 30);
    const char* ab = p.ws + WS_ACT + ((size_t)mb * KS2 + k0) * 4096;
    const char* wb = p.ws + WS_W2P + ((size_t)nb * KS2 + k0) * 5120;
    u4v ra = {(u32)(size_t)ab, (u32)((size_t)ab >> 32), (u32)nk * 4096u, 0x00020000u};
    u4v rw = {(u32)(size_t)wb, (u32)((size_t)wb >> 32), (u32)nk * 5120u, 0x00020000u};
    const u32 lo16 = (threadIdx.x & 63) * 16;
    const u32 lb = lds_addr(smem);
    const u32 ao[6] = {lo16 + (w & 3) * 1024, lo16 + ((w + 1) & 3) * 1024, lo16 + ((w + 2) & 3) * 1024, lo16 + ((w + 3) & 3) * 1024, lo16, lo16 + 4096};
    SGDECL SG(0)
    if (w < nch) {
        const char* src = p.ws + WS_REC + (size_t)(ch0 + w) * REC;
#pragma unroll
        for (int i = 0; i < 9; ++i) GLDS16(lb + w * CV_WAVE + i * 1024, lo16, src + i * 1024);
    }
    u4v af[6][4], wf[6][5];
    SG(1)
    f4v acc2[5][4];
    if (w < nch) {
        conv_wave(p, ch0 + w, lb + w * CV_WAVE, smem + w * CV_WAVE, af, wf, acc2, ra, rw, ao, (u32)w,
                  (const u32*)(p.ws + WS_CNT + nb * 256), p.ws + WS_YT + (size_t)(nb * 80) * 4096 + (size_t)mb * 128, lb + K2_YS);
    } else {
        G2_LOAD(0) G2_LOAD(1) G2_LOAD(2) G2_LOAD(3) G2_LOAD(4) G2_LOAD(5)
        G2_ZERO()
        u32 sa = 6 * 4096, sw = 6 * 5120;
#pragma nounroll
        for (int r = 0; r < nk; r += 6) { G2_STEP(0) G2_STEP(1) G2_STEP(2) G2_STEP(3) G2_STEP(4) G2_STEP(5) }
        asm volatile("s_waitcnt vmcnt(0)" : G2_BUF("+", 0), G2_BUF("+", 1), G2_BUF("+", 2)::"memory");
        asm volatile("" : G2_BUF("+", 3), G2_BUF("+", 4), G2_BUF("+", 5)::"memory");
        asm volatile("s_nop 7\n\ts_nop 7" : G2_ACC(0), G2_ACC(1), G2_ACC(2), G2_ACC(3), G2_ACC(4)::"memory");
    }
    SG(2)
    SG(3)
    int lane = threadIdx.x & 63;
    asm volatile("" : "+v"(lane));
    const int l15 = lane & 15, q4 = lane >> 4;
    const int t = mb * 64 + w * 16 + l15;
    const int dbase = nb * 80 + 4 * q4;
    u2v xv[5];
#pragma unroll
    for (int dt = 0; dt < 5; ++dt) xv[dt] = *(const u2v*)(p.x + (size_t)t * D + dbase + dt * 16);
    char* red = smem + K2_RED;
#pragma unroll
    for (int tt = 1; tt < 4; ++tt) {
        char* dst = red + (((tt + w) & 3) * 3 + tt - 1) * 5120 + lane * 16;
#pragma unroll
        for (int dt = 0; dt < 5; ++dt) *(f4v*)(dst + dt * 1024) = acc2[dt][tt];
    }
    RAWBAR()
    SG(4)
    const char* ys = smem + K2_YS + (4 * q4 + (l15 >> 2)) * 128 + w * 32 + (lane & 3) * 8;
    const char* rsrc = red + w * 15360 + lane * 16;
    u2v svv[5];
    f4v part[5][3];
#pragma unroll
    for (int dt = 0; dt < 5; ++dt) {
        svv[dt] = tr_read(ys + dt * 2048);
#pragma unroll
        for (int i = 0; i < 3; ++i) part[dt][i] = *(const f4v*)(rsrc + i * 5120 + dt * 1024);
    }
    __builtin_amdgcn_sched_barrier(0);
#pragma unroll
    for (int dt = 0; dt < 5; ++dt) {
        const u2v sv = svv[dt];
        const f4v g = (acc2[dt][0] + part[dt][0]) + (part[dt][1] + part[dt][2]);
        u2v o;
#pragma unroll
        for (int r2 = 0; r2 < 2; ++r2) {
            const float p0 = g[2 * r2], p1 = g[2 * r2 + 1];
            const float g0 = p0 * __builtin_amdgcn_rcpf(1.0f + __builtin_amdgcn_exp2f(-1.44269504f * p0));
            const float g1 = p1 * __builtin_amdgcn_rcpf(1.0f + __builtin_amdgcn_exp2f(-1.44269504f * p1));
            o[r2] = pkbf(fmaf(bflo(sv[r2]), g0, bflo(xv[dt][r2])), fmaf(bfhi(sv[r2]), g1, bfhi(xv[dt][r2])));
        }
        *(u2v*)(p.out + (size_t)t * D + dbase + dt * 16) = o;
    }
    SG(5) SGDUMP(6)
    TLE(1 << 20)
}

extern "C" void kernel_launch(void* const* d_in, const int* in_sizes, int n_in, void* d_out, int out_size, void* d_ws,
                              size_t ws_size, hipStream_t stream) {
    Params p;
    p.x = (const u16*)d_in[0];
    p.Are = (const u16*)d_in[1]; p.Aim = (const u16*)d_in[2]; p.Bre = (const u16*)d_in[3]; p.Bim = (const u16*)d_in[4];
    p.Cre = (const u16*)d_in[5]; p.Cim = (const u16*)d_in[6]; p.logdt = (const u16*)d_in[7]; p.Dskip = (const u16*)d_in[8];
    p.w1 = (const u16*)d_in[10]; p.b1 = (const u16*)d_in[11]; p.w2 = (const u16*)d_in[12];
    p.out = (u16*)d_out;
    p.ws = (char*)d_ws;
    (void)hipFuncSetAttribute((const void*)k1, hipFuncAttributeMaxDynamicSharedMemorySize, LDS_BYTES);
    (void)hipFuncSetAttribute((const void*)k2, hipFuncAttributeMaxDynamicSharedMemorySize, K2_LDS);
    k1<<<502, 512, LDS_BYTES, stream>>>(p);
    k2<<<256, 256, K2_LDS, stream>>>(p);
}
```

```cpp
#include <hip/hip_runtime.h>
#include <stdint.h>

#if defined(__HIP_DEVICE_COMPILE__) && !defined(ALLOWPK)
#define NOPK __attribute__((target("no-packed-fp32-ops")))
#else
#define NOPK
#endif
typedef unsigned short u16;
typedef unsigned int u32;
typedef short s4v __attribute__((ext_vector_type(4)));
typedef u32 u2v __attribute__((ext_vector_type(2)));
typedef u32 u4v __attribute__((ext_vector_type(4)));
typedef float f2v __attribute__((ext_vector_type(2)));
typedef float f4v __attribute__((ext_vector_type(4)));
typedef _Float16 h2v __attribute__((ext_vector_type(2)));
typedef _Float16 h8v __attribute__((ext_vector_type(8)));
typedef __bf16 b2v __attribute__((ext_vector_type(2)));
typedef __bf16 b8v __attribute__((ext_vector_type(8)));
#define LDSP(T, p) ((__attribute__((address_space(3))) T*)(p))
#define GLB(T, p) ((__attribute__((address_space(1))) T*)(p))

static constexpr int L = 2048, D = 640, NS = 128, F = 1728;
static constexpr int KS2 = F / 32;

static constexpr size_t WS_ACT = 0;
static constexpr size_t WS_W2P = WS_ACT + (size_t)L * F * 2;
static constexpr int REC_K = 32, REC_X = 4160 + 512, REC = 8768;
static constexpr size_t WS_REC = WS_W2P + (size_t)F * D * 2;
static constexpr size_t WS_YT = WS_REC + (size_t)641 * REC + 192;
static constexpr size_t WS_CNT = WS_YT + (size_t)L * D * 2;
static_assert(WS_REC % 256 == 0 && WS_YT % 256 == 0, "alignment");

static constexpr int LDS_BYTES = 80 * 1024;

__device__ __forceinline__ NOPK u32 f2u(float v) { return __builtin_bit_cast(u32, v); }
__device__ __forceinline__ NOPK float bf2f(u32 v) { return __builtin_bit_cast(float, (u32)(v << 16)); }
__device__ __forceinline__ NOPK float bflo(u32 v) { return __builtin_bit_cast(float, (u32)(v << 16)); }
__device__ __forceinline__ NOPK float bfhi(u32 v) { return __builtin_bit_cast(float, (u32)(v & 0xffff0000u)); }
__device__ __forceinline__ NOPK u32 pkbf(float a, float b) {
    f2v v = {a, b};
    return __builtin_bit_cast(u32, __builtin_convertvector(v, b2v));
}
__device__ __forceinline__ NOPK u32 pkh(float a, float b) {
    f2v v = {a, b};
    return __builtin_bit_cast(u32, __builtin_convertvector(v, h2v));
}
__device__ __forceinline__ NOPK float rbf(float a) { return bflo(pkbf(a, a)); }
__device__ __forceinline__ NOPK f4v mfma_bf(u4v a, u4v b, f4v c) {
    return __builtin_amdgcn_mfma_f32_16x16x32_bf16(__builtin_bit_cast(b8v, a), __builtin_bit_cast(b8v, b), c, 0, 0, 0);
}
__device__ __forceinline__ NOPK f4v mfma_h(u4v a, u4v b, f4v c) {
    return __builtin_amdgcn_mfma_f32_16x16x32_f16(__builtin_bit_cast(h8v, a), __builtin_bit_cast(h8v, b), c, 0, 0, 0);
}
__device__ __forceinline__ NOPK u2v tr_read(const char* p) {
    return __builtin_bit_cast(u2v, __builtin_amdgcn_ds_read_tr16_b64_v4i16(LDSP(s4v, p)));
}
__device__ __forceinline__ NOPK float dot2bf(u32 a, u32 b, float c) {
    return __builtin_amdgcn_fdot2_f32_bf16(__builtin_bit_cast(b2v, a), __builtin_bit_cast(b2v, b), c, false);
}
__device__ __forceinline__ NOPK void glds16(const void* g, void* l) {
    __builtin_amdgcn_global_load_lds((const __attribute__((address_space(1))) void*)g,
                                     (__attribute__((address_space(3))) void*)l, 16, 0, 0);
}
__device__ __forceinline__ NOPK u32 lds_addr(const void* l) { return (u32)(size_t)(__attribute__((address_space(3))) const char*)l; }
#define GLDS16(ldsa, voff, sbase) \
    asm volatile("s_mov_b32 m0, %0\n\ts_nop 0\n\tglobal_load_lds_dwordx4 %1, %2" ::"s"(ldsa), "v"(voff), "s"(sbase) : "memory")
#define GLDS4(ldsa, voff, sbase) \
    asm volatile("s_mov_b32 m0, %0\n\ts_nop 0\n\tglobal_load_lds_dword %1, %2" ::"s"(ldsa), "v"(voff), "s"(sbase) : "memory")
template <int CTRL> __device__ __forceinline__ NOPK float dpp(float v) {
    return __builtin_bit_cast(float, __builtin_amdgcn_update_dpp(0, __builtin_bit_cast(int, v), CTRL, 0xf, 0xf, true));
}
__device__ __forceinline__ NOPK float swap32(float v) {
    const u32 b = __builtin_bit_cast(u32, v);
    const auto r = __builtin_amdgcn_permlane32_swap(b, b, false, false);
    return __builtin_bit_cast(float, (threadIdx.x & 32) ? r[0] : r[1]);
}
__device__ __forceinline__ NOPK float red16(float v) {
    v += dpp<0xB1>(v); v += dpp<0x4E>(v); v += dpp<0x141>(v); v += dpp<0x140>(v);
    return v;
}
__device__ __forceinline__ NOPK float max64(float v) {
    v = fmaxf(v, dpp<0xB1>(v)); v = fmaxf(v, dpp<0x4E>(v)); v = fmaxf(v, dpp<0x141>(v)); v = fmaxf(v, dpp<0x140>(v));
    const int b = __builtin_bit_cast(int, v);
    float a0 = __builtin_bit_cast(float, __builtin_amdgcn_readlane(b, 0)), a1 = __builtin_bit_cast(float, __builtin_amdgcn_readlane(b, 16));
    float a2 = __builtin_bit_cast(float, __builtin_amdgcn_readlane(b, 32)), a3 = __builtin_bit_cast(float, __builtin_amdgcn_readlane(b, 48));
    return fmaxf(fmaxf(a0, a1), fmaxf(a2, a3));
}
__device__ __forceinline__ NOPK void st16(void* ptr, u4v v) {
    asm volatile("global_store_dwordx4 %0, %1, off sc1\n\ts_nop 1" ::"v"(ptr), "v"(v) : "memory");
}
__device__ __forceinline__ NOPK void ln_stats(float s1, float s2, float& mb, float& den) {
    float mean = s1 * (1.0f / D);
    float var = fmaxf(s2 * (1.0f / D) - mean * mean, 0.f);
    mb = rbf(mean);
    den = rbf(__builtin_amdgcn_sqrtf(rbf(rbf(var) + 1e-5f)));
}
__device__ __forceinline__ NOPK float ln_apply(float x, float mb, float rden) { return rbf(rbf(x - mb) * rden); }

#define SG(i) {}
#define SGDECL
#define SGDUMP(n)
#define TLB()
#define TLE(off)
struct Params {
    const u16 *x, *Are, *Aim, *Bre, *Bim, *Cre, *Cim, *logdt, *Dskip, *w1, *b1, *w2;
    u16* out;
    char* ws;
};

typedef float f16v __attribute__((ext_vector_type(16)));
__device__ __forceinline__ NOPK f16v mfma32_bf(u4v a, u4v b, f16v c) {
    return __builtin_amdgcn_mfma_f32_32x32x16_bf16(__builtin_bit_cast(b8v, a), __builtin_bit_cast(b8v, b), c, 0, 0, 0);
}
#define STATV 0
#define VMWAIT(N) asm volatile("s_waitcnt vmcnt(" #N ") lgkmcnt(0)" ::: "memory")

#define RAWBAR() { asm volatile("s_waitcnt lgkmcnt(0)" ::: "memory"); __builtin_amdgcn_s_barrier(); asm volatile("" ::: "memory"); }

__device__ __forceinline__ NOPK void gemm1_role(const Params& p, int b, char* smem) {
    const int tid = threadIdx.x, lane = tid & 63;
    const int w = __builtin_amdgcn_readfirstlane(tid >> 6);
    const int wt = w >> 2, wf = w & 3;
    const int xcd = b & 7, j = b >> 3;
    const int mb = 4 * (xcd >> 1) + (j & 3), nt = 7 * (xcd & 1) + (j >> 2);
    const int l31 = lane & 31, hh = lane >> 5;
    const bool half = (nt == 13);
    SGDECL
#define STOFF 0
    SG(0)
    u32 xo[2], wo;
#pragma unroll
    for (int i = 0; i < 2; ++i) {
        int P = (2 * w + i) * 64 + lane, row = P >> 3, ch = (P & 7) ^ ((row >> 1) & 7);
        xo[i] = (u32)(row * (D * 2) + ch * 16);
    }
    {
        int P = w * 64 + lane, k = P >> 4, ch = (P & 15) ^ ((k & 3) << 2);
        if (half) ch &= 7;
        wo = (u32)(k * (F * 2) + ch * 16);
    }
    const char* xb = (const char*)p.x + (size_t)mb * 128 * (D * 2);
    const char* wb = (const char*)p.w1 + nt * 256;
    const u32 lx = lds_addr(smem) + w * 2048, lw = lds_addr(smem) + 49152 + w * 1024;
#define SROT(st) (st)
#define G1_X(st) { const char* s_ = xb + SROT(st) * 128; GLDS16(lx + ((st) % 3) * 16384, xo[0], s_); GLDS16(lx + ((st) % 3) * 16384 + 1024, xo[1], s_); }
#define G1_W(h) { const char* s_ = wb + (size_t)(SROT((h) >> 1) * 64 + ((h)&1) * 32) * (F * 2); GLDS16(lw + ((h)&3) * 8192, wo, s_); }
    G1_W(0) G1_X(0)
    u2v bia[4];
    {
        const u32 bo = (u32)min(nt * 128 + 32 * wf + 4 * hh, F - 28) * 2u;
        asm volatile("global_load_dwordx2 %0, %4, %5\n\tglobal_load_dwordx2 %1, %4, %5 offset:16\n\t"
                     "global_load_dwordx2 %2, %4, %5 offset:32\n\tglobal_load_dwordx2 %3, %4, %5 offset:48"
                     : "=&v"(bia[0]), "=&v"(bia[1]), "=&v"(bia[2]), "=&v"(bia[3]) : "v"(bo), "s"(p.b1) : "memory");
    }
    G1_W(1) G1_X(1) G1_W(2) G1_W(3) G1_X(2)

    const int q = (lane & 15) >> 2, pp = lane & 3, blk = (lane >> 4) & 1;
    const int ro = (wf & 1) ^ wt, ri = wf >> 1;
    const char* xa[4][2];
    const char* wa[2];
#pragma unroll
    for (int u = 0; u < 4; ++u)
#pragma unroll
        for (int i = 0; i < 2; ++i)
            xa[u][i] = smem + (64 * wt + 32 * (i ^ ri) + l31) * 128 + (((2 * (u ^ ro) + hh) ^ ((l31 >> 1) & 7)) << 4);
#pragma unroll
    for (int o = 0; o < 2; ++o)
        wa[o] = smem + 49152 + (16 * (o ^ ro) + 8 * hh + q) * 256 + (((4 * wf + 2 * blk + (pp >> 1)) ^ (q << 2)) << 4) + 8 * (pp & 1);

    f16v acc[2];
#pragma unroll
    for (int i = 0; i < 16; ++i) acc[0][i] = acc[1][i] = 0.f;
    u4v xf[2][2][2], wfr[2][2];
    float s1a = 0.f, s1b = 0.f, s2a = 0.f, s2b = 0.f, csa = 0.f, csb = 0.f;
    SG(1)
#define G1_READ(h, o)                                                                                    \
    {                                                                                                    \
        u2v lo = tr_read(wa[o] + ((h)&3) * 8192), hi = tr_read(wa[o] + ((h)&3) * 8192 + 1024);           \
        wfr[(h)&1][o] = (u4v){lo[0], lo[1], hi[0], hi[1]};                                               \
        xf[(h)&1][o][0] = *(const u4v*)(xa[((h)&1) * 2 + o][0] + (((h) >> 1) % 3) * 16384);              \
        xf[(h)&1][o][1] = *(const u4v*)(xa[((h)&1) * 2 + o][1] + (((h) >> 1) % 3) * 16384);              \
    }
#define G1_M(h, o, i)                                                                                    \
    if ((h) >= 0) {                                                                                      \
        acc[i] = mfma32_bf(wfr[(h)&1][o], xf[(h)&1][o][i], acc[i]);                                      \
        asm volatile("" : "+v"(acc[i]));                                                                 \
    }                                                                                                    \
    __builtin_amdgcn_sched_barrier(0);
#define G1_STAT(h, e)                                                                                    \
    if ((h) >= 0) {                                                                                      \
        const u32 xv_ = xf[(h)&1][0][0][e];                                                              \
        s1a = dot2bf(xv_, 0x3f803f80u, s1a); s2a = dot2bf(xv_, xv_, s2a);                                \
        csa = dot2bf(wfr[(h)&1][0][e], 0x3f803f80u, csa);                                                \
    }
#define G1_PIN() asm volatile("" : "+v"(s1a), "+v"(s1b), "+v"(s2a), "+v"(s2b), "+v"(csa), "+v"(csb)); __builtin_amdgcn_sched_barrier(0);
#define G1_IT(h, NW)                                                                                     \
    {                                                                                                    \
        VMWAIT(NW);                                                                                      \
        __builtin_amdgcn_s_barrier();                                                                    \
        G1_M(h, 0, 0)                                                                                    \
        if ((h) >= 0 && (h) + 4 <= 19) G1_W((h) + 4)                                                     \
        if ((h) >= 1 && ((h)&1) && ((h) - 1) / 2 + 3 <= 9) G1_X(((h) - 1) / 2 + 3)                       \
        G1_M(h, 0, 1)                                                                                    \
        G1_READ((h) + 1, 0) G1_STAT(h, 0) G1_PIN()                                                       \
        G1_M(h, 1, 0)                                                                                    \
        G1_READ((h) + 1, 1) G1_STAT(h, 1) G1_PIN()                                                       \
        G1_M(h, 1, 1)                                                                                    \
        G1_STAT(h, 2) G1_STAT(h, 3) G1_PIN()                                                             \
    }
    G1_IT(-1, 11) G1_IT(0, 6) G1_IT(1, 4) G1_IT(2, 6) G1_IT(3, 4) SG(2) G1_IT(4, 6) G1_IT(5, 4) G1_IT(6, 6) G1_IT(7, 4)
    G1_IT(8, 6) SG(3) G1_IT(9, 4) G1_IT(10, 6) G1_IT(11, 4) G1_IT(12, 6) G1_IT(13, 4) SG(4) G1_IT(14, 6) G1_IT(15, 4)
    G1_IT(16, 4) G1_IT(17, 1) G1_IT(18, 0)
    G1_M(19, 0, 0) G1_M(19, 0, 1) G1_M(19, 1, 0) G1_M(19, 1, 1) G1_STAT(19, 0) G1_STAT(19, 1) G1_STAT(19, 2) G1_STAT(19, 3)
    s1a += s1b; s2a += s2b; csa += csb;
    SG(5)
    asm volatile("" : "+v"(bia[0]), "+v"(bia[1]), "+v"(bia[2]), "+v"(bia[3]));
    char* rsb = smem + 16384;
    char* csl = smem + 16384 + 2048;
    {
        const float a1 = s1a + swap32(s1a), a2 = s2a + swap32(s2a), ac = csa + swap32(csa);
        if (hh == 0) *(f2v*)(rsb + ((64 * wt + 32 * ri + l31) * 2 + (wf & 1)) * 8) = (f2v){a1, a2};
        else *(float*)(csl + ((32 * wf + l31) * 2 + wt) * 4) = ac;
    }
    RAWBAR()
    SG(6)
    if (!(half && wf >= 2)) {
        float csv[16], bsv[16];
#pragma unroll
        for (int g = 0; g < 4; ++g) {
            const f4v c0 = *(const f4v*)(csl + (32 * wf + 8 * g + 4 * hh) * 8), c1 = *(const f4v*)(csl + (32 * wf + 8 * g + 4 * hh) * 8 + 16);
            csv[4 * g] = c0[0] + c0[1]; csv[4 * g + 1] = c0[2] + c0[3]; csv[4 * g + 2] = c1[0] + c1[1]; csv[4 * g + 3] = c1[2] + c1[3];
            bsv[4 * g] = bflo(bia[g][0]) * 1.44269504f; bsv[4 * g + 1] = bfhi(bia[g][0]) * 1.44269504f;
            bsv[4 * g + 2] = bflo(bia[g][1]) * 1.44269504f; bsv[4 * g + 3] = bfhi(bia[g][1]) * 1.44269504f;
        }
        char* actb = p.ws + WS_ACT + ((size_t)(mb * 2 + wt) * KS2 + nt * 4 + wf) * 4096 + (size_t)((l31 >> 4) * 1024 + ((l31 & 15) + 16 * hh) * 16);
#pragma unroll
        for (int i = 0; i < 2; ++i) {
            const f4v u0 = *(const f4v*)(rsb + (64 * wt + 32 * (i ^ ri) + l31) * 16);
            float mean, den;
            ln_stats(u0[0] + u0[2], u0[1] + u0[3], mean, den);
            const float rdl = __builtin_amdgcn_rcpf(den) * 1.44269504f;
#pragma unroll
            for (int c = 0; c < 2; ++c) {
                u4v o;
#pragma unroll
                for (int e = 0; e < 4; ++e) {
                    float r[2];
#pragma unroll
                    for (int z = 0; z < 2; ++z) {
                        const int ix = 8 * c + 2 * e + z;
                        const float u = fmaf(fmaf(-mean, csv[ix], acc[i][ix]), rdl, bsv[ix]);
                        const float e1 = __builtin_amdgcn_exp2f(u) + 1.0f;
                        r[z] = u * fmaf(__builtin_amdgcn_rcpf(fmaf(e1, e1, 1.0f)), -1.38629436f, 0.69314718f);
                    }
                    o[e] = pkbf(r[0], r[1]);
                }
                st16(actb + (i ^ ri) * 2048 + c * 512, o);
            }
        }
    }
    SG(7)
    SGDUMP(8)
}

__device__ __forceinline__ NOPK void ln_role(const Params& p, int rb, char* smem) {
    const int tid = threadIdx.x, lane = tid & 63;
    const int w = __builtin_amdgcn_readfirstlane(tid >> 6);
    const int rl = 4 * w + (lane >> 4), t = rb * 32 + rl, c15 = lane & 15;
    u4v v[5];
#pragma unroll
    for (int i = 0; i < 5; ++i) v[i] = *(const u4v*)(p.x + (size_t)t * D + (c15 + 16 * i) * 8);
    float s = 0.f;
#pragma unroll
    for (int i = 0; i < 5; ++i)
#pragma unroll
        for (int e = 0; e < 4; ++e) s += bflo(v[i][e]) + bfhi(v[i][e]);
    const float mean = red16(s) * (1.0f / D);
    float qv = 0.f;
#pragma unroll
    for (int i = 0; i < 5; ++i)
#pragma unroll
        for (int e = 0; e < 4; ++e) {
            float a = bflo(v[i][e]) - mean, c = bfhi(v[i][e]) - mean;
            qv = fmaf(a, a, qv); qv = fmaf(c, c, qv);
        }
    const float var = red16(qv) * (1.0f / D);
    const float mbf = rbf(mean);
    const float rd = 1.0f / rbf(__builtin_sqrtf(rbf(rbf(var) + 1e-5f)));
#pragma unroll
    for (int i = 0; i < 5; ++i) {
        u2v o0, o1;
        o0[0] = pkh(ln_apply(bflo(v[i][0]), mbf, rd), ln_apply(bfhi(v[i][0]), mbf, rd));
        o0[1] = pkh(ln_apply(bflo(v[i][1]), mbf, rd), ln_apply(bfhi(v[i][1]), mbf, rd));
        o1[0] = pkh(ln_apply(bflo(v[i][2]), mbf, rd), ln_apply(bfhi(v[i][2]), mbf, rd));
        o1[1] = pkh(ln_apply(bflo(v[i][3]), mbf, rd), ln_apply(bfhi(v[i][3]), mbf, rd));
        *(u2v*)(smem + rl * 1288 + (c15 + 16 * i) * 16) = o0;
        *(u2v*)(smem + rl * 1288 + (c15 + 16 * i) * 16 + 8) = o1;
    }
    RAWBAR()
    const int G = lane >> 4, q = c15 >> 2, pp = lane & 3;
    const char* src = smem + (16 * (G >> 1) + 4 * q + 2 * (G & 1)) * 1288 + pp * 8;
    char* dst = p.ws + WS_REC + (size_t)c15 * REC + REC_X + (size_t)rb * 64 + (G ^ ((rb >> 2) & 1)) * 16;
#pragma unroll
    for (int it = 0; it < 5; ++it) {
        const int cg = w * 5 + it;
        u2v lo = tr_read(src + cg * 32), hi = tr_read(src + cg * 32 + 1288);
        st16(dst + (size_t)cg * 16 * REC, (u4v){lo[0], lo[1], hi[0], hi[1]});
    }
}

struct cplx2 { f2v r, i; };
__device__ __forceinline__ NOPK cplx2 cmul(cplx2 a, cplx2 b) {
    cplx2 o;
    o.r = a.r * b.r - a.i * b.i;
    o.i = a.r * b.i + a.i * b.r;
    return o;
}
struct cplxh { h2v r, i; };
__device__ __forceinline__ NOPK cplxh toh(cplx2 a) { return {__builtin_convertvector(a.r, h2v), __builtin_convertvector(a.i, h2v)}; }
__device__ __forceinline__ NOPK cplx2 cexp2(f2v zr, f2v zi, float c) {
    cplx2 o;
#pragma unroll
    for (int m = 0; m < 2; ++m) {
        float mag = __builtin_amdgcn_exp2f(zr[m] * c * 1.44269504089f);
        float ph = zi[m] * c;
        const float CH = 0.15915494f, CL = 6.4206382e-9f;
        float hi = ph * CH;
        float lo = fmaf(ph, CH, -hi) + ph * CL;
        float fr = __builtin_amdgcn_fractf(hi) + lo;
        o.r[m] = mag * __builtin_amdgcn_cosf(fr);
        o.i[m] = mag * __builtin_amdgcn_sinf(fr);
    }
    return o;
}
static constexpr int KG_ROW = 544;

__device__ __forceinline__ NOPK void kgen_role(const Params& p, int d, int part, char* sw  ) {
    const int lane = threadIdx.x & 63, l15 = lane & 15, q4 = lane >> 4;
    const int o = d * NS + 2 * lane;
    u32 are = *(const u32*)(p.Are + o), aim = *(const u32*)(p.Aim + o), bre = *(const u32*)(p.Bre + o);
    u32 bim = *(const u32*)(p.Bim + o), cre = *(const u32*)(p.Cre + o), cim = *(const u32*)(p.Cim + o);
    float dt = rbf(expf(bf2f(p.logdt[d])));
    f2v zr = {dt * bflo(are), dt * bfhi(are)}, zi = {dt * bflo(aim), dt * bfhi(aim)};
    cplx2 dB = {{dt * bflo(bre), dt * bfhi(bre)}, {dt * bflo(bim), dt * bfhi(bim)}};
    cplx2 C = {{bflo(cre), bfhi(cre)}, {bflo(cim), bfhi(cim)}};
    cplx2 W = cmul(C, dB);
    float wm = fmaxf(fmaxf(fabsf(W.r[0]), fabsf(W.r[1])), fmaxf(fabsf(W.i[0]), fabsf(W.i[1])));
    const float dsk = bf2f(p.Dskip[d]);
    wm = fmaxf(max64(wm), fabsf(dsk) * (1.0f / 128.0f));
    int eU = 14 - __builtin_amdgcn_frexp_expf(wm);
    eU = min(max(eU, -100), 100);
    const float sU = __builtin_amdgcn_ldexpf(1.0f, eU);
    W.r *= sU; W.i *= sU;
    char* rec = p.ws + WS_REC + (size_t)d * REC;
    if (part == 0 && lane < 34) {
        u4v z = {0u, 0u, 0u, 0u};
        if (lane == 0) z[0] = f2u(__builtin_amdgcn_ldexpf(1.0f, 8 - eU));
        *(u4v*)(rec + (lane < 2 ? lane * 16 : 4160 + (lane - 2) * 16)) = z;
    }
    const float KSC = 2.384185791015625e-07f;

    char* r0 = sw;
    char* kc = sw + 16 * KG_ROW;
#define KG_PUT(row, v) *(u2v*)(r0 + (row)*KG_ROW + lane * 8) = (u2v){pkh((v).r[0], (v).r[1]), pkh((v).i[0], (v).i[1])}
#define KG_PUTH(row, T_, E_)                                                                            \
    *(u2v*)(r0 + (row)*KG_ROW + lane * 8) = (u2v){__builtin_bit_cast(u32, __builtin_elementwise_fma(T_.r, E_.r, -(T_.i * E_.i))), \
                                                  __builtin_bit_cast(u32, __builtin_elementwise_fma(T_.r, E_.i, T_.i * E_.r))}
#define KG_MMA(A)                                         \
    A = (f4v){0.f, 0.f, 0.f, 0.f};                        \
    _Pragma("unroll") for (int ks = 0; ks < 8; ++ks) A = mfma_h(*(const u4v*)(r0 + l15 * KG_ROW + ks * 64 + q4 * 16), vf[ks], A);
    const cplx2 G = cexp2(zr, zi, 128.f);
    u16* kch = (u16*)kc;
    char* kx = rec + REC_K;
    u4v vf[8];
    if (part == 0) {
        {
            cplx2 v1 = cexp2(zr, zi, 8.f);
            cplx2 v = {{16384.f, 16384.f}, {0.f, 0.f}};
#pragma unroll
            for (int jj = 0; jj < 16; ++jj) {
                cplx2 t = {v.r, -v.i};
                KG_PUT(jj, t);
                if (jj < 15) v = cmul(v, v1);
            }
        }
        RAWBAR()
#pragma unroll
        for (int ks = 0; ks < 8; ++ks) vf[ks] = *(const u4v*)(r0 + l15 * KG_ROW + ks * 64 + q4 * 16);
        cplx2 E[8];
        E[1] = cexp2(zr, zi, 1.f);
        E[2] = cmul(E[1], E[1]); E[3] = cmul(E[2], E[1]); E[4] = cmul(E[2], E[2]);
        E[5] = cmul(E[4], E[1]); E[6] = cmul(E[4], E[2]); E[7] = cmul(E[4], E[3]);
        const cplxh T0 = toh(W), T1 = toh(cmul(W, G));
        RAWBAR()
        *(u2v*)(r0 + lane * 8) = (u2v){__builtin_bit_cast(u32, T0.r), __builtin_bit_cast(u32, T0.i)};
        *(u2v*)(r0 + 8 * KG_ROW + lane * 8) = (u2v){__builtin_bit_cast(u32, T1.r), __builtin_bit_cast(u32, T1.i)};
#pragma unroll
        for (int c = 1; c < 8; ++c) {
            const cplxh Eh = toh(E[c]);
            KG_PUTH(c, T0, Eh);
            KG_PUTH(8 + c, T1, Eh);
        }
        f4v acc;
        KG_MMA(acc)
        if (lane == 0) acc[0] = fmaf(dsk * sU, 16384.f, acc[0]);
        *(u2v*)(kx + (128 * (q4 >> 1) + 4 * (q4 & 1) + 8 * l15) * 2) = (u2v){pkh(acc[0] * KSC, acc[1] * KSC), pkh(acc[2] * KSC, acc[3] * KSC)};
    } else {
        const cplx2 E2 = cexp2(zr, zi, 2.f);
        const cplx2 E4 = cmul(E2, E2);
        const cplxh E2h = toh(E2), E4h = toh(E4), E6h = toh(cmul(E4, E2));
        cplx2 T = cmul(W, cexp2(zr, zi, 256.f));
#pragma unroll
        for (int a = 0; a < 6; ++a) {
            const cplxh Th = toh(T);
            const int row = a < 2 ? 4 * a : 4 + 2 * a;
            *(u2v*)(r0 + row * KG_ROW + lane * 8) = (u2v){__builtin_bit_cast(u32, Th.r), __builtin_bit_cast(u32, Th.i)};
            if (a < 2) { KG_PUTH(row + 1, Th, E2h); KG_PUTH(row + 2, Th, E4h); KG_PUTH(row + 3, Th, E6h); }
            else KG_PUTH(row + 1, Th, E4h);
            if (a < 5) T = cmul(T, G);
        }
        RAWBAR()
#pragma unroll
        for (int ks = 0; ks < 8; ++ks) vf[ks] = *(const u4v*)(r0 - 10240 + l15 * KG_ROW + ks * 64 + q4 * 16);
        RAWBAR()
        f4v acc1, acc2;
        KG_MMA(acc1)
        T = cmul(W, cexp2(zr, zi, 1024.f));
#pragma unroll
        for (int a = 0; a < 9; ++a) {
            KG_PUT(a, T);
            if (a < 8) T = cmul(T, G);
        }
        KG_MMA(acc2)
        {
            u32 h01 = pkh(acc1[0] * KSC, acc1[1] * KSC), h23 = pkh(acc1[2] * KSC, acc1[3] * KSC);
            if (q4 < 2) {
                *(u2v*)(kc + (64 * q4 + 4 * l15) * 2) = (u2v){h01, h23};
            } else {
                *(u32*)(kc + (128 + 64 * (q4 - 2) + 2 * l15) * 2) = h01;
                *(u32*)(kc + (128 + 64 * (q4 - 2) + 32 + 2 * l15) * 2) = h23;
            }
            u32 g01 = pkh(acc2[0] * KSC, acc2[1] * KSC), g23 = pkh(acc2[2] * KSC, acc2[3] * KSC);
            if (q4 < 2) {
                kch[256 + 64 * q4 + l15] = (u16)g01;
                kch[256 + 64 * q4 + 16 + l15] = (u16)(g01 >> 16);
                kch[256 + 64 * q4 + 32 + l15] = (u16)g23;
                kch[256 + 64 * q4 + 48 + l15] = (u16)(g23 >> 16);
            } else if (lane == 32) {
                kch[384] = (u16)g01;
            }
        }
        if (lane < 32) {
            u2v s = *(const u2v*)(kc + lane * 8);
            u32 s4 = kch[lane * 4 + 4];
            u4v b2;
            b2[0] = (s[0] & 0xffffu) | (s[0] << 16);
            b2[1] = (s[0] >> 16) | (s[1] << 16);
            b2[2] = (s[1] & 0xffffu) | (s[1] << 16);
            b2[3] = (s[1] >> 16) | (s4 << 16);
            st16(kx + 512 + lane * 16, b2);
        }
        {
            u32 s = *(const u32*)(kc + 256 + lane * 4);
            u32 c = kch[128 + lane * 2 + 2];
            u4v v;
            v[0] = (s & 0xffffu) | (s << 16);
            v[1] = s;
            v[2] = (s >> 16) | (s & 0xffff0000u);
            v[3] = c | (c << 16);
            st16(kx + 1024 + lane * 16, v);
        }
#pragma unroll
        for (int it = 0; it < 2; ++it) {
            int oc = lane + 64 * it;
            u32 a = kch[256 + oc], c = kch[257 + oc];
            u4v v;
            v[0] = a | (a << 16);
            v[1] = v[0];
            v[2] = ((oc & 1) ? c : a) | (c << 16);
            v[3] = c | (c << 16);
            st16(kx + 2048 + oc * 16, v);
        }
    }
}

__device__ __forceinline__ NOPK void w2pack_role(const Params& p, int unit, char* sw) {
    const int lane = threadIdx.x & 63, l15 = lane & 15, q4 = lane >> 4;
    const int nb = unit / KS2, ks = unit - nb * KS2;
#pragma unroll
    for (int i = 0; i < 5; ++i) {
        int c = lane + 64 * i, row = c / 10, ch = c - row * 10;
        *(u4v*)(sw + c * 16) = *(const u4v*)(p.w2 + (size_t)(ks * 32 + row) * D + nb * 80 + ch * 8);
    }
    const int q = l15 >> 2, pp = lane & 3;
    char* dst = p.ws + WS_W2P + (size_t)unit * 5120 + lane * 16;
    const char* src = sw + (16 * (q4 >> 1) + 4 * (q4 & 1) + q) * 160 + pp * 8;
#pragma unroll
    for (int dt = 0; dt < 5; ++dt) {
        u2v lo = tr_read(src + dt * 32);
        u2v hi = tr_read(src + 8 * 160 + dt * 32);
        st16(dst + dt * 1024, (u4v){lo[0], lo[1], hi[0], hi[1]});
    }
}

__global__ __launch_bounds__(512, 4) NOPK void k1(Params p) {
    extern __shared__ __attribute__((aligned(16))) char smem[];
    const int b = blockIdx.x;
    const int w = __builtin_amdgcn_readfirstlane(threadIdx.x >> 6);
    TLB()
    if (b < 224) {
        gemm1_role(p, b, smem);
    } else if (b < 438) {
        const int d = (b - 224) * 3 + (w >> 1);
        if (w < 6 && d < D) {
            kgen_role(p, d, w & 1, smem + w * 10240);
        } else {
            if (w >= 6) {
                const int unit = (b - 224) * 2 + (w - 6);
                w2pack_role(p, unit, smem + w * 10240);
                if (unit < 4) w2pack_role(p, 428 + unit, smem + w * 10240);
                if (b == 437 && (threadIdx.x & 63) < 8 && w == 7) *(u32*)(p.ws + WS_CNT + (threadIdx.x & 63) * 256) = 0u;
            }
            __builtin_amdgcn_s_barrier();
            __builtin_amdgcn_s_barrier();
        }
    } else {
        ln_role(p, b - 438, smem);
    }
    TLE(0)
}

static constexpr int CV_WAVE = 9216;
static constexpr int K2_RED = 3 * CV_WAVE;
static constexpr int K2_YS = K2_RED + 61440;
static constexpr int K2_LDS = K2_YS + 10240;

#define AC0 "a"
#define AC1 "a"
#define AC2 "a"
#define AC3 "v"
#define AC4 "v"
#define AC5 "v"
#define G2_BUF(C, i) C AC##i(af[i][0]), C AC##i(af[i][1]), C AC##i(af[i][2]), C AC##i(af[i][3]), C "a"(wf[i][0]), C "a"(wf[i][1]), C "a"(wf[i][2]), C "a"(wf[i][3]), C "a"(wf[i][4])
#define G2_LOAD(i)                                                                                                   \
    asm volatile("buffer_load_dwordx4 %0, %9, %15, %17 offen\n\tbuffer_load_dwordx4 %4, %13, %16, %18 offen\n\t"     \
                 "buffer_load_dwordx4 %1, %10, %15, %17 offen\n\tbuffer_load_dwordx4 %2, %11, %15, %17 offen\n\t"    \
                 "buffer_load_dwordx4 %3, %12, %15, %17 offen\n\tbuffer_load_dwordx4 %5, %13, %16, %18 offen offset:1024\n\t" \
                 "buffer_load_dwordx4 %6, %13, %16, %18 offen offset:2048\n\tbuffer_load_dwordx4 %7, %13, %16, %18 offen offset:3072\n\t" \
                 "buffer_load_dwordx4 %8, %14, %16, %18 offen"                                                       \
                 : G2_BUF("=&", i) : "v"(ao[0]), "v"(ao[1]), "v"(ao[2]), "v"(ao[3]), "v"(ao[4]), "v"(ao[5]), "s"(ra), "s"(rw), \
                   "s"((i)*4096), "s"((i)*5120) : "memory");
#define G2_MF(d, w_, a_) "v_mfma_f32_16x16x32_bf16 %" #d ", %" #w_ ", %" #a_ ", %" #d "\n\t"
#define G2_M4(dt, i, VO, OFF)                                                                           \
    asm volatile(G2_MF(0, 4, 5) G2_MF(1, 4, 6) G2_MF(2, 4, 7) G2_MF(3, 4, 8)                            \
                 "buffer_load_dwordx4 %4, %9, %10, %11 offen offset:" #OFF                              \
                 : "+a"(acc2[dt][0]), "+a"(acc2[dt][1]), "+a"(acc2[dt][2]), "+a"(acc2[dt][3]), "+a"(wf[i][dt]) \
                 : AC##i(af[i][0]), AC##i(af[i][1]), AC##i(af[i][2]), AC##i(af[i][3]), "v"(ao[VO]), "s"(rw), "s"(sw) : "memory");
#define G2_BODY(i)                                                                                      \
    {                                                                                                   \
        G2_M4(0, i, 4, 0) G2_M4(1, i, 4, 1024) G2_M4(2, i, 4, 2048) G2_M4(3, i, 4, 3072) G2_M4(4, i, 5, 0) \
        asm volatile("buffer_load_dwordx4 %0, %4, %8, %9 offen\n\tbuffer_load_dwordx4 %1, %5, %8, %9 offen\n\t" \
                     "buffer_load_dwordx4 %2, %6, %8, %9 offen\n\tbuffer_load_dwordx4 %3, %7, %8, %9 offen" \
                     : "+" AC##i(af[i][0]), "+" AC##i(af[i][1]), "+" AC##i(af[i][2]), "+" AC##i(af[i][3])                   \
                     : "v"(ao[0]), "v"(ao[1]), "v"(ao[2]), "v"(ao[3]), "s"(ra), "s"(sa) : "memory");    \
    }
#define G2_STEP(i) { asm volatile("s_waitcnt vmcnt(45)" : G2_BUF("+", i)::"memory"); G2_BODY(i) sa += 4096; sw += 5120; }
#define G2_M4N(dt, i)                                                                                   \
    asm volatile(G2_MF(0, 4, 5) G2_MF(1, 4, 6) G2_MF(2, 4, 7) G2_MF(3, 4, 8) ""                         \
                 : "+a"(acc2[dt][0]), "+a"(acc2[dt][1]), "+a"(acc2[dt][2]), "+a"(acc2[dt][3])           \
                 : "a"(wf[i][dt]), AC##i(af[i][0]), AC##i(af[i][1]), AC##i(af[i][2]), AC##i(af[i][3]));
#define G2_SSTEP(k, i)                                                                                  \
    {                                                                                                   \
        if constexpr ((k) < 2) {                                                                        \
            const u32 sa = ((k) + 6) * 4096, sw = ((k) + 6) * 5120;                                     \
            G2_BODY(i)                                                                                  \
        } else {                                                                                        \
            G2_M4N(0, i) G2_M4N(1, i) G2_M4N(2, i) G2_M4N(3, i) G2_M4N(4, i)                            \
        }                                                                                               \
        __builtin_amdgcn_sched_barrier(0);                                                              \
    }
#define G2_ACC(a0) "+a"(acc2[a0][0]), "+a"(acc2[a0][1]), "+a"(acc2[a0][2]), "+a"(acc2[a0][3])
#define G2_ZERO()                                                                                       \
    _Pragma("unroll") for (int dt = 0; dt < 5; ++dt) _Pragma("unroll") for (int tt = 0; tt < 4; ++tt) acc2[dt][tt] = (f4v){0.f, 0.f, 0.f, 0.f}; \
    asm volatile("s_nop 1" : G2_ACC(0), G2_ACC(1), G2_ACC(2), G2_ACC(3), G2_ACC(4));
#undef STOFF
#define STOFF (4 << 20)
__device__ __forceinline__ NOPK void conv_wave(const Params& p, int d, u32 lb, char* img, u4v (&af)[6][4], u4v (&wf)[6][5],
                                                f4v (&acc2)[5][4], u4v ra, u4v rw, const u32 (&ao)[6], u32 svc, const u32* cnt,
                                                const char* yb, u32 ysl) {
    const int lane = threadIdx.x & 63, l15 = lane & 15, q4 = lane >> 4;
    const int q = l15 >> 2, pp = lane & 3;
    const u32 kra = lb + REC_K + (32 * q4 - 4 * q + 4 * pp) * 2;
    u32 xra[2][2][2];
#pragma unroll
    for (int ap = 0; ap < 2; ++ap)
#pragma unroll
        for (int h = 0; h < 2; ++h)
#pragma unroll
            for (int g = 0; g < 2; ++g) {
                int G = l15 + 8 * g - 2 * q4 - h + 8;
                xra[ap][h][g] = lb + 4160 + G * 32 + ((ap ^ ((G >> 3) & 1)) * 16);
            }
    const u32 csrc = (lane >= 16 ? lane - 16 : (lane == 0 ? 63 : lane + 47)) * 4;
    asm volatile("s_waitcnt vmcnt(5)" ::: "memory");
    u4v kf[16], xf[4];
    f4v acc[8];
    float Y[8][7];
    float scale;
    asm volatile("ds_read_b32 %0, %1" : "=v"(scale) : "v"(lb));
#define CV_XRD(s)                                                                                          \
    {                                                                                                      \
        u2v v0, v1;                                                                                        \
        asm volatile("ds_read_b64 %0, %2 offset:%4\n\tds_read_b64 %1, %3 offset:%4" : "=&v"(v0), "=&v"(v1) \
                     : "v"(xra[(s) >> 5][0][(s)&1]), "v"(xra[(s) >> 5][1][(s)&1]), "i"((((s)&15) >> 1) * 512 + (((s) >> 4) & 1) * 8)); \
        xf[(s)&3] = (u4v){v0[0], v0[1], v1[0], v1[1]};                                                     \
    }
    CV_XRD(0) CV_XRD(1) CV_XRD(2)
#pragma unroll
    for (int e = 0; e < 16; ++e) {
        u2v lo, hi;
        asm volatile("ds_read_b64_tr_b16 %0, %2 offset:%3\n\tds_read_b64_tr_b16 %1, %2 offset:%4" : "=&v"(lo), "=&v"(hi)
                     : "v"(kra), "i"(e * 256), "i"(e * 256 + 32));
        kf[e] = (u4v){lo[0], lo[1], hi[0], hi[1]};
    }
    asm volatile("s_waitcnt lgkmcnt(0)" : "+v"(kf[0]), "+v"(kf[1]), "+v"(kf[2]), "+v"(kf[3]), "+v"(kf[4]), "+v"(kf[5]), "+v"(kf[6]), "+v"(kf[7]),
                 "+v"(kf[8]), "+v"(kf[9]), "+v"(kf[10]), "+v"(kf[11]), "+v"(kf[12]), "+v"(kf[13]), "+v"(kf[14]), "+v"(kf[15]), "+v"(scale));
#define CV_STEP(s)                                                                                         \
    {                                                                                                      \
        constexpr int a = (s) >> 4, g = (s)&15;                                                            \
        if constexpr ((s) + 3 < 64) CV_XRD(((s) + 3) & 63)                                                 \
          \
        _Pragma("unroll") for (int e = (g + 1) & 1; e + g <= 15; e += 2) {                                 \
            if constexpr (g == 0) {                                                                        \
                if (e < 2) asm volatile("s_waitcnt lgkmcnt(6)\n\tv_mfma_f32_16x16x32_f16 %0, %1, %2, 0" : "=&v"(acc[(e + g) >> 1]) : "v"(kf[e]), "v"(xf[(s)&3])); \
                else asm volatile("v_mfma_f32_16x16x32_f16 %0, %1, %2, 0" : "=&v"(acc[(e + g) >> 1]) : "v"(kf[e]), "v"(xf[(s)&3])); \
            } else {                                                                                       \
                if (e < 2) asm volatile("s_waitcnt lgkmcnt(%3)\n\tv_mfma_f32_16x16x32_f16 %0, %1, %2, %0" : "+v"(acc[(e + g) >> 1]) : "v"(kf[e]), "v"(xf[(s)&3]), "i"((s) + 3 < 64 ? 6 : 2 * (63 - (s)))); \
                else asm volatile("v_mfma_f32_16x16x32_f16 %0, %1, %2, %0" : "+v"(acc[(e + g) >> 1]) : "v"(kf[e]), "v"(xf[(s)&3])); \
            }                                                                                              \
        }                                                                                                  \
        if constexpr (g == 15) {      \
            asm volatile("s_nop 7\n\ts_nop 3" : "+v"(acc[0]), "+v"(acc[1]), "+v"(acc[2]), "+v"(acc[3]), "+v"(acc[4]), "+v"(acc[5]), "+v"(acc[6]), "+v"(acc[7])); \
            _Pragma("unroll") for (int t = 0; t < 8; ++t) _Pragma("unroll") for (int r = 0; r < 4; ++r)    \
                Y[t][r + a] = (a == 0 || r == 3) ? acc[t][r] : Y[t][r + a] + acc[t][r];                    \
            __builtin_amdgcn_sched_barrier(0);                                                             \
        }                                                                                                  \
    }
#define CV_S4(s) CV_STEP(s) CV_STEP((s) + 1) CV_STEP((s) + 2) CV_STEP((s) + 3)
#define CV_S16(s) CV_S4(s) CV_S4((s) + 4) CV_S4((s) + 8) CV_S4((s) + 12)
#define CV_S8(s) CV_S4(s) CV_S4((s) + 4)
    CV_S16(0) G2_LOAD(0) CV_S8(16) G2_LOAD(1) CV_S8(24) G2_LOAD(2) CV_S8(32) G2_LOAD(3) CV_S8(40) G2_LOAD(4) CV_S8(48) G2_LOAD(5) CV_S8(56)
    float cr[8][3];
#pragma unroll
    for (int t = 0; t < 8; ++t)
#pragma unroll
        for (int r = 0; r < 3; ++r)
            cr[t][r] = __builtin_bit_cast(float, __builtin_amdgcn_ds_bpermute(csrc, __builtin_bit_cast(int, Y[t][4 + r])));
    __builtin_amdgcn_sched_barrier(0);
#pragma unroll
    for (int t = 0; t < 8; ++t) {
        float y[4];
#pragma unroll
        for (int r = 0; r < 3; ++r) y[r] = Y[t][r] + (lane == 0 ? (t ? cr[t - (t > 0)][r] : 0.f) : cr[t][r]);
        y[3] = Y[t][3];
        *(u2v*)(img + t * 512 + (16 * l15 + 4 * q4) * 2) = (u2v){pkbf(y[0] * scale, y[1] * scale), pkbf(y[2] * scale, y[3] * scale)};
    }
    char* dst = p.ws + WS_YT + (size_t)d * 4096 + lane * 16;
    {
        u4v o[4];
#pragma unroll
        for (int i = 0; i < 4; ++i) o[i] = *(const u4v*)(img + i * 1024 + lane * 16);
#pragma unroll
        for (int i = 0; i < 4; ++i)
            asm volatile("global_store_dwordx4 %0, %1, off offset:%2 sc1\n\ts_nop 1" ::"v"(dst), "v"(o[i]), "i"(i * 1024) : "memory");
    }
    G2_ZERO()
    asm volatile("s_waitcnt vmcnt(0)\n\ts_mov_b64 exec, 1\n\tglobal_atomic_add %0, %1, %2\n\ts_mov_b64 exec, -1"
                 ::"v"(0u), "v"(1u), "s"(p.ws + WS_CNT + (d / 80) * 256) : "memory");
    asm volatile("" : G2_BUF("+", 0), G2_BUF("+", 1), G2_BUF("+", 2)::"memory");
    asm volatile("" : G2_BUF("+", 3), G2_BUF("+", 4), G2_BUF("+", 5)::"memory");
    G2_SSTEP(0, 0) G2_SSTEP(1, 1) G2_SSTEP(2, 2) G2_SSTEP(3, 3)
    u32 cv = 0;
    asm volatile("s_cmp_lg_u32 %1, 0\n\ts_cbranch_scc1 .Lsv%=\n\tglobal_load_dword %0, %0, %2 sc1\n.Lsv%=:" : "+v"(cv) : "s"(svc), "s"(cnt) : "memory", "scc");
    G2_SSTEP(4, 4) G2_SSTEP(5, 5)
    asm volatile("s_waitcnt vmcnt(0)" : "+v"(cv), G2_BUF("+", 0), G2_BUF("+", 1)::"memory");
    if (svc == 0) {
        unsigned spins = 0;
        while (__builtin_amdgcn_readfirstlane(cv) < 80u && ++spins < (1u << 22)) {
            __builtin_amdgcn_s_sleep(4);
            cv = __hip_atomic_load(cnt, __ATOMIC_RELAXED, __HIP_MEMORY_SCOPE_AGENT);
        }
        const u32 yo = (lane >> 3) * 4096 + (lane & 7) * 16;
#pragma unroll
        for (int i = 0; i < 10; ++i)
            asm volatile("s_mov_b32 m0, %0\n\ts_nop 0\n\tglobal_load_lds_dwordx4 %1, %2 sc1" ::"s"(ysl + i * 1024), "v"(yo), "s"(yb + i * 32768) : "memory");
    }
    G2_SSTEP(6, 0) G2_SSTEP(7, 1)
    asm volatile("s_waitcnt vmcnt(0)\n\ts_nop 7\n\ts_nop 7" : G2_ACC(0), G2_ACC(1), G2_ACC(2), G2_ACC(3), G2_ACC(4)::"memory");
}

__global__ __launch_bounds__(256, 1) NOPK void k2(Params p) {
    extern __shared__ __attribute__((aligned(16))) char smem[];
    const int b = blockIdx.x;
    const int w = __builtin_amdgcn_readfirstlane(threadIdx.x >> 6);
    TLB()
    const int xcd = b & 7, j = b >> 3;
    const int mb = 8 * (xcd >> 1) + (j & 7), nb = 4 * (xcd & 1) + (j >> 3);
    const int nch = (j & 1) ? 2 : 3;
    const int ch0 = xcd * 80 + (j >> 1) * 5 + ((j & 1) ? 3 : 0);
    const int k0 = ((j & 1) && w == 3) ? 34 : 8 * w;
    const int nk = w < nch ? 8 : ((j & 1) ? 2 * w + 14 : 30);
    const char* ab = p.ws + WS_ACT + ((size_t)mb * KS2 + k0) * 4096;
    const char* wb = p.ws + WS_W2P + ((size_t)nb * KS2 + k0) * 5120;
    u4v ra = {(u32)(size_t)ab, (u32)((size_t)ab >> 32), (u32)nk * 4096u, 0x00020000u};
    u4v rw = {(u32)(size_t)wb, (u32)((size_t)wb >> 32), (u32)nk * 5120u, 0x00020000u};
    const u32 lo16 = (threadIdx.x & 63) * 16;
    const u32 lb = lds_addr(smem);
    const u32 ao[6] = {lo16 + (w & 3) * 1024, lo16 + ((w + 1) & 3) * 1024, lo16 + ((w + 2) & 3) * 1024, lo16 + ((w + 3) & 3) * 1024, lo16, lo16 + 4096};
    SGDECL SG(0)
    if (w < nch) {
        const char* src = p.ws + WS_REC + (size_t)(ch0 + w) * REC;
#pragma unroll
        for (int i = 0; i < 9; ++i) GLDS16(lb + w * CV_WAVE + i * 1024, lo16, src + i * 1024);
    }
    u4v af[6][4], wf[6][5];
    u2v xv[5];
    {
        const u16* xp = p.x + (size_t)(mb * 64 + w * 16 + (threadIdx.x & 15)) * D + nb * 80 + ((threadIdx.x >> 4) & 3) * 4;
#pragma unroll
        for (int dt = 0; dt < 5; ++dt) xv[dt] = *(const u2v*)(xp + dt * 16);
    }
    SG(1)
    f4v acc2[5][4];
    if (w < nch) {
        conv_wave(p, ch0 + w, lb + w * CV_WAVE, smem + w * CV_WAVE, af, wf, acc2, ra, rw, ao, (u32)w,
                  (const u32*)(p.ws + WS_CNT + nb * 256), p.ws + WS_YT + (size_t)(nb * 80) * 4096 + (size_t)mb * 128, lb + K2_YS);
    } else {
        G2_LOAD(0) G2_LOAD(1) G2_LOAD(2) G2_LOAD(3) G2_LOAD(4) G2_LOAD(5)
        G2_ZERO()
        u32 sa = 6 * 4096, sw = 6 * 5120;
#pragma nounroll
        for (int r = 6; r < nk; r += 6) { G2_STEP(0) G2_STEP(1) G2_STEP(2) G2_STEP(3) G2_STEP(4) G2_STEP(5) }
#define G2_LAST(i, NW) { asm volatile("s_waitcnt vmcnt(" #NW ")" : G2_BUF("+", i)::"memory"); G2_SSTEP(2, i) }
        G2_LAST(0, 45) G2_LAST(1, 36) G2_LAST(2, 27) G2_LAST(3, 18) G2_LAST(4, 9) G2_LAST(5, 0)
        asm volatile("s_nop 7\n\ts_nop 7" : G2_ACC(0), G2_ACC(1), G2_ACC(2), G2_ACC(3), G2_ACC(4)::"memory");
    }
    SG(2)
    SG(3)
    int lane = threadIdx.x & 63;
    asm volatile("" : "+v"(lane));
    const int l15 = lane & 15, q4 = lane >> 4;
    const int t = mb * 64 + w * 16 + l15;
    const int dbase = nb * 80 + 4 * q4;
    char* red = smem + K2_RED;
#pragma unroll
    for (int tt = 1; tt < 4; ++tt) {
        char* dst = red + (((tt + w) & 3) * 3 + tt - 1) * 5120 + lane * 16;
#pragma unroll
        for (int dt = 0; dt < 5; ++dt) *(f4v*)(dst + dt * 1024) = acc2[dt][tt];
    }
    RAWBAR()
    SG(4)
    const char* ys = smem + K2_YS + (4 * q4 + (l15 >> 2)) * 128 + w * 32 + (lane & 3) * 8;
    const char* rsrc = red + w * 15360 + lane * 16;
    u2v svv[5];
    f4v part[5][3];
#pragma unroll
    for (int dt = 0; dt < 5; ++dt) {
        svv[dt] = tr_read(ys + dt * 2048);
#pragma unroll
        for (int i = 0; i < 3; ++i) part[dt][i] = *(const f4v*)(rsrc + i * 5120 + dt * 1024);
    }
    __builtin_amdgcn_sched_barrier(0);
#pragma unroll
    for (int dt = 0; dt < 5; ++dt) {
        const u2v sv = svv[dt];
        const f4v g = (acc2[dt][0] + part[dt][0]) + (part[dt][1] + part[dt][2]);
        u2v o;
#pragma unroll
        for (int r2 = 0; r2 < 2; ++r2) {
            const float p0 = g[2 * r2], p1 = g[2 * r2 + 1];
            const float g0 = p0 * __builtin_amdgcn_rcpf(1.0f + __builtin_amdgcn_exp2f(-1.44269504f * p0));
            const float g1 = p1 * __builtin_amdgcn_rcpf(1.0f + __builtin_amdgcn_exp2f(-1.44269504f * p1));
            o[r2] = pkbf(fmaf(bflo(sv[r2]), g0, bflo(xv[dt][r2])), fmaf(bfhi(sv[r2]), g1, bfhi(xv[dt][r2])));
        }
        *(u2v*)(p.out + (size_t)t * D + dbase + dt * 16) = o;
    }
    SG(5) SGDUMP(6)
    TLE(1 << 20)
}

extern "C" void kernel_launch(void* const* d_in, const int* in_sizes, int n_in, void* d_out, int out_size, void* d_ws,
                              size_t ws_size, hipStream_t stream) {
    Params p;
    p.x = (const u16*)d_in[0];
    p.Are = (const u16*)d_in[1]; p.Aim = (const u16*)d_in[2]; p.Bre = (const u16*)d_in[3]; p.Bim = (const u16*)d_in[4];
    p.Cre = (const u16*)d_in[5]; p.Cim = (const u16*)d_in[6]; p.logdt = (const u16*)d_in[7]; p.Dskip = (const u16*)d_in[8];
    p.w1 = (const u16*)d_in[10]; p.b1 = (const u16*)d_in[11]; p.w2 = (const u16*)d_in[12];
    p.out = (u16*)d_out;
    p.ws = (char*)d_ws;
    (void)hipFuncSetAttribute((const void*)k1, hipFuncAttributeMaxDynamicSharedMemorySize, LDS_BYTES);
    (void)hipFuncSetAttribute((const void*)k2, hipFuncAttributeMaxDynamicSharedMemorySize, K2_LDS);
    k1<<<502, 512, LDS_BYTES, stream>>>(p);
    k2<<<256, 256, K2_LDS, stream>>>(p);
}
```

```cpp
#define PT 11
#include <hip/hip_runtime.h>
#include <stdint.h>

#if defined(__HIP_DEVICE_COMPILE__) && !defined(ALLOWPK)
#define NOPK __attribute__((target("no-packed-fp32-ops")))
#else
#define NOPK
#endif
typedef unsigned short u16;
typedef unsigned int u32;
typedef short s4v __attribute__((ext_vector_type(4)));
typedef u32 u2v __attribute__((ext_vector_type(2)));
typedef u32 u4v __attribute__((ext_vector_type(4)));
typedef float f2v __attribute__((ext_vector_type(2)));
typedef float f4v __attribute__((ext_vector_type(4)));
typedef _Float16 h2v __attribute__((ext_vector_type(2)));
typedef _Float16 h8v __attribute__((ext_vector_type(8)));
typedef __bf16 b2v __attribute__((ext_vector_type(2)));
typedef __bf16 b8v __attribute__((ext_vector_type(8)));
#define LDSP(T, p) ((__attribute__((address_space(3))) T*)(p))
#define GLB(T, p) ((__attribute__((address_space(1))) T*)(p))

static constexpr int L = 2048, D = 640, NS = 128, F = 1728;
static constexpr int KS2 = F / 32;

static constexpr size_t WS_ACT = 0;
static constexpr size_t WS_W2P = WS_ACT + (size_t)L * F * 2;
static constexpr int REC_K = 32, REC_X = 4160 + 512, REC = 8768;
static constexpr size_t WS_REC = WS_W2P + (size_t)F * D * 2;
static constexpr size_t WS_YT = WS_REC + (size_t)641 * REC + 192;
static constexpr size_t WS_CNT = WS_YT + (size_t)L * D * 2;
static_assert(WS_REC % 256 == 0 && WS_YT % 256 == 0, "alignment");

static constexpr int LDS_BYTES = 80 * 1024;

__device__ __forceinline__ NOPK u32 f2u(float v) { return __builtin_bit_cast(u32, v); }
__device__ __forceinline__ NOPK float bf2f(u32 v) { return __builtin_bit_cast(float, (u32)(v << 16)); }
__device__ __forceinline__ NOPK float bflo(u32 v) { return __builtin_bit_cast(float, (u32)(v << 16)); }
__device__ __forceinline__ NOPK float bfhi(u32 v) { return __builtin_bit_cast(float, (u32)(v & 0xffff0000u)); }
__device__ __forceinline__ NOPK u32 pkbf(float a, float b) {
    f2v v = {a, b};
    return __builtin_bit_cast(u32, __builtin_convertvector(v, b2v));
}
__device__ __forceinline__ NOPK u32 pkh(float a, float b) {
    f2v v = {a, b};
    return __builtin_bit_cast(u32, __builtin_convertvector(v, h2v));
}
__device__ __forceinline__ NOPK float rbf(float a) { return bflo(pkbf(a, a)); }
__device__ __forceinline__ NOPK f4v mfma_bf(u4v a, u4v b, f4v c) {
    return __builtin_amdgcn_mfma_f32_16x16x32_bf16(__builtin_bit_cast(b8v, a), __builtin_bit_cast(b8v, b), c, 0, 0, 0);
}
__device__ __forceinline__ NOPK f4v mfma_h(u4v a, u4v b, f4v c) {
    return __builtin_amdgcn_mfma_f32_16x16x32_f16(__builtin_bit_cast(h8v, a), __builtin_bit_cast(h8v, b), c, 0, 0, 0);
}
__device__ __forceinline__ NOPK u2v tr_read(const char* p) {
    return __builtin_bit_cast(u2v, __builtin_amdgcn_ds_read_tr16_b64_v4i16(LDSP(s4v, p)));
}
__device__ __forceinline__ NOPK float dot2bf(u32 a, u32 b, float c) {
    return __builtin_amdgcn_fdot2_f32_bf16(__builtin_bit_cast(b2v, a), __builtin_bit_cast(b2v, b), c, false);
}
__device__ __forceinline__ NOPK void glds16(const void* g, void* l) {
    __builtin_amdgcn_global_load_lds((const __attribute__((address_space(1))) void*)g,
                                     (__attribute__((address_space(3))) void*)l, 16, 0, 0);
}
__device__ __forceinline__ NOPK u32 lds_addr(const void* l) { return (u32)(size_t)(__attribute__((address_space(3))) const char*)l; }
#define GLDS16(ldsa, voff, sbase) \
    asm volatile("s_mov_b32 m0, %0\n\ts_nop 0\n\tglobal_load_lds_dwordx4 %1, %2" ::"s"(ldsa), "v"(voff), "s"(sbase) : "memory")
#define GLDS4(ldsa, voff, sbase) \
    asm volatile("s_mov_b32 m0, %0\n\ts_nop 0\n\tglobal_load_lds_dword %1, %2" ::"s"(ldsa), "v"(voff), "s"(sbase) : "memory")
template <int CTRL> __device__ __forceinline__ NOPK float dpp(float v) {
    return __builtin_bit_cast(float, __builtin_amdgcn_update_dpp(0, __builtin_bit_cast(int, v), CTRL, 0xf, 0xf, true));
}
__device__ __forceinline__ NOPK float swap32(float v) {
    const u32 b = __builtin_bit_cast(u32, v);
    const auto r = __builtin_amdgcn_permlane32_swap(b, b, false, false);
    return __builtin_bit_cast(float, (threadIdx.x & 32) ? r[0] : r[1]);
}
__device__ __forceinline__ NOPK float red16(float v) {
    v += dpp<0xB1>(v); v += dpp<0x4E>(v); v += dpp<0x141>(v); v += dpp<0x140>(v);
    return v;
}
__device__ __forceinline__ NOPK float max64(float v) {
    v = fmaxf(v, dpp<0xB1>(v)); v = fmaxf(v, dpp<0x4E>(v)); v = fmaxf(v, dpp<0x141>(v)); v = fmaxf(v, dpp<0x140>(v));
    const int b = __builtin_bit_cast(int, v);
    float a0 = __builtin_bit_cast(float, __builtin_amdgcn_readlane(b, 0)), a1 = __builtin_bit_cast(float, __builtin_amdgcn_readlane(b, 16));
    float a2 = __builtin_bit_cast(float, __builtin_amdgcn_readlane(b, 32)), a3 = __builtin_bit_cast(float, __builtin_amdgcn_readlane(b, 48));
    return fmaxf(fmaxf(a0, a1), fmaxf(a2, a3));
}
__device__ __forceinline__ NOPK void st16(void* ptr, u4v v) {
    asm volatile("global_store_dwordx4 %0, %1, off sc1\n\ts_nop 1" ::"v"(ptr), "v"(v) : "memory");
}
__device__ __forceinline__ NOPK void ln_stats(float s1, float s2, float& mb, float& den) {
    float mean = s1 * (1.0f / D);
    float var = fmaxf(s2 * (1.0f / D) - mean * mean, 0.f);
    mb = rbf(mean);
    den = rbf(__builtin_amdgcn_sqrtf(rbf(rbf(var) + 1e-5f)));
}
__device__ __forceinline__ NOPK float ln_apply(float x, float mb, float rden) { return rbf(rbf(x - mb) * rden); }

#define SG(i) {}
#define SGDECL
#define SGDUMP(n)
#define TLB()
#define TLE(off)
#define MARK(n) { if (PT == (n)) tmark = __builtin_amdgcn_s_memrealtime(); }
#define PADEND() { const unsigned long long g_ = tmark - tstart, t0_ = __builtin_amdgcn_s_memrealtime(); while (__builtin_amdgcn_s_memrealtime() - t0_ < 4ull * g_) __builtin_amdgcn_s_sleep(1); }
struct Params {
    const u16 *x, *Are, *Aim, *Bre, *Bim, *Cre, *Cim, *logdt, *Dskip, *w1, *b1, *w2;
    u16* out;
    char* ws;
};

typedef float f16v __attribute__((ext_vector_type(16)));
__device__ __forceinline__ NOPK f16v mfma32_bf(u4v a, u4v b, f16v c) {
    return __builtin_amdgcn_mfma_f32_32x32x16_bf16(__builtin_bit_cast(b8v, a), __builtin_bit_cast(b8v, b), c, 0, 0, 0);
}
#define STATV 0
#define VMWAIT(N) asm volatile("s_waitcnt vmcnt(" #N ") lgkmcnt(0)" ::: "memory")

#define RAWBAR() { asm volatile("s_waitcnt lgkmcnt(0)" ::: "memory"); __builtin_amdgcn_s_barrier(); asm volatile("" ::: "memory"); }

__device__ __forceinline__ NOPK void gemm1_role(const Params& p, int b, char* smem) {
    const int tid = threadIdx.x, lane = tid & 63;
    const int w = __builtin_amdgcn_readfirstlane(tid >> 6);
    const int wt = w >> 2, wf = w & 3;
    const int xcd = b & 7, j = b >> 3;
    const int mb = 4 * (xcd >> 1) + (j & 3), nt = 7 * (xcd & 1) + (j >> 2);
    const int l31 = lane & 31, hh = lane >> 5;
    const bool half = (nt == 13);
    const unsigned long long tstart = __builtin_amdgcn_s_memrealtime();
    unsigned long long tmark = tstart;
    u32 xo[2], wo;
#pragma unroll
    for (int i = 0; i < 2; ++i) {
        int P = (2 * w + i) * 64 + lane, row = P >> 3, ch = (P & 7) ^ ((row >> 1) & 7);
        xo[i] = (u32)(row * (D * 2) + ch * 16);
    }
    {
        int P = w * 64 + lane, k = P >> 4, ch = (P & 15) ^ ((k & 3) << 2);
        if (half) ch &= 7;
        wo = (u32)(k * (F * 2) + ch * 16);
    }
    const char* xb = (const char*)p.x + (size_t)mb * 128 * (D * 2);
    const char* wb = (const char*)p.w1 + nt * 256;
    const u32 lx = lds_addr(smem) + w * 2048, lw = lds_addr(smem) + 49152 + w * 1024;
#define SROT(st) (st)
#define G1_X(st) { const char* s_ = xb + SROT(st) * 128; GLDS16(lx + ((st) % 3) * 16384, xo[0], s_); GLDS16(lx + ((st) % 3) * 16384 + 1024, xo[1], s_); }
#define G1_W(h) { const char* s_ = wb + (size_t)(SROT((h) >> 1) * 64 + ((h)&1) * 32) * (F * 2); GLDS16(lw + ((h)&3) * 8192, wo, s_); }
    G1_W(0) G1_X(0)
    u2v bia[4];
    {
        const u32 bo = (u32)min(nt * 128 + 32 * wf + 4 * hh, F - 28) * 2u;
        asm volatile("global_load_dwordx2 %0, %4, %5\n\tglobal_load_dwordx2 %1, %4, %5 offset:16\n\t"
                     "global_load_dwordx2 %2, %4, %5 offset:32\n\tglobal_load_dwordx2 %3, %4, %5 offset:48"
                     : "=&v"(bia[0]), "=&v"(bia[1]), "=&v"(bia[2]), "=&v"(bia[3]) : "v"(bo), "s"(p.b1) : "memory");
    }
    G1_W(1) G1_X(1) G1_W(2) G1_W(3) G1_X(2)

    const int q = (lane & 15) >> 2, pp = lane & 3, blk = (lane >> 4) & 1;
    const int ro = (wf & 1) ^ wt, ri = wf >> 1;
    const char* xa[4][2];
    const char* wa[2];
#pragma unroll
    for (int u = 0; u < 4; ++u)
#pragma unroll
        for (int i = 0; i < 2; ++i)
            xa[u][i] = smem + (64 * wt + 32 * (i ^ ri) + l31) * 128 + (((2 * (u ^ ro) + hh) ^ ((l31 >> 1) & 7)) << 4);
#pragma unroll
    for (int o = 0; o < 2; ++o)
        wa[o] = smem + 49152 + (16 * (o ^ ro) + 8 * hh + q) * 256 + (((4 * wf + 2 * blk + (pp >> 1)) ^ (q << 2)) << 4) + 8 * (pp & 1);

    f16v acc[2];
#pragma unroll
    for (int i = 0; i < 16; ++i) acc[0][i] = acc[1][i] = 0.f;
    u4v xf[2][2][2], wfr[2][2];
    float s1a = 0.f, s1b = 0.f, s2a = 0.f, s2b = 0.f, csa = 0.f, csb = 0.f;
    SG(1)
#define G1_READ(h, o)                                                                                    \
    {                                                                                                    \
        u2v lo = tr_read(wa[o] + ((h)&3) * 8192), hi = tr_read(wa[o] + ((h)&3) * 8192 + 1024);           \
        wfr[(h)&1][o] = (u4v){lo[0], lo[1], hi[0], hi[1]};                                               \
        xf[(h)&1][o][0] = *(const u4v*)(xa[((h)&1) * 2 + o][0] + (((h) >> 1) % 3) * 16384);              \
        xf[(h)&1][o][1] = *(const u4v*)(xa[((h)&1) * 2 + o][1] + (((h) >> 1) % 3) * 16384);              \
    }
#define G1_M(h, o, i)                                                                                    \
    if ((h) >= 0) {                                                                                      \
        acc[i] = mfma32_bf(wfr[(h)&1][o], xf[(h)&1][o][i], acc[i]);                                      \
        asm volatile("" : "+v"(acc[i]));                                                                 \
    }                                                                                                    \
    __builtin_amdgcn_sched_barrier(0);
#define G1_STAT(h, e)                                                                                    \
    if ((h) >= 0) {                                                                                      \
        const u32 xv_ = xf[(h)&1][0][0][e];                                                              \
        s1a = dot2bf(xv_, 0x3f803f80u, s1a); s2a = dot2bf(xv_, xv_, s2a);                                \
        csa = dot2bf(wfr[(h)&1][0][e], 0x3f803f80u, csa);                                                \
    }
#define G1_PIN() asm volatile("" : "+v"(s1a), "+v"(s1b), "+v"(s2a), "+v"(s2b), "+v"(csa), "+v"(csb)); __builtin_amdgcn_sched_barrier(0);
#define G1_IT(h, NW)                                                                                     \
    {                                                                                                    \
        VMWAIT(NW);                                                                                      \
        __builtin_amdgcn_s_barrier();                                                                    \
        G1_M(h, 0, 0)                                                                                    \
        if ((h) >= 0 && (h) + 4 <= 19) G1_W((h) + 4)                                                     \
        if ((h) >= 1 && ((h)&1) && ((h) - 1) / 2 + 3 <= 9) G1_X(((h) - 1) / 2 + 3)                       \
        G1_M(h, 0, 1)                                                                                    \
        G1_READ((h) + 1, 0) G1_STAT(h, 0) G1_PIN()                                                       \
        G1_M(h, 1, 0)                                                                                    \
        G1_READ((h) + 1, 1) G1_STAT(h, 1) G1_PIN()                                                       \
        G1_M(h, 1, 1)                                                                                    \
        G1_STAT(h, 2) G1_STAT(h, 3) G1_PIN()                                                             \
    }
    G1_IT(-1, 11) MARK(11) G1_IT(0, 6) G1_IT(1, 4) G1_IT(2, 6) G1_IT(3, 4) MARK(12) G1_IT(4, 6) G1_IT(5, 4) G1_IT(6, 6) G1_IT(7, 4)
    G1_IT(8, 6) MARK(13) G1_IT(9, 4) G1_IT(10, 6) G1_IT(11, 4) G1_IT(12, 6) G1_IT(13, 4) MARK(14) G1_IT(14, 6) G1_IT(15, 4)
    G1_IT(16, 4) G1_IT(17, 1) G1_IT(18, 0)
    G1_M(19, 0, 0) G1_M(19, 0, 1) G1_M(19, 1, 0) G1_M(19, 1, 1) G1_STAT(19, 0) G1_STAT(19, 1) G1_STAT(19, 2) G1_STAT(19, 3)
    s1a += s1b; s2a += s2b; csa += csb;
    MARK(15)
    asm volatile("" : "+v"(bia[0]), "+v"(bia[1]), "+v"(bia[2]), "+v"(bia[3]));
    char* rsb = smem + 16384;
    char* csl = smem + 16384 + 2048;
    {
        const float a1 = s1a + swap32(s1a), a2 = s2a + swap32(s2a), ac = csa + swap32(csa);
        if (hh == 0) *(f2v*)(rsb + ((64 * wt + 32 * ri + l31) * 2 + (wf & 1)) * 8) = (f2v){a1, a2};
        else *(float*)(csl + ((32 * wf + l31) * 2 + wt) * 4) = ac;
    }
    RAWBAR()
    SG(6)
    if (!(half && wf >= 2)) {
        float csv[16], bsv[16];
#pragma unroll
        for (int g = 0; g < 4; ++g) {
            const f4v c0 = *(const f4v*)(csl + (32 * wf + 8 * g + 4 * hh) * 8), c1 = *(const f4v*)(csl + (32 * wf + 8 * g + 4 * hh) * 8 + 16);
            csv[4 * g] = c0[0] + c0[1]; csv[4 * g + 1] = c0[2] + c0[3]; csv[4 * g + 2] = c1[0] + c1[1]; csv[4 * g + 3] = c1[2] + c1[3];
            bsv[4 * g] = bflo(bia[g][0]) * 1.44269504f; bsv[4 * g + 1] = bfhi(bia[g][0]) * 1.44269504f;
            bsv[4 * g + 2] = bflo(bia[g][1]) * 1.44269504f; bsv[4 * g + 3] = bfhi(bia[g][1]) * 1.44269504f;
        }
        char* actb = p.ws + WS_ACT + ((size_t)(mb * 2 + wt) * KS2 + nt * 4 + wf) * 4096 + (size_t)((l31 >> 4) * 1024 + ((l31 & 15) + 16 * hh) * 16);
#pragma unroll
        for (int i = 0; i < 2; ++i) {
            const f4v u0 = *(const f4v*)(rsb + (64 * wt + 32 * (i ^ ri) + l31) * 16);
            float mean, den;
            ln_stats(u0[0] + u0[2], u0[1] + u0[3], mean, den);
            const float rdl = __builtin_amdgcn_rcpf(den) * 1.44269504f;
#pragma unroll
            for (int c = 0; c < 2; ++c) {
                u4v o;
#pragma unroll
                for (int e = 0; e < 4; ++e) {
                    float r[2];
#pragma unroll
                    for (int z = 0; z < 2; ++z) {
                        const int ix = 8 * c + 2 * e + z;
                        const float u = fmaf(fmaf(-mean, csv[ix], acc[i][ix]), rdl, bsv[ix]);
                        const float e1 = __builtin_amdgcn_exp2f(u) + 1.0f;
                        r[z] = u * fmaf(__builtin_amdgcn_rcpf(fmaf(e1, e1, 1.0f)), -1.38629436f, 0.69314718f);
                    }
                    o[e] = pkbf(r[0], r[1]);
                }
                st16(actb + (i ^ ri) * 2048 + c * 512, o);
            }
        }
    }
    MARK(16)
    PADEND()
}

__device__ __forceinline__ NOPK void ln_role(const Params& p, int rb, char* smem) {
    const int tid = threadIdx.x, lane = tid & 63;
    const int w = __builtin_amdgcn_readfirstlane(tid >> 6);
    const int rl = 4 * w + (lane >> 4), t = rb * 32 + rl, c15 = lane & 15;
    u4v v[5];
#pragma unroll
    for (int i = 0; i < 5; ++i) v[i] = *(const u4v*)(p.x + (size_t)t * D + (c15 + 16 * i) * 8);
    float s = 0.f;
#pragma unroll
    for (int i = 0; i < 5; ++i)
#pragma unroll
        for (int e = 0; e < 4; ++e) s += bflo(v[i][e]) + bfhi(v[i][e]);
    const float mean = red16(s) * (1.0f / D);
    float qv = 0.f;
#pragma unroll
    for (int i = 0; i < 5; ++i)
#pragma unroll
        for (int e = 0; e < 4; ++e) {
            float a = bflo(v[i][e]) - mean, c = bfhi(v[i][e]) - mean;
            qv = fmaf(a, a, qv); qv = fmaf(c, c, qv);
        }
    const float var = red16(qv) * (1.0f / D);
    const float mbf = rbf(mean);
    const float rd = 1.0f / rbf(__builtin_sqrtf(rbf(rbf(var) + 1e-5f)));
#pragma unroll
    for (int i = 0; i < 5; ++i) {
        u2v o0, o1;
        o0[0] = pkh(ln_apply(bflo(v[i][0]), mbf, rd), ln_apply(bfhi(v[i][0]), mbf, rd));
        o0[1] = pkh(ln_apply(bflo(v[i][1]), mbf, rd), ln_apply(bfhi(v[i][1]), mbf, rd));
        o1[0] = pkh(ln_apply(bflo(v[i][2]), mbf, rd), ln_apply(bfhi(v[i][2]), mbf, rd));
        o1[1] = pkh(ln_apply(bflo(v[i][3]), mbf, rd), ln_apply(bfhi(v[i][3]), mbf, rd));
        *(u2v*)(smem + rl * 1288 + (c15 + 16 * i) * 16) = o0;
        *(u2v*)(smem + rl * 1288 + (c15 + 16 * i) * 16 + 8) = o1;
    }
    RAWBAR()
    const int G = lane >> 4, q = c15 >> 2, pp = lane & 3;
    const char* src = smem + (16 * (G >> 1) + 4 * q + 2 * (G & 1)) * 1288 + pp * 8;
    char* dst = p.ws + WS_REC + (size_t)c15 * REC + REC_X + (size_t)rb * 64 + (G ^ ((rb >> 2) & 1)) * 16;
#pragma unroll
    for (int it = 0; it < 5; ++it) {
        const int cg = w * 5 + it;
        u2v lo = tr_read(src + cg * 32), hi = tr_read(src + cg * 32 + 1288);
        st16(dst + (size_t)cg * 16 * REC, (u4v){lo[0], lo[1], hi[0], hi[1]});
    }
}

struct cplx2 { f2v r, i; };
__device__ __forceinline__ NOPK cplx2 cmul(cplx2 a, cplx2 b) {
    cplx2 o;
    o.r = a.r * b.r - a.i * b.i;
    o.i = a.r * b.i + a.i * b.r;
    return o;
}
struct cplxh { h2v r, i; };
__device__ __forceinline__ NOPK cplxh toh(cplx2 a) { return {__builtin_convertvector(a.r, h2v), __builtin_convertvector(a.i, h2v)}; }
__device__ __forceinline__ NOPK cplx2 cexp2(f2v zr, f2v zi, float c) {
    cplx2 o;
#pragma unroll
    for (int m = 0; m < 2; ++m) {
        float mag = __builtin_amdgcn_exp2f(zr[m] * c * 1.44269504089f);
        float ph = zi[m] * c;
        const float CH = 0.15915494f, CL = 6.4206382e-9f;
        float hi = ph * CH;
        float lo = fmaf(ph, CH, -hi) + ph * CL;
        float fr = __builtin_amdgcn_fractf(hi) + lo;
        o.r[m] = mag * __builtin_amdgcn_cosf(fr);
        o.i[m] = mag * __builtin_amdgcn_sinf(fr);
    }
    return o;
}
static constexpr int KG_ROW = 544;

__device__ __forceinline__ NOPK void kgen_role(const Params& p, int d, int part, char* sw  ) {
    const int lane = threadIdx.x & 63, l15 = lane & 15, q4 = lane >> 4;
    const int o = d * NS + 2 * lane;
    u32 are = *(const u32*)(p.Are + o), aim = *(const u32*)(p.Aim + o), bre = *(const u32*)(p.Bre + o);
    u32 bim = *(const u32*)(p.Bim + o), cre = *(const u32*)(p.Cre + o), cim = *(const u32*)(p.Cim + o);
    float dt = rbf(expf(bf2f(p.logdt[d])));
    f2v zr = {dt * bflo(are), dt * bfhi(are)}, zi = {dt * bflo(aim), dt * bfhi(aim)};
    cplx2 dB = {{dt * bflo(bre), dt * bfhi(bre)}, {dt * bflo(bim), dt * bfhi(bim)}};
    cplx2 C = {{bflo(cre), bfhi(cre)}, {bflo(cim), bfhi(cim)}};
    cplx2 W = cmul(C, dB);
    float wm = fmaxf(fmaxf(fabsf(W.r[0]), fabsf(W.r[1])), fmaxf(fabsf(W.i[0]), fabsf(W.i[1])));
    const float dsk = bf2f(p.Dskip[d]);
    wm = fmaxf(max64(wm), fabsf(dsk) * (1.0f / 128.0f));
    int eU = 14 - __builtin_amdgcn_frexp_expf(wm);
    eU = min(max(eU, -100), 100);
    const float sU = __builtin_amdgcn_ldexpf(1.0f, eU);
    W.r *= sU; W.i *= sU;
    char* rec = p.ws + WS_REC + (size_t)d * REC;
    if (part == 0 && lane < 34) {
        u4v z = {0u, 0u, 0u, 0u};
        if (lane == 0) z[0] = f2u(__builtin_amdgcn_ldexpf(1.0f, 8 - eU));
        *(u4v*)(rec + (lane < 2 ? lane * 16 : 4160 + (lane - 2) * 16)) = z;
    }
    const float KSC = 2.384185791015625e-07f;

    char* r0 = sw;
    char* kc = sw + 16 * KG_ROW;
#define KG_PUT(row, v) *(u2v*)(r0 + (row)*KG_ROW + lane * 8) = (u2v){pkh((v).r[0], (v).r[1]), pkh((v).i[0], (v).i[1])}
#define KG_PUTH(row, T_, E_)                                                                            \
    *(u2v*)(r0 + (row)*KG_ROW + lane * 8) = (u2v){__builtin_bit_cast(u32, __builtin_elementwise_fma(T_.r, E_.r, -(T_.i * E_.i))), \
                                                  __builtin_bit_cast(u32, __builtin_elementwise_fma(T_.r, E_.i, T_.i * E_.r))}
#define KG_MMA(A)                                         \
    A = (f4v){0.f, 0.f, 0.f, 0.f};                        \
    _Pragma("unroll") for (int ks = 0; ks < 8; ++ks) A = mfma_h(*(const u4v*)(r0 + l15 * KG_ROW + ks * 64 + q4 * 16), vf[ks], A);
    const cplx2 G = cexp2(zr, zi, 128.f);
    u16* kch = (u16*)kc;
    char* kx = rec + REC_K;
    u4v vf[8];
    if (part == 0) {
        {
            cplx2 v1 = cexp2(zr, zi, 8.f);
            cplx2 v = {{16384.f, 16384.f}, {0.f, 0.f}};
#pragma unroll
            for (int jj = 0; jj < 16; ++jj) {
                cplx2 t = {v.r, -v.i};
                KG_PUT(jj, t);
                if (jj < 15) v = cmul(v, v1);
            }
        }
        RAWBAR()
#pragma unroll
        for (int ks = 0; ks < 8; ++ks) vf[ks] = *(const u4v*)(r0 + l15 * KG_ROW + ks * 64 + q4 * 16);
        cplx2 E[8];
        E[1] = cexp2(zr, zi, 1.f);
        E[2] = cmul(E[1], E[1]); E[3] = cmul(E[2], E[1]); E[4] = cmul(E[2], E[2]);
        E[5] = cmul(E[4], E[1]); E[6] = cmul(E[4], E[2]); E[7] = cmul(E[4], E[3]);
        const cplxh T0 = toh(W), T1 = toh(cmul(W, G));
        RAWBAR()
        *(u2v*)(r0 + lane * 8) = (u2v){__builtin_bit_cast(u32, T0.r), __builtin_bit_cast(u32, T0.i)};
        *(u2v*)(r0 + 8 * KG_ROW + lane * 8) = (u2v){__builtin_bit_cast(u32, T1.r), __builtin_bit_cast(u32, T1.i)};
#pragma unroll
        for (int c = 1; c < 8; ++c) {
            const cplxh Eh = toh(E[c]);
            KG_PUTH(c, T0, Eh);
            KG_PUTH(8 + c, T1, Eh);
        }
        f4v acc;
        KG_MMA(acc)
        if (lane == 0) acc[0] = fmaf(dsk * sU, 16384.f, acc[0]);
        *(u2v*)(kx + (128 * (q4 >> 1) + 4 * (q4 & 1) + 8 * l15) * 2) = (u2v){pkh(acc[0] * KSC, acc[1] * KSC), pkh(acc[2] * KSC, acc[3] * KSC)};
    } else {
        const cplx2 E2 = cexp2(zr, zi, 2.f);
        const cplx2 E4 = cmul(E2, E2);
        const cplxh E2h = toh(E2), E4h = toh(E4), E6h = toh(cmul(E4, E2));
        cplx2 T = cmul(W, cexp2(zr, zi, 256.f));
#pragma unroll
        for (int a = 0; a < 6; ++a) {
            const cplxh Th = toh(T);
            const int row = a < 2 ? 4 * a : 4 + 2 * a;
            *(u2v*)(r0 + row * KG_ROW + lane * 8) = (u2v){__builtin_bit_cast(u32, Th.r), __builtin_bit_cast(u32, Th.i)};
            if (a < 2) { KG_PUTH(row + 1, Th, E2h); KG_PUTH(row + 2, Th, E4h); KG_PUTH(row + 3, Th, E6h); }
            else KG_PUTH(row + 1, Th, E4h);
            if (a < 5) T = cmul(T, G);
        }
        RAWBAR()
#pragma unroll
        for (int ks = 0; ks < 8; ++ks) vf[ks] = *(const u4v*)(r0 - 10240 + l15 * KG_ROW + ks * 64 + q4 * 16);
        RAWBAR()
        f4v acc1, acc2;
        KG_MMA(acc1)
        T = cmul(W, cexp2(zr, zi, 1024.f));
#pragma unroll
        for (int a = 0; a < 9; ++a) {
            KG_PUT(a, T);
            if (a < 8) T = cmul(T, G);
        }
        KG_MMA(acc2)
        {
            u32 h01 = pkh(acc1[0] * KSC, acc1[1] * KSC), h23 = pkh(acc1[2] * KSC, acc1[3] * KSC);
            if (q4 < 2) {
                *(u2v*)(kc + (64 * q4 + 4 * l15) * 2) = (u2v){h01, h23};
            } else {
                *(u32*)(kc + (128 + 64 * (q4 - 2) + 2 * l15) * 2) = h01;
                *(u32*)(kc + (128 + 64 * (q4 - 2) + 32 + 2 * l15) * 2) = h23;
            }
            u32 g01 = pkh(acc2[0] * KSC, acc2[1] * KSC), g23 = pkh(acc2[2] * KSC, acc2[3] * KSC);
            if (q4 < 2) {
                kch[256 + 64 * q4 + l15] = (u16)g01;
                kch[256 + 64 * q4 + 16 + l15] = (u16)(g01 >> 16);
                kch[256 + 64 * q4 + 32 + l15] = (u16)g23;
                kch[256 + 64 * q4 + 48 + l15] = (u16)(g23 >> 16);
            } else if (lane == 32) {
                kch[384] = (u16)g01;
            }
        }
        if (lane < 32) {
            u2v s = *(const u2v*)(kc + lane * 8);
            u32 s4 = kch[lane * 4 + 4];
            u4v b2;
            b2[0] = (s[0] & 0xffffu) | (s[0] << 16);
            b2[1] = (s[0] >> 16) | (s[1] << 16);
            b2[2] = (s[1] & 0xffffu) | (s[1] << 16);
            b2[3] = (s[1] >> 16) | (s4 << 16);
            st16(kx + 512 + lane * 16, b2);
        }
        {
            u32 s = *(const u32*)(kc + 256 + lane * 4);
            u32 c = kch[128 + lane * 2 + 2];
            u4v v;
            v[0] = (s & 0xffffu) | (s << 16);
            v[1] = s;
            v[2] = (s >> 16) | (s & 0xffff0000u);
            v[3] = c | (c << 16);
            st16(kx + 1024 + lane * 16, v);
        }
#pragma unroll
        for (int it = 0; it < 2; ++it) {
            int oc = lane + 64 * it;
            u32 a = kch[256 + oc], c = kch[257 + oc];
            u4v v;
            v[0] = a | (a << 16);
            v[1] = v[0];
            v[2] = ((oc & 1) ? c : a) | (c << 16);
            v[3] = c | (c << 16);
            st16(kx + 2048 + oc * 16, v);
        }
    }
}

__device__ __forceinline__ NOPK void w2pack_role(const Params& p, int unit, char* sw) {
    const int lane = threadIdx.x & 63, l15 = lane & 15, q4 = lane >> 4;
    const int nb = unit / KS2, ks = unit - nb * KS2;
#pragma unroll
    for (int i = 0; i < 5; ++i) {
        int c = lane + 64 * i, row = c / 10, ch = c - row * 10;
        *(u4v*)(sw + c * 16) = *(const u4v*)(p.w2 + (size_t)(ks * 32 + row) * D + nb * 80 + ch * 8);
    }
    const int q = l15 >> 2, pp = lane & 3;
    char* dst = p.ws + WS_W2P + (size_t)unit * 5120 + lane * 16;
    const char* src = sw + (16 * (q4 >> 1) + 4 * (q4 & 1) + q) * 160 + pp * 8;
#pragma unroll
    for (int dt = 0; dt < 5; ++dt) {
        u2v lo = tr_read(src + dt * 32);
        u2v hi = tr_read(src + 8 * 160 + dt * 32);
        st16(dst + dt * 1024, (u4v){lo[0], lo[1], hi[0], hi[1]});
    }
}

__global__ __launch_bounds__(512, 4) NOPK void k1(Params p) {
    extern __shared__ __attribute__((aligned(16))) char smem[];
    const int b = blockIdx.x;
    const int w = __builtin_amdgcn_readfirstlane(threadIdx.x >> 6);
    TLB()
    if (b < 224) {
        gemm1_role(p, b, smem);
    } else if (b < 438) {
        const int d = (b - 224) * 3 + (w >> 1);
        if (w < 6 && d < D) {
            kgen_role(p, d, w & 1, smem + w * 10240);
        } else {
            if (w >= 6) {
                const int unit = (b - 224) * 2 + (w - 6);
                w2pack_role(p, unit, smem + w * 10240);
                if (unit < 4) w2pack_role(p, 428 + unit, smem + w * 10240);
                if (b == 437 && (threadIdx.x & 63) < 8 && w == 7) *(u32*)(p.ws + WS_CNT + (threadIdx.x & 63) * 256) = 0u;
            }
            __builtin_amdgcn_s_barrier();
            __builtin_amdgcn_s_barrier();
        }
    } else {
        ln_role(p, b - 438, smem);
    }
    TLE(0)
}

static constexpr int CV_WAVE = 9216;
static constexpr int K2_RED = 3 * CV_WAVE;
static constexpr int K2_YS = K2_RED + 61440;
static constexpr int K2_LDS = K2_YS + 10240;

#define AC0 "a"
#define AC1 "a"
#define AC2 "a"
#define AC3 "v"
#define AC4 "v"
#define AC5 "v"
#define G2_BUF(C, i) C AC##i(af[i][0]), C AC##i(af[i][1]), C AC##i(af[i][2]), C AC##i(af[i][3]), C "a"(wf[i][0]), C "a"(wf[i][1]), C "a"(wf[i][2]), C "a"(wf[i][3]), C "a"(wf[i][4])
#define G2_LOAD(i)                                                                                                   \
    asm volatile("buffer_load_dwordx4 %0, %9, %15, %17 offen\n\tbuffer_load_dwordx4 %4, %13, %16, %18 offen\n\t"     \
                 "buffer_load_dwordx4 %1, %10, %15, %17 offen\n\tbuffer_load_dwordx4 %2, %11, %15, %17 offen\n\t"    \
                 "buffer_load_dwordx4 %3, %12, %15, %17 offen\n\tbuffer_load_dwordx4 %5, %13, %16, %18 offen offset:1024\n\t" \
                 "buffer_load_dwordx4 %6, %13, %16, %18 offen offset:2048\n\tbuffer_load_dwordx4 %7, %13, %16, %18 offen offset:3072\n\t" \
                 "buffer_load_dwordx4 %8, %14, %16, %18 offen"                                                       \
                 : G2_BUF("=&", i) : "v"(ao[0]), "v"(ao[1]), "v"(ao[2]), "v"(ao[3]), "v"(ao[4]), "v"(ao[5]), "s"(ra), "s"(rw), \
                   "s"((i)*4096), "s"((i)*5120) : "memory");
#define G2_MF(d, w_, a_) "v_mfma_f32_16x16x32_bf16 %" #d ", %" #w_ ", %" #a_ ", %" #d "\n\t"
#define G2_M4(dt, i, VO, OFF)                                                                           \
    asm volatile(G2_MF(0, 4, 5) G2_MF(1, 4, 6) G2_MF(2, 4, 7) G2_MF(3, 4, 8)                            \
                 "buffer_load_dwordx4 %4, %9, %10, %11 offen offset:" #OFF                              \
                 : "+a"(acc2[dt][0]), "+a"(acc2[dt][1]), "+a"(acc2[dt][2]), "+a"(acc2[dt][3]), "+a"(wf[i][dt]) \
                 : AC##i(af[i][0]), AC##i(af[i][1]), AC##i(af[i][2]), AC##i(af[i][3]), "v"(ao[VO]), "s"(rw), "s"(sw) : "memory");
#define G2_BODY(i)                                                                                      \
    {                                                                                                   \
        G2_M4(0, i, 4, 0) G2_M4(1, i, 4, 1024) G2_M4(2, i, 4, 2048) G2_M4(3, i, 4, 3072) G2_M4(4, i, 5, 0) \
        asm volatile("buffer_load_dwordx4 %0, %4, %8, %9 offen\n\tbuffer_load_dwordx4 %1, %5, %8, %9 offen\n\t" \
                     "buffer_load_dwordx4 %2, %6, %8, %9 offen\n\tbuffer_load_dwordx4 %3, %7, %8, %9 offen" \
                     : "+" AC##i(af[i][0]), "+" AC##i(af[i][1]), "+" AC##i(af[i][2]), "+" AC##i(af[i][3])                   \
                     : "v"(ao[0]), "v"(ao[1]), "v"(ao[2]), "v"(ao[3]), "s"(ra), "s"(sa) : "memory");    \
    }
#define G2_STEP(i) { asm volatile("s_waitcnt vmcnt(45)" : G2_BUF("+", i)::"memory"); G2_BODY(i) sa += 4096; sw += 5120; }
#define G2_M4N(dt, i)                                                                                   \
    asm volatile(G2_MF(0, 4, 5) G2_MF(1, 4, 6) G2_MF(2, 4, 7) G2_MF(3, 4, 8) ""                         \
                 : "+a"(acc2[dt][0]), "+a"(acc2[dt][1]), "+a"(acc2[dt][2]), "+a"(acc2[dt][3])           \
                 : "a"(wf[i][dt]), AC##i(af[i][0]), AC##i(af[i][1]), AC##i(af[i][2]), AC##i(af[i][3]));
#define G2_SSTEP(k, i)                                                                                  \
    {                                                                                                   \
        if constexpr ((k) < 2) {                                                                        \
            const u32 sa = ((k) + 6) * 4096, sw = ((k) + 6) * 5120;                                     \
            G2_BODY(i)                                                                                  \
        } else {                                                                                        \
            G2_M4N(0, i) G2_M4N(1, i) G2_M4N(2, i) G2_M4N(3, i) G2_M4N(4, i)                            \
        }                                                                                               \
        __builtin_amdgcn_sched_barrier(0);                                                              \
    }
#define G2_ACC(a0) "+a"(acc2[a0][0]), "+a"(acc2[a0][1]), "+a"(acc2[a0][2]), "+a"(acc2[a0][3])
#define G2_ZERO()                                                                                       \
    _Pragma("unroll") for (int dt = 0; dt < 5; ++dt) _Pragma("unroll") for (int tt = 0; tt < 4; ++tt) acc2[dt][tt] = (f4v){0.f, 0.f, 0.f, 0.f}; \
    asm volatile("s_nop 1" : G2_ACC(0), G2_ACC(1), G2_ACC(2), G2_ACC(3), G2_ACC(4));
#undef STOFF
#define STOFF (4 << 20)
__device__ __forceinline__ NOPK void conv_wave(const Params& p, int d, u32 lb, char* img, u4v (&af)[6][4], u4v (&wf)[6][5],
                                                f4v (&acc2)[5][4], u4v ra, u4v rw, const u32 (&ao)[6], u32 svc, const u32* cnt,
                                                const char* yb, u32 ysl, unsigned long long& tmark) {
    const int lane = threadIdx.x & 63, l15 = lane & 15, q4 = lane >> 4;
    const int q = l15 >> 2, pp = lane & 3;
    const u32 kra = lb + REC_K + (32 * q4 - 4 * q + 4 * pp) * 2;
    u32 xra[2][2][2];
#pragma unroll
    for (int ap = 0; ap < 2; ++ap)
#pragma unroll
        for (int h = 0; h < 2; ++h)
#pragma unroll
            for (int g = 0; g < 2; ++g) {
                int G = l15 + 8 * g - 2 * q4 - h + 8;
                xra[ap][h][g] = lb + 4160 + G * 32 + ((ap ^ ((G >> 3) & 1)) * 16);
            }
    const u32 csrc = (lane >= 16 ? lane - 16 : (lane == 0 ? 63 : lane + 47)) * 4;
    asm volatile("s_waitcnt vmcnt(5)" ::: "memory");
    MARK(1)
    u4v kf[16], xf[4];
    f4v acc[8];
    float Y[8][7];
    float scale;
    asm volatile("ds_read_b32 %0, %1" : "=v"(scale) : "v"(lb));
#define CV_XRD(s)                                                                                          \
    {                                                                                                      \
        u2v v0, v1;                                                                                        \
        asm volatile("ds_read_b64 %0, %2 offset:%4\n\tds_read_b64 %1, %3 offset:%4" : "=&v"(v0), "=&v"(v1) \
                     : "v"(xra[(s) >> 5][0][(s)&1]), "v"(xra[(s) >> 5][1][(s)&1]), "i"((((s)&15) >> 1) * 512 + (((s) >> 4) & 1) * 8)); \
        xf[(s)&3] = (u4v){v0[0], v0[1], v1[0], v1[1]};                                                     \
    }
    CV_XRD(0) CV_XRD(1) CV_XRD(2)
#pragma unroll
    for (int e = 0; e < 16; ++e) {
        u2v lo, hi;
        asm volatile("ds_read_b64_tr_b16 %0, %2 offset:%3\n\tds_read_b64_tr_b16 %1, %2 offset:%4" : "=&v"(lo), "=&v"(hi)
                     : "v"(kra), "i"(e * 256), "i"(e * 256 + 32));
        kf[e] = (u4v){lo[0], lo[1], hi[0], hi[1]};
    }
    asm volatile("s_waitcnt lgkmcnt(0)" : "+v"(kf[0]), "+v"(kf[1]), "+v"(kf[2]), "+v"(kf[3]), "+v"(kf[4]), "+v"(kf[5]), "+v"(kf[6]), "+v"(kf[7]),
                 "+v"(kf[8]), "+v"(kf[9]), "+v"(kf[10]), "+v"(kf[11]), "+v"(kf[12]), "+v"(kf[13]), "+v"(kf[14]), "+v"(kf[15]), "+v"(scale));
    MARK(2)
#define CV_STEP(s)                                                                                         \
    {                                                                                                      \
        constexpr int a = (s) >> 4, g = (s)&15;                                                            \
        if constexpr ((s) + 3 < 64) CV_XRD(((s) + 3) & 63)                                                 \
          \
        _Pragma("unroll") for (int e = (g + 1) & 1; e + g <= 15; e += 2) {                                 \
            if constexpr (g == 0) {                                                                        \
                if (e < 2) asm volatile("s_waitcnt lgkmcnt(6)\n\tv_mfma_f32_16x16x32_f16 %0, %1, %2, 0" : "=&v"(acc[(e + g) >> 1]) : "v"(kf[e]), "v"(xf[(s)&3])); \
                else asm volatile("v_mfma_f32_16x16x32_f16 %0, %1, %2, 0" : "=&v"(acc[(e + g) >> 1]) : "v"(kf[e]), "v"(xf[(s)&3])); \
            } else {                                                                                       \
                if (e < 2) asm volatile("s_waitcnt lgkmcnt(%3)\n\tv_mfma_f32_16x16x32_f16 %0, %1, %2, %0" : "+v"(acc[(e + g) >> 1]) : "v"(kf[e]), "v"(xf[(s)&3]), "i"((s) + 3 < 64 ? 6 : 2 * (63 - (s)))); \
                else asm volatile("v_mfma_f32_16x16x32_f16 %0, %1, %2, %0" : "+v"(acc[(e + g) >> 1]) : "v"(kf[e]), "v"(xf[(s)&3])); \
            }                                                                                              \
        }                                                                                                  \
        if constexpr (g == 15) {      \
            asm volatile("s_nop 7\n\ts_nop 3" : "+v"(acc[0]), "+v"(acc[1]), "+v"(acc[2]), "+v"(acc[3]), "+v"(acc[4]), "+v"(acc[5]), "+v"(acc[6]), "+v"(acc[7])); \
            _Pragma("unroll") for (int t = 0; t < 8; ++t) _Pragma("unroll") for (int r = 0; r < 4; ++r)    \
                Y[t][r + a] = (a == 0 || r == 3) ? acc[t][r] : Y[t][r + a] + acc[t][r];                    \
            __builtin_amdgcn_sched_barrier(0);                                                             \
        }                                                                                                  \
    }
#define CV_S4(s) CV_STEP(s) CV_STEP((s) + 1) CV_STEP((s) + 2) CV_STEP((s) + 3)
#define CV_S16(s) CV_S4(s) CV_S4((s) + 4) CV_S4((s) + 8) CV_S4((s) + 12)
#define CV_S8(s) CV_S4(s) CV_S4((s) + 4)
    CV_S16(0) G2_LOAD(0) CV_S8(16) G2_LOAD(1) CV_S8(24) G2_LOAD(2) CV_S8(32) G2_LOAD(3) CV_S8(40) G2_LOAD(4) CV_S8(48) G2_LOAD(5) CV_S8(56)
    MARK(3)
    float cr[8][3];
#pragma unroll
    for (int t = 0; t < 8; ++t)
#pragma unroll
        for (int r = 0; r < 3; ++r)
            cr[t][r] = __builtin_bit_cast(float, __builtin_amdgcn_ds_bpermute(csrc, __builtin_bit_cast(int, Y[t][4 + r])));
    __builtin_amdgcn_sched_barrier(0);
#pragma unroll
    for (int t = 0; t < 8; ++t) {
        float y[4];
#pragma unroll
        for (int r = 0; r < 3; ++r) y[r] = Y[t][r] + (lane == 0 ? (t ? cr[t - (t > 0)][r] : 0.f) : cr[t][r]);
        y[3] = Y[t][3];
        *(u2v*)(img + t * 512 + (16 * l15 + 4 * q4) * 2) = (u2v){pkbf(y[0] * scale, y[1] * scale), pkbf(y[2] * scale, y[3] * scale)};
    }
    char* dst = p.ws + WS_YT + (size_t)d * 4096 + lane * 16;
    {
        u4v o[4];
#pragma unroll
        for (int i = 0; i < 4; ++i) o[i] = *(const u4v*)(img + i * 1024 + lane * 16);
#pragma unroll
        for (int i = 0; i < 4; ++i)
            asm volatile("global_store_dwordx4 %0, %1, off offset:%2 sc1\n\ts_nop 1" ::"v"(dst), "v"(o[i]), "i"(i * 1024) : "memory");
    }
    G2_ZERO()
    asm volatile("s_waitcnt vmcnt(0)\n\ts_mov_b64 exec, 1\n\tglobal_atomic_add %0, %1, %2\n\ts_mov_b64 exec, -1"
                 ::"v"(0u), "v"(1u), "s"(p.ws + WS_CNT + (d / 80) * 256) : "memory");
    MARK(4)
    asm volatile("" : G2_BUF("+", 0), G2_BUF("+", 1), G2_BUF("+", 2)::"memory");
    asm volatile("" : G2_BUF("+", 3), G2_BUF("+", 4), G2_BUF("+", 5)::"memory");
    G2_SSTEP(0, 0) G2_SSTEP(1, 1) G2_SSTEP(2, 2) G2_SSTEP(3, 3)
    u32 cv = 0;
    asm volatile("s_cmp_lg_u32 %1, 0\n\ts_cbranch_scc1 .Lsv%=\n\tglobal_load_dword %0, %0, %2 sc1\n.Lsv%=:" : "+v"(cv) : "s"(svc), "s"(cnt) : "memory", "scc");
    G2_SSTEP(4, 4) G2_SSTEP(5, 5)
    asm volatile("s_waitcnt vmcnt(0)" : "+v"(cv), G2_BUF("+", 0), G2_BUF("+", 1)::"memory");
    if (svc == 0) {
        unsigned spins = 0;
        while (__builtin_amdgcn_readfirstlane(cv) < 80u && ++spins < (1u << 22)) {
            __builtin_amdgcn_s_sleep(4);
            cv = __hip_atomic_load(cnt, __ATOMIC_RELAXED, __HIP_MEMORY_SCOPE_AGENT);
        }
        const u32 yo = (lane >> 3) * 4096 + (lane & 7) * 16;
#pragma unroll
        for (int i = 0; i < 10; ++i)
            asm volatile("s_mov_b32 m0, %0\n\ts_nop 0\n\tglobal_load_lds_dwordx4 %1, %2 sc1" ::"s"(ysl + i * 1024), "v"(yo), "s"(yb + i * 32768) : "memory");
    }
    G2_SSTEP(6, 0) G2_SSTEP(7, 1)
    asm volatile("s_waitcnt vmcnt(0)\n\ts_nop 7\n\ts_nop 7" : G2_ACC(0), G2_ACC(1), G2_ACC(2), G2_ACC(3), G2_ACC(4)::"memory");
}

__global__ __launch_bounds__(256, 1) NOPK void k2(Params p) {
    extern __shared__ __attribute__((aligned(16))) char smem[];
    const int b = blockIdx.x;
    const int w = __builtin_amdgcn_readfirstlane(threadIdx.x >> 6);
    const unsigned long long tstart = __builtin_amdgcn_s_memrealtime();
    unsigned long long tmark = tstart;
    const int xcd = b & 7, j = b >> 3;
    const int mb = 8 * (xcd >> 1) + (j & 7), nb = 4 * (xcd & 1) + (j >> 3);
    const int nch = (j & 1) ? 2 : 3;
    const int ch0 = xcd * 80 + (j >> 1) * 5 + ((j & 1) ? 3 : 0);
    const int k0 = ((j & 1) && w == 3) ? 34 : 8 * w;
    const int nk = w < nch ? 8 : ((j & 1) ? 2 * w + 14 : 30);
    const char* ab = p.ws + WS_ACT + ((size_t)mb * KS2 + k0) * 4096;
    const char* wb = p.ws + WS_W2P + ((size_t)nb * KS2 + k0) * 5120;
    u4v ra = {(u32)(size_t)ab, (u32)((size_t)ab >> 32), (u32)nk * 4096u, 0x00020000u};
    u4v rw = {(u32)(size_t)wb, (u32)((size_t)wb >> 32), (u32)nk * 5120u, 0x00020000u};
    const u32 lo16 = (threadIdx.x & 63) * 16;
    const u32 lb = lds_addr(smem);
    const u32 ao[6] = {lo16 + (w & 3) * 1024, lo16 + ((w + 1) & 3) * 1024, lo16 + ((w + 2) & 3) * 1024, lo16 + ((w + 3) & 3) * 1024, lo16, lo16 + 4096};
    SGDECL SG(0)
    if (w < nch) {
        const char* src = p.ws + WS_REC + (size_t)(ch0 + w) * REC;
#pragma unroll
        for (int i = 0; i < 9; ++i) GLDS16(lb + w * CV_WAVE + i * 1024, lo16, src + i * 1024);
    }
    u4v af[6][4], wf[6][5];
    u2v xv[5];
    {
        const u16* xp = p.x + (size_t)(mb * 64 + w * 16 + (threadIdx.x & 15)) * D + nb * 80 + ((threadIdx.x >> 4) & 3) * 4;
#pragma unroll
        for (int dt = 0; dt < 5; ++dt) xv[dt] = *(const u2v*)(xp + dt * 16);
    }
    SG(1)
    f4v acc2[5][4];
    if (w < nch) {
        conv_wave(p, ch0 + w, lb + w * CV_WAVE, smem + w * CV_WAVE, af, wf, acc2, ra, rw, ao, (u32)w,
                  (const u32*)(p.ws + WS_CNT + nb * 256), p.ws + WS_YT + (size_t)(nb * 80) * 4096 + (size_t)mb * 128, lb + K2_YS, tmark);
    } else {
        G2_LOAD(0) G2_LOAD(1) G2_LOAD(2) G2_LOAD(3) G2_LOAD(4) G2_LOAD(5)
        G2_ZERO()
        u32 sa = 6 * 4096, sw = 6 * 5120;
#pragma nounroll
        for (int r = 6; r < nk; r += 6) { G2_STEP(0) G2_STEP(1) G2_STEP(2) G2_STEP(3) G2_STEP(4) G2_STEP(5) }
#define G2_LAST(i, NW) { asm volatile("s_waitcnt vmcnt(" #NW ")" : G2_BUF("+", i)::"memory"); G2_SSTEP(2, i) }
        G2_LAST(0, 45) G2_LAST(1, 36) G2_LAST(2, 27) G2_LAST(3, 18) G2_LAST(4, 9) G2_LAST(5, 0)
        asm volatile("s_nop 7\n\ts_nop 7" : G2_ACC(0), G2_ACC(1), G2_ACC(2), G2_ACC(3), G2_ACC(4)::"memory");
    }
    if (w < nch) MARK(5)
    if (w >= nch) MARK(6)
    int lane = threadIdx.x & 63;
    asm volatile("" : "+v"(lane));
    const int l15 = lane & 15, q4 = lane >> 4;
    const int t = mb * 64 + w * 16 + l15;
    const int dbase = nb * 80 + 4 * q4;
    char* red = smem + K2_RED;
#pragma unroll
    for (int tt = 1; tt < 4; ++tt) {
        char* dst = red + (((tt + w) & 3) * 3 + tt - 1) * 5120 + lane * 16;
#pragma unroll
        for (int dt = 0; dt < 5; ++dt) *(f4v*)(dst + dt * 1024) = acc2[dt][tt];
    }
    RAWBAR()
    SG(4)
    const char* ys = smem + K2_YS + (4 * q4 + (l15 >> 2)) * 128 + w * 32 + (lane & 3) * 8;
    const char* rsrc = red + w * 15360 + lane * 16;
    u2v svv[5];
    f4v part[5][3];
#pragma unroll
    for (int dt = 0; dt < 5; ++dt) {
        svv[dt] = tr_read(ys + dt * 2048);
#pragma unroll
        for (int i = 0; i < 3; ++i) part[dt][i] = *(const f4v*)(rsrc + i * 5120 + dt * 1024);
    }
    __builtin_amdgcn_sched_barrier(0);
#pragma unroll
    for (int dt = 0; dt < 5; ++dt) {
        const u2v sv = svv[dt];
        const f4v g = (acc2[dt][0] + part[dt][0]) + (part[dt][1] + part[dt][2]);
        u2v o;
#pragma unroll
        for (int r2 = 0; r2 < 2; ++r2) {
            const float p0 = g[2 * r2], p1 = g[2 * r2 + 1];
            const float g0 = p0 * __builtin_amdgcn_rcpf(1.0f + __builtin_amdgcn_exp2f(-1.44269504f * p0));
            const float g1 = p1 * __builtin_amdgcn_rcpf(1.0f + __builtin_amdgcn_exp2f(-1.44269504f * p1));
            o[r2] = pkbf(fmaf(bflo(sv[r2]), g0, bflo(xv[dt][r2])), fmaf(bfhi(sv[r2]), g1, bfhi(xv[dt][r2])));
        }
        *(u2v*)(p.out + (size_t)t * D + dbase + dt * 16) = o;
    }
    SG(5) SGDUMP(6)
    PADEND()
}

extern "C" void kernel_launch(void* const* d_in, const int* in_sizes, int n_in, void* d_out, int out_size, void* d_ws,
                              size_t ws_size, hipStream_t stream) {
    Params p;
    p.x = (const u16*)d_in[0];
    p.Are = (const u16*)d_in[1]; p.Aim = (const u16*)d_in[2]; p.Bre = (const u16*)d_in[3]; p.Bim = (const u16*)d_in[4];
    p.Cre = (const u16*)d_in[5]; p.Cim = (const u16*)d_in[6]; p.logdt = (const u16*)d_in[7]; p.Dskip = (const u16*)d_in[8];
    p.w1 = (const u16*)d_in[10]; p.b1 = (const u16*)d_in[11]; p.w2 = (const u16*)d_in[12];
    p.out = (u16*)d_out;
    p.ws = (char*)d_ws;
    (void)hipFuncSetAttribute((const void*)k1, hipFuncAttributeMaxDynamicSharedMemorySize, LDS_BYTES);
    (void)hipFuncSetAttribute((const void*)k2, hipFuncAttributeMaxDynamicSharedMemorySize, K2_LDS);
    k1<<<502, 512, LDS_BYTES, stream>>>(p);
    k2<<<256, 256, K2_LDS, stream>>>(p);
}
```

```cpp
#define ACTST 0
#include <hip/hip_runtime.h>
#include <stdint.h>

#if defined(__HIP_DEVICE_COMPILE__) && !defined(ALLOWPK)
#define NOPK __attribute__((target("no-packed-fp32-ops")))
#else
#define NOPK
#endif
typedef unsigned short u16;
typedef unsigned int u32;
typedef short s4v __attribute__((ext_vector_type(4)));
typedef u32 u2v __attribute__((ext_vector_type(2)));
typedef u32 u4v __attribute__((ext_vector_type(4)));
typedef float f2v __attribute__((ext_vector_type(2)));
typedef float f4v __attribute__((ext_vector_type(4)));
typedef _Float16 h2v __attribute__((ext_vector_type(2)));
typedef _Float16 h8v __attribute__((ext_vector_type(8)));
typedef __bf16 b2v __attribute__((ext_vector_type(2)));
typedef __bf16 b8v __attribute__((ext_vector_type(8)));
#define LDSP(T, p) ((__attribute__((address_space(3))) T*)(p))
#define GLB(T, p) ((__attribute__((address_space(1))) T*)(p))

static constexpr int L = 2048, D = 640, NS = 128, F = 1728;
static constexpr int KS2 = F / 32;

static constexpr size_t WS_ACT = 0;
static constexpr size_t WS_W2P = WS_ACT + (size_t)L * F * 2;
static constexpr int REC_K = 32, REC_X = 4160 + 512, REC = 8768;
static constexpr size_t WS_REC = WS_W2P + (size_t)F * D * 2;
static constexpr size_t WS_YT = WS_REC + (size_t)641 * REC + 192;
static constexpr size_t WS_CNT = WS_YT + (size_t)L * D * 2;
static_assert(WS_REC % 256 == 0 && WS_YT % 256 == 0, "alignment");

static constexpr int LDS_BYTES = 80 * 1024;

__device__ __forceinline__ NOPK u32 f2u(float v) { return __builtin_bit_cast(u32, v); }
__device__ __forceinline__ NOPK float bf2f(u32 v) { return __builtin_bit_cast(float, (u32)(v << 16)); }
__device__ __forceinline__ NOPK float bflo(u32 v) { return __builtin_bit_cast(float, (u32)(v << 16)); }
__device__ __forceinline__ NOPK float bfhi(u32 v) { return __builtin_bit_cast(float, (u32)(v & 0xffff0000u)); }
__device__ __forceinline__ NOPK u32 pkbf(float a, float b) {
    f2v v = {a, b};
    return __builtin_bit_cast(u32, __builtin_convertvector(v, b2v));
}
__device__ __forceinline__ NOPK u32 pkh(float a, float b) {
    f2v v = {a, b};
    return __builtin_bit_cast(u32, __builtin_convertvector(v, h2v));
}
__device__ __forceinline__ NOPK float rbf(float a) { return bflo(pkbf(a, a)); }
__device__ __forceinline__ NOPK f4v mfma_bf(u4v a, u4v b, f4v c) {
    return __builtin_amdgcn_mfma_f32_16x16x32_bf16(__builtin_bit_cast(b8v, a), __builtin_bit_cast(b8v, b), c, 0, 0, 0);
}
__device__ __forceinline__ NOPK f4v mfma_h(u4v a, u4v b, f4v c) {
    return __builtin_amdgcn_mfma_f32_16x16x32_f16(__builtin_bit_cast(h8v, a), __builtin_bit_cast(h8v, b), c, 0, 0, 0);
}
__device__ __forceinline__ NOPK u2v tr_read(const char* p) {
    return __builtin_bit_cast(u2v, __builtin_amdgcn_ds_read_tr16_b64_v4i16(LDSP(s4v, p)));
}
__device__ __forceinline__ NOPK float dot2bf(u32 a, u32 b, float c) {
    return __builtin_amdgcn_fdot2_f32_bf16(__builtin_bit_cast(b2v, a), __builtin_bit_cast(b2v, b), c, false);
}
__device__ __forceinline__ NOPK void glds16(const void* g, void* l) {
    __builtin_amdgcn_global_load_lds((const __attribute__((address_space(1))) void*)g,
                                     (__attribute__((address_space(3))) void*)l, 16, 0, 0);
}
__device__ __forceinline__ NOPK u32 lds_addr(const void* l) { return (u32)(size_t)(__attribute__((address_space(3))) const char*)l; }
#define GLDS16(ldsa, voff, sbase) \
    asm volatile("s_mov_b32 m0, %0\n\ts_nop 0\n\tglobal_load_lds_dwordx4 %1, %2" ::"s"(ldsa), "v"(voff), "s"(sbase) : "memory")
#define GLDS4(ldsa, voff, sbase) \
    asm volatile("s_mov_b32 m0, %0\n\ts_nop 0\n\tglobal_load_lds_dword %1, %2" ::"s"(ldsa), "v"(voff), "s"(sbase) : "memory")
template <int CTRL> __device__ __forceinline__ NOPK float dpp(float v) {
    return __builtin_bit_cast(float, __builtin_amdgcn_update_dpp(0, __builtin_bit_cast(int, v), CTRL, 0xf, 0xf, true));
}
__device__ __forceinline__ NOPK float swap32(float v) {
    const u32 b = __builtin_bit_cast(u32, v);
    const auto r = __builtin_amdgcn_permlane32_swap(b, b, false, false);
    return __builtin_bit_cast(float, (threadIdx.x & 32) ? r[0] : r[1]);
}
__device__ __forceinline__ NOPK float red16(float v) {
    v += dpp<0xB1>(v); v += dpp<0x4E>(v); v += dpp<0x141>(v); v += dpp<0x140>(v);
    return v;
}
__device__ __forceinline__ NOPK float max64(float v) {
    v = fmaxf(v, dpp<0xB1>(v)); v = fmaxf(v, dpp<0x4E>(v)); v = fmaxf(v, dpp<0x141>(v)); v = fmaxf(v, dpp<0x140>(v));
    const int b = __builtin_bit_cast(int, v);
    float a0 = __builtin_bit_cast(float, __builtin_amdgcn_readlane(b, 0)), a1 = __builtin_bit_cast(float, __builtin_amdgcn_readlane(b, 16));
    float a2 = __builtin_bit_cast(float, __builtin_amdgcn_readlane(b, 32)), a3 = __builtin_bit_cast(float, __builtin_amdgcn_readlane(b, 48));
    return fmaxf(fmaxf(a0, a1), fmaxf(a2, a3));
}
__device__ __forceinline__ NOPK void st16(void* ptr, u4v v) {
    asm volatile("global_store_dwordx4 %0, %1, off sc1\n\ts_nop 1" ::"v"(ptr), "v"(v) : "memory");
}
__device__ __forceinline__ NOPK void ln_stats(float s1, float s2, float& mb, float& den) {
    float mean = s1 * (1.0f / D);
    float var = fmaxf(s2 * (1.0f / D) - mean * mean, 0.f);
    mb = rbf(mean);
    den = rbf(__builtin_amdgcn_sqrtf(rbf(rbf(var) + 1e-5f)));
}
__device__ __forceinline__ NOPK float ln_apply(float x, float mb, float rden) { return rbf(rbf(x - mb) * rden); }

#define SG(i) {}
#define SGDECL
#define SGDUMP(n)
#define TLB()
#define TLE(off)
struct Params {
    const u16 *x, *Are, *Aim, *Bre, *Bim, *Cre, *Cim, *logdt, *Dskip, *w1, *b1, *w2;
    u16* out;
    char* ws;
};

typedef float f16v __attribute__((ext_vector_type(16)));
__device__ __forceinline__ NOPK f16v mfma32_bf(u4v a, u4v b, f16v c) {
    return __builtin_amdgcn_mfma_f32_32x32x16_bf16(__builtin_bit_cast(b8v, a), __builtin_bit_cast(b8v, b), c, 0, 0, 0);
}
#define STATV 0
#define VMWAIT(N) asm volatile("s_waitcnt vmcnt(" #N ") lgkmcnt(0)" ::: "memory")

#define RAWBAR() { asm volatile("s_waitcnt lgkmcnt(0)" ::: "memory"); __builtin_amdgcn_s_barrier(); asm volatile("" ::: "memory"); }

__device__ __forceinline__ NOPK void gemm1_role(const Params& p, int b, char* smem) {
    const int tid = threadIdx.x, lane = tid & 63;
    const int w = __builtin_amdgcn_readfirstlane(tid >> 6);
    const int wt = w >> 2, wf = w & 3;
    const int xcd = b & 7, j = b >> 3;
    const int mb = 4 * (xcd >> 1) + (j & 3), nt = 7 * (xcd & 1) + (j >> 2);
    const int l31 = lane & 31, hh = lane >> 5;
    const bool half = (nt == 13);
    SGDECL
#define STOFF 0
    SG(0)
    u32 xo[2], wo;
#pragma unroll
    for (int i = 0; i < 2; ++i) {
        int P = (2 * w + i) * 64 + lane, row = P >> 3, ch = (P & 7) ^ ((row >> 1) & 7);
        xo[i] = (u32)(row * (D * 2) + ch * 16);
    }
    {
        int P = w * 64 + lane, k = P >> 4, ch = (P & 15) ^ ((k & 3) << 2);
        if (half) ch &= 7;
        wo = (u32)(k * (F * 2) + ch * 16);
    }
    const char* xb = (const char*)p.x + (size_t)mb * 128 * (D * 2);
    const char* wb = (const char*)p.w1 + nt * 256;
    const u32 lx = lds_addr(smem) + w * 2048, lw = lds_addr(smem) + 49152 + w * 1024;
#define SROT(st) (st)
#define G1_X(st) { const char* s_ = xb + SROT(st) * 128; GLDS16(lx + ((st) % 3) * 16384, xo[0], s_); GLDS16(lx + ((st) % 3) * 16384 + 1024, xo[1], s_); }
#define G1_W(h) { const char* s_ = wb + (size_t)(SROT((h) >> 1) * 64 + ((h)&1) * 32) * (F * 2); GLDS16(lw + ((h)&3) * 8192, wo, s_); }
    G1_W(0) G1_X(0)
    u2v bia[4];
    {
        const u32 bo = (u32)min(nt * 128 + 32 * wf + 4 * hh, F - 28) * 2u;
        asm volatile("global_load_dwordx2 %0, %4, %5\n\tglobal_load_dwordx2 %1, %4, %5 offset:16\n\t"
                     "global_load_dwordx2 %2, %4, %5 offset:32\n\tglobal_load_dwordx2 %3, %4, %5 offset:48"
                     : "=&v"(bia[0]), "=&v"(bia[1]), "=&v"(bia[2]), "=&v"(bia[3]) : "v"(bo), "s"(p.b1) : "memory");
    }
    G1_W(1) G1_X(1) G1_W(2) G1_W(3) G1_X(2)

    const int q = (lane & 15) >> 2, pp = lane & 3, blk = (lane >> 4) & 1;
    const int ro = (wf & 1) ^ wt, ri = wf >> 1;
    const char* xa[4][2];
    const char* wa[2];
#pragma unroll
    for (int u = 0; u < 4; ++u)
#pragma unroll
        for (int i = 0; i < 2; ++i)
            xa[u][i] = smem + (64 * wt + 32 * (i ^ ri) + l31) * 128 + (((2 * (u ^ ro) + hh) ^ ((l31 >> 1) & 7)) << 4);
#pragma unroll
    for (int o = 0; o < 2; ++o)
        wa[o] = smem + 49152 + (16 * (o ^ ro) + 8 * hh + q) * 256 + (((4 * wf + 2 * blk + (pp >> 1)) ^ (q << 2)) << 4) + 8 * (pp & 1);

    f16v acc[2];
#pragma unroll
    for (int i = 0; i < 16; ++i) acc[0][i] = acc[1][i] = 0.f;
    u4v xf[2][2][2], wfr[2][2];
    float s1a = 0.f, s1b = 0.f, s2a = 0.f, s2b = 0.f, csa = 0.f, csb = 0.f;
    SG(1)
#define G1_READ(h, o)                                                                                    \
    {                                                                                                    \
        u2v lo = tr_read(wa[o] + ((h)&3) * 8192), hi = tr_read(wa[o] + ((h)&3) * 8192 + 1024);           \
        wfr[(h)&1][o] = (u4v){lo[0], lo[1], hi[0], hi[1]};                                               \
        xf[(h)&1][o][0] = *(const u4v*)(xa[((h)&1) * 2 + o][0] + (((h) >> 1) % 3) * 16384);              \
        xf[(h)&1][o][1] = *(const u4v*)(xa[((h)&1) * 2 + o][1] + (((h) >> 1) % 3) * 16384);              \
    }
#define G1_M(h, o, i)                                                                                    \
    if ((h) >= 0) {                                                                                      \
        acc[i] = mfma32_bf(wfr[(h)&1][o], xf[(h)&1][o][i], acc[i]);                                      \
        asm volatile("" : "+v"(acc[i]));                                                                 \
    }                                                                                                    \
    __builtin_amdgcn_sched_barrier(0);
#define G1_STAT(h, e)                                                                                    \
    if ((h) >= 0) {                                                                                      \
        const u32 xv_ = xf[(h)&1][0][0][e];                                                              \
        s1a = dot2bf(xv_, 0x3f803f80u, s1a); s2a = dot2bf(xv_, xv_, s2a);                                \
        csa = dot2bf(wfr[(h)&1][0][e], 0x3f803f80u, csa);                                                \
    }
#define G1_PIN() asm volatile("" : "+v"(s1a), "+v"(s1b), "+v"(s2a), "+v"(s2b), "+v"(csa), "+v"(csb)); __builtin_amdgcn_sched_barrier(0);
#define G1_IT(h, NW)                                                                                     \
    {                                                                                                    \
        VMWAIT(NW);                                                                                      \
        __builtin_amdgcn_s_barrier();                                                                    \
        G1_M(h, 0, 0)                                                                                    \
        if ((h) >= 0 && (h) + 4 <= 19) G1_W((h) + 4)                                                     \
        if ((h) >= 1 && ((h)&1) && ((h) - 1) / 2 + 3 <= 9) G1_X(((h) - 1) / 2 + 3)                       \
        G1_M(h, 0, 1)                                                                                    \
        G1_READ((h) + 1, 0) G1_STAT(h, 0) G1_PIN()                                                       \
        G1_M(h, 1, 0)                                                                                    \
        G1_READ((h) + 1, 1) G1_STAT(h, 1) G1_PIN()                                                       \
        G1_M(h, 1, 1)                                                                                    \
        G1_STAT(h, 2) G1_STAT(h, 3) G1_PIN()                                                             \
    }
    G1_IT(-1, 11) G1_IT(0, 6) G1_IT(1, 4) G1_IT(2, 6) G1_IT(3, 4) SG(2) G1_IT(4, 6) G1_IT(5, 4) G1_IT(6, 6) G1_IT(7, 4)
    G1_IT(8, 6) SG(3) G1_IT(9, 4) G1_IT(10, 6) G1_IT(11, 4) G1_IT(12, 6) G1_IT(13, 4) SG(4) G1_IT(14, 6) G1_IT(15, 4)
    G1_IT(16, 4) G1_IT(17, 1) G1_IT(18, 0)
    G1_M(19, 0, 0) G1_M(19, 0, 1) G1_M(19, 1, 0) G1_M(19, 1, 1) G1_STAT(19, 0) G1_STAT(19, 1) G1_STAT(19, 2) G1_STAT(19, 3)
    s1a += s1b; s2a += s2b; csa += csb;
    SG(5)
    asm volatile("" : "+v"(bia[0]), "+v"(bia[1]), "+v"(bia[2]), "+v"(bia[3]));
    char* rsb = smem + 16384;
    char* csl = smem + 16384 + 2048;
    {
        const float a1 = s1a + swap32(s1a), a2 = s2a + swap32(s2a), ac = csa + swap32(csa);
        if (hh == 0) *(f2v*)(rsb + ((64 * wt + 32 * ri + l31) * 2 + (wf & 1)) * 8) = (f2v){a1, a2};
        else *(float*)(csl + ((32 * wf + l31) * 2 + wt) * 4) = ac;
    }
    RAWBAR()
    SG(6)
    if (!(half && wf >= 2)) {
        float csv[16], bsv[16];
#pragma unroll
        for (int g = 0; g < 4; ++g) {
            const f4v c0 = *(const f4v*)(csl + (32 * wf + 8 * g + 4 * hh) * 8), c1 = *(const f4v*)(csl + (32 * wf + 8 * g + 4 * hh) * 8 + 16);
            csv[4 * g] = c0[0] + c0[1]; csv[4 * g + 1] = c0[2] + c0[3]; csv[4 * g + 2] = c1[0] + c1[1]; csv[4 * g + 3] = c1[2] + c1[3];
            bsv[4 * g] = bflo(bia[g][0]) * 1.44269504f; bsv[4 * g + 1] = bfhi(bia[g][0]) * 1.44269504f;
            bsv[4 * g + 2] = bflo(bia[g][1]) * 1.44269504f; bsv[4 * g + 3] = bfhi(bia[g][1]) * 1.44269504f;
        }
        char* actb = p.ws + WS_ACT + ((size_t)(mb * 2 + wt) * KS2 + nt * 4 + wf) * 4096 + (size_t)((l31 >> 4) * 1024 + ((l31 & 15) + 16 * hh) * 16);
#pragma unroll
        for (int i = 0; i < 2; ++i) {
            const f4v u0 = *(const f4v*)(rsb + (64 * wt + 32 * (i ^ ri) + l31) * 16);
            float mean, den;
            ln_stats(u0[0] + u0[2], u0[1] + u0[3], mean, den);
            const float rdl = __builtin_amdgcn_rcpf(den) * 1.44269504f;
#pragma unroll
            for (int c = 0; c < 2; ++c) {
                u4v o;
#pragma unroll
                for (int e = 0; e < 4; ++e) {
                    float r[2];
#pragma unroll
                    for (int z = 0; z < 2; ++z) {
                        const int ix = 8 * c + 2 * e + z;
                        const float u = fmaf(fmaf(-mean, csv[ix], acc[i][ix]), rdl, bsv[ix]);
                        const float e1 = __builtin_amdgcn_exp2f(u) + 1.0f;
                        r[z] = u * fmaf(__builtin_amdgcn_rcpf(fmaf(e1, e1, 1.0f)), -1.38629436f, 0.69314718f);
                    }
                    o[e] = pkbf(r[0], r[1]);
                }
                st16(actb + (i ^ ri) * 2048 + c * 512, o);
            }
        }
    }
    SG(7)
    SGDUMP(8)
}

__device__ __forceinline__ NOPK void ln_role(const Params& p, int rb, char* smem) {
    const int tid = threadIdx.x, lane = tid & 63;
    const int w = __builtin_amdgcn_readfirstlane(tid >> 6);
    const int rl = 4 * w + (lane >> 4), t = rb * 32 + rl, c15 = lane & 15;
    u4v v[5];
#pragma unroll
    for (int i = 0; i < 5; ++i) v[i] = *(const u4v*)(p.x + (size_t)t * D + (c15 + 16 * i) * 8);
    float s = 0.f;
#pragma unroll
    for (int i = 0; i < 5; ++i)
#pragma unroll
        for (int e = 0; e < 4; ++e) s += bflo(v[i][e]) + bfhi(v[i][e]);
    const float mean = red16(s) * (1.0f / D);
    float qv = 0.f;
#pragma unroll
    for (int i = 0; i < 5; ++i)
#pragma unroll
        for (int e = 0; e < 4; ++e) {
            float a = bflo(v[i][e]) - mean, c = bfhi(v[i][e]) - mean;
            qv = fmaf(a, a, qv); qv = fmaf(c, c, qv);
        }
    const float var = red16(qv) * (1.0f / D);
    const float mbf = rbf(mean);
    const float rd = 1.0f / rbf(__builtin_sqrtf(rbf(rbf(var) + 1e-5f)));
#pragma unroll
    for (int i = 0; i < 5; ++i) {
        u2v o0, o1;
        o0[0] = pkh(ln_apply(bflo(v[i][0]), mbf, rd), ln_apply(bfhi(v[i][0]), mbf, rd));
        o0[1] = pkh(ln_apply(bflo(v[i][1]), mbf, rd), ln_apply(bfhi(v[i][1]), mbf, rd));
        o1[0] = pkh(ln_apply(bflo(v[i][2]), mbf, rd), ln_apply(bfhi(v[i][2]), mbf, rd));
        o1[1] = pkh(ln_apply(bflo(v[i][3]), mbf, rd), ln_apply(bfhi(v[i][3]), mbf, rd));
        *(u2v*)(smem + rl * 1288 + (c15 + 16 * i) * 16) = o0;
        *(u2v*)(smem + rl * 1288 + (c15 + 16 * i) * 16 + 8) = o1;
    }
    RAWBAR()
    const int G = lane >> 4, q = c15 >> 2, pp = lane & 3;
    const char* src = smem + (16 * (G >> 1) + 4 * q + 2 * (G & 1)) * 1288 + pp * 8;
    char* dst = p.ws + WS_REC + (size_t)c15 * REC + REC_X + (size_t)rb * 64 + (G ^ ((rb >> 2) & 1)) * 16;
#pragma unroll
    for (int it = 0; it < 5; ++it) {
        const int cg = w * 5 + it;
        u2v lo = tr_read(src + cg * 32), hi = tr_read(src + cg * 32 + 1288);
        st16(dst + (size_t)cg * 16 * REC, (u4v){lo[0], lo[1], hi[0], hi[1]});
    }
}

struct cplx2 { f2v r, i; };
__device__ __forceinline__ NOPK cplx2 cmul(cplx2 a, cplx2 b) {
    cplx2 o;
    o.r = a.r * b.r - a.i * b.i;
    o.i = a.r * b.i + a.i * b.r;
    return o;
}
struct cplxh { h2v r, i; };
__device__ __forceinline__ NOPK cplxh toh(cplx2 a) { return {__builtin_convertvector(a.r, h2v), __builtin_convertvector(a.i, h2v)}; }
__device__ __forceinline__ NOPK cplx2 cexp2(f2v zr, f2v zi, float c) {
    cplx2 o;
#pragma unroll
    for (int m = 0; m < 2; ++m) {
        float mag = __builtin_amdgcn_exp2f(zr[m] * c * 1.44269504089f);
        float ph = zi[m] * c;
        const float CH = 0.15915494f, CL = 6.4206382e-9f;
        float hi = ph * CH;
        float lo = fmaf(ph, CH, -hi) + ph * CL;
        float fr = __builtin_amdgcn_fractf(hi) + lo;
        o.r[m] = mag * __builtin_amdgcn_cosf(fr);
        o.i[m] = mag * __builtin_amdgcn_sinf(fr);
    }
    return o;
}
static constexpr int KG_ROW = 544;

__device__ __forceinline__ NOPK void kgen_role(const Params& p, int d, int part, char* sw  ) {
    const int lane = threadIdx.x & 63, l15 = lane & 15, q4 = lane >> 4;
    const int o = d * NS + 2 * lane;
    u32 are = *(const u32*)(p.Are + o), aim = *(const u32*)(p.Aim + o), bre = *(const u32*)(p.Bre + o);
    u32 bim = *(const u32*)(p.Bim + o), cre = *(const u32*)(p.Cre + o), cim = *(const u32*)(p.Cim + o);
    float dt = rbf(expf(bf2f(p.logdt[d])));
    f2v zr = {dt * bflo(are), dt * bfhi(are)}, zi = {dt * bflo(aim), dt * bfhi(aim)};
    cplx2 dB = {{dt * bflo(bre), dt * bfhi(bre)}, {dt * bflo(bim), dt * bfhi(bim)}};
    cplx2 C = {{bflo(cre), bfhi(cre)}, {bflo(cim), bfhi(cim)}};
    cplx2 W = cmul(C, dB);
    float wm = fmaxf(fmaxf(fabsf(W.r[0]), fabsf(W.r[1])), fmaxf(fabsf(W.i[0]), fabsf(W.i[1])));
    const float dsk = bf2f(p.Dskip[d]);
    wm = fmaxf(max64(wm), fabsf(dsk) * (1.0f / 128.0f));
    int eU = 14 - __builtin_amdgcn_frexp_expf(wm);
    eU = min(max(eU, -100), 100);
    const float sU = __builtin_amdgcn_ldexpf(1.0f, eU);
    W.r *= sU; W.i *= sU;
    char* rec = p.ws + WS_REC + (size_t)d * REC;
    if (part == 0 && lane < 34) {
        u4v z = {0u, 0u, 0u, 0u};
        if (lane == 0) z[0] = f2u(__builtin_amdgcn_ldexpf(1.0f, 8 - eU));
        *(u4v*)(rec + (lane < 2 ? lane * 16 : 4160 + (lane - 2) * 16)) = z;
    }
    const float KSC = 2.384185791015625e-07f;

    char* r0 = sw;
    char* kc = sw + 16 * KG_ROW;
#define KG_PUT(row, v) *(u2v*)(r0 + (row)*KG_ROW + lane * 8) = (u2v){pkh((v).r[0], (v).r[1]), pkh((v).i[0], (v).i[1])}
#define KG_PUTH(row, T_, E_)                                                                            \
    *(u2v*)(r0 + (row)*KG_ROW + lane * 8) = (u2v){__builtin_bit_cast(u32, __builtin_elementwise_fma(T_.r, E_.r, -(T_.i * E_.i))), \
                                                  __builtin_bit_cast(u32, __builtin_elementwise_fma(T_.r, E_.i, T_.i * E_.r))}
#define KG_MMA(A)                                         \
    A = (f4v){0.f, 0.f, 0.f, 0.f};                        \
    _Pragma("unroll") for (int ks = 0; ks < 8; ++ks) A = mfma_h(*(const u4v*)(r0 + l15 * KG_ROW + ks * 64 + q4 * 16), vf[ks], A);
    const cplx2 G = cexp2(zr, zi, 128.f);
    u16* kch = (u16*)kc;
    char* kx = rec + REC_K;
    u4v vf[8];
    if (part == 0) {
        {
            cplx2 v1 = cexp2(zr, zi, 8.f);
            cplx2 v = {{16384.f, 16384.f}, {0.f, 0.f}};
#pragma unroll
            for (int jj = 0; jj < 16; ++jj) {
                cplx2 t = {v.r, -v.i};
                KG_PUT(jj, t);
                if (jj < 15) v = cmul(v, v1);
            }
        }
        RAWBAR()
#pragma unroll
        for (int ks = 0; ks < 8; ++ks) vf[ks] = *(const u4v*)(r0 + l15 * KG_ROW + ks * 64 + q4 * 16);
        cplx2 E[8];
        E[1] = cexp2(zr, zi, 1.f);
        E[2] = cmul(E[1], E[1]); E[3] = cmul(E[2], E[1]); E[4] = cmul(E[2], E[2]);
        E[5] = cmul(E[4], E[1]); E[6] = cmul(E[4], E[2]); E[7] = cmul(E[4], E[3]);
        const cplxh T0 = toh(W), T1 = toh(cmul(W, G));
        RAWBAR()
        *(u2v*)(r0 + lane * 8) = (u2v){__builtin_bit_cast(u32, T0.r), __builtin_bit_cast(u32, T0.i)};
        *(u2v*)(r0 + 8 * KG_ROW + lane * 8) = (u2v){__builtin_bit_cast(u32, T1.r), __builtin_bit_cast(u32, T1.i)};
#pragma unroll
        for (int c = 1; c < 8; ++c) {
            const cplxh Eh = toh(E[c]);
            KG_PUTH(c, T0, Eh);
            KG_PUTH(8 + c, T1, Eh);
        }
        f4v acc;
        KG_MMA(acc)
        if (lane == 0) acc[0] = fmaf(dsk * sU, 16384.f, acc[0]);
        *(u2v*)(kx + (128 * (q4 >> 1) + 4 * (q4 & 1) + 8 * l15) * 2) = (u2v){pkh(acc[0] * KSC, acc[1] * KSC), pkh(acc[2] * KSC, acc[3] * KSC)};
    } else {
        const cplx2 E2 = cexp2(zr, zi, 2.f);
        const cplx2 E4 = cmul(E2, E2);
        const cplxh E2h = toh(E2), E4h = toh(E4), E6h = toh(cmul(E4, E2));
        cplx2 T = cmul(W, cexp2(zr, zi, 256.f));
#pragma unroll
        for (int a = 0; a < 6; ++a) {
            const cplxh Th = toh(T);
            const int row = a < 2 ? 4 * a : 4 + 2 * a;
            *(u2v*)(r0 + row * KG_ROW + lane * 8) = (u2v){__builtin_bit_cast(u32, Th.r), __builtin_bit_cast(u32, Th.i)};
            if (a < 2) { KG_PUTH(row + 1, Th, E2h); KG_PUTH(row + 2, Th, E4h); KG_PUTH(row + 3, Th, E6h); }
            else KG_PUTH(row + 1, Th, E4h);
            if (a < 5) T = cmul(T, G);
        }
        RAWBAR()
#pragma unroll
        for (int ks = 0; ks < 8; ++ks) vf[ks] = *(const u4v*)(r0 - 10240 + l15 * KG_ROW + ks * 64 + q4 * 16);
        RAWBAR()
        f4v acc1, acc2;
        KG_MMA(acc1)
        T = cmul(W, cexp2(zr, zi, 1024.f));
#pragma unroll
        for (int a = 0; a < 9; ++a) {
            KG_PUT(a, T);
            if (a < 8) T = cmul(T, G);
        }
        KG_MMA(acc2)
        {
            u32 h01 = pkh(acc1[0] * KSC, acc1[1] * KSC), h23 = pkh(acc1[2] * KSC, acc1[3] * KSC);
            if (q4 < 2) {
                *(u2v*)(kc + (64 * q4 + 4 * l15) * 2) = (u2v){h01, h23};
            } else {
                *(u32*)(kc + (128 + 64 * (q4 - 2) + 2 * l15) * 2) = h01;
                *(u32*)(kc + (128 + 64 * (q4 - 2) + 32 + 2 * l15) * 2) = h23;
            }
            u32 g01 = pkh(acc2[0] * KSC, acc2[1] * KSC), g23 = pkh(acc2[2] * KSC, acc2[3] * KSC);
            if (q4 < 2) {
                kch[256 + 64 * q4 + l15] = (u16)g01;
                kch[256 + 64 * q4 + 16 + l15] = (u16)(g01 >> 16);
                kch[256 + 64 * q4 + 32 + l15] = (u16)g23;
                kch[256 + 64 * q4 + 48 + l15] = (u16)(g23 >> 16);
            } else if (lane == 32) {
                kch[384] = (u16)g01;
            }
        }
        if (lane < 32) {
            u2v s = *(const u2v*)(kc + lane * 8);
            u32 s4 = kch[lane * 4 + 4];
            u4v b2;
            b2[0] = (s[0] & 0xffffu) | (s[0] << 16);
            b2[1] = (s[0] >> 16) | (s[1] << 16);
            b2[2] = (s[1] & 0xffffu) | (s[1] << 16);
            b2[3] = (s[1] >> 16) | (s4 << 16);
            st16(kx + 512 + lane * 16, b2);
        }
        {
            u32 s = *(const u32*)(kc + 256 + lane * 4);
            u32 c = kch[128 + lane * 2 + 2];
            u4v v;
            v[0] = (s & 0xffffu) | (s << 16);
            v[1] = s;
            v[2] = (s >> 16) | (s & 0xffff0000u);
            v[3] = c | (c << 16);
            st16(kx + 1024 + lane * 16, v);
        }
#pragma unroll
        for (int it = 0; it < 2; ++it) {
            int oc = lane + 64 * it;
            u32 a = kch[256 + oc], c = kch[257 + oc];
            u4v v;
            v[0] = a | (a << 16);
            v[1] = v[0];
            v[2] = ((oc & 1) ? c : a) | (c << 16);
            v[3] = c | (c << 16);
            st16(kx + 2048 + oc * 16, v);
        }
    }
}

__device__ __forceinline__ NOPK void w2pack_role(const Params& p, int unit, char* sw) {
    const int lane = threadIdx.x & 63, l15 = lane & 15, q4 = lane >> 4;
    const int nb = unit / KS2, ks = unit - nb * KS2;
#pragma unroll
    for (int i = 0; i < 5; ++i) {
        int c = lane + 64 * i, row = c / 10, ch = c - row * 10;
        *(u4v*)(sw + c * 16) = *(const u4v*)(p.w2 + (size_t)(ks * 32 + row) * D + nb * 80 + ch * 8);
    }
    const int q = l15 >> 2, pp = lane & 3;
    char* dst = p.ws + WS_W2P + (size_t)unit * 5120 + lane * 16;
    const char* src = sw + (16 * (q4 >> 1) + 4 * (q4 & 1) + q) * 160 + pp * 8;
#pragma unroll
    for (int dt = 0; dt < 5; ++dt) {
        u2v lo = tr_read(src + dt * 32);
        u2v hi = tr_read(src + 8 * 160 + dt * 32);
        st16(dst + dt * 1024, (u4v){lo[0], lo[1], hi[0], hi[1]});
    }
}

__global__ __launch_bounds__(512, 4) NOPK void k1(Params p) {
    extern __shared__ __attribute__((aligned(16))) char smem[];
    const int b = blockIdx.x;
    const int w = __builtin_amdgcn_readfirstlane(threadIdx.x >> 6);
    TLB()
    if (b < 224) {
        gemm1_role(p, b, smem);
    } else if (b >= 256 && b < 480) {
        if (b >= 470) return;
        const int d = (b - 256) * 3 + (w >> 1);
        if (w < 6 && d < D) {
            kgen_role(p, d, w & 1, smem + w * 10240);
        } else {
            if (w >= 6) {
                const int unit = (b - 256) * 2 + (w - 6);
                w2pack_role(p, unit, smem + w * 10240);
                if (unit < 4) w2pack_role(p, 428 + unit, smem + w * 10240);
                if (b == 469 && (threadIdx.x & 63) < 8 && w == 7) *(u32*)(p.ws + WS_CNT + (threadIdx.x & 63) * 256) = 0u;
            }
            __builtin_amdgcn_s_barrier();
            __builtin_amdgcn_s_barrier();
        }
    } else {
        ln_role(p, b < 256 ? b - 224 : b - 448, smem);
    }
    TLE(0)
}

static constexpr int CV_WAVE = 9216;
static constexpr int K2_RED = 3 * CV_WAVE;
static constexpr int K2_YS = K2_RED + 61440;
static constexpr int K2_LDS = K2_YS + 10240;

#define AC0 "a"
#define AC1 "a"
#define AC2 "a"
#define AC3 "v"
#define AC4 "v"
#define AC5 "v"
#define G2_BUF(C, i) C AC##i(af[i][0]), C AC##i(af[i][1]), C AC##i(af[i][2]), C AC##i(af[i][3]), C "a"(wf[i][0]), C "a"(wf[i][1]), C "a"(wf[i][2]), C "a"(wf[i][3]), C "a"(wf[i][4])
#define G2_LOAD(i)                                                                                                   \
    asm volatile("buffer_load_dwordx4 %0, %9, %15, %17 offen\n\tbuffer_load_dwordx4 %4, %13, %16, %18 offen\n\t"     \
                 "buffer_load_dwordx4 %1, %10, %15, %17 offen\n\tbuffer_load_dwordx4 %2, %11, %15, %17 offen\n\t"    \
                 "buffer_load_dwordx4 %3, %12, %15, %17 offen\n\tbuffer_load_dwordx4 %5, %13, %16, %18 offen offset:1024\n\t" \
                 "buffer_load_dwordx4 %6, %13, %16, %18 offen offset:2048\n\tbuffer_load_dwordx4 %7, %13, %16, %18 offen offset:3072\n\t" \
                 "buffer_load_dwordx4 %8, %14, %16, %18 offen"                                                       \
                 : G2_BUF("=&", i) : "v"(ao[0]), "v"(ao[1]), "v"(ao[2]), "v"(ao[3]), "v"(ao[4]), "v"(ao[5]), "s"(ra), "s"(rw), \
                   "s"((i)*4096), "s"((i)*5120) : "memory");
#define G2_MF(d, w_, a_) "v_mfma_f32_16x16x32_bf16 %" #d ", %" #w_ ", %" #a_ ", %" #d "\n\t"
#define G2_M4(dt, i, VO, OFF)                                                                           \
    asm volatile(G2_MF(0, 4, 5) G2_MF(1, 4, 6) G2_MF(2, 4, 7) G2_MF(3, 4, 8)                            \
                 "buffer_load_dwordx4 %4, %9, %10, %11 offen offset:" #OFF                              \
                 : "+a"(acc2[dt][0]), "+a"(acc2[dt][1]), "+a"(acc2[dt][2]), "+a"(acc2[dt][3]), "+a"(wf[i][dt]) \
                 : AC##i(af[i][0]), AC##i(af[i][1]), AC##i(af[i][2]), AC##i(af[i][3]), "v"(ao[VO]), "s"(rw), "s"(sw) : "memory");
#define G2_BODY(i)                                                                                      \
    {                                                                                                   \
        G2_M4(0, i, 4, 0) G2_M4(1, i, 4, 1024) G2_M4(2, i, 4, 2048) G2_M4(3, i, 4, 3072) G2_M4(4, i, 5, 0) \
        asm volatile("buffer_load_dwordx4 %0, %4, %8, %9 offen\n\tbuffer_load_dwordx4 %1, %5, %8, %9 offen\n\t" \
                     "buffer_load_dwordx4 %2, %6, %8, %9 offen\n\tbuffer_load_dwordx4 %3, %7, %8, %9 offen" \
                     : "+" AC##i(af[i][0]), "+" AC##i(af[i][1]), "+" AC##i(af[i][2]), "+" AC##i(af[i][3])                   \
                     : "v"(ao[0]), "v"(ao[1]), "v"(ao[2]), "v"(ao[3]), "s"(ra), "s"(sa) : "memory");    \
    }
#define G2_STEP(i) { asm volatile("s_waitcnt vmcnt(45)" : G2_BUF("+", i)::"memory"); G2_BODY(i) sa += 4096; sw += 5120; }
#define G2_M4N(dt, i)                                                                                   \
    asm volatile(G2_MF(0, 4, 5) G2_MF(1, 4, 6) G2_MF(2, 4, 7) G2_MF(3, 4, 8) ""                         \
                 : "+a"(acc2[dt][0]), "+a"(acc2[dt][1]), "+a"(acc2[dt][2]), "+a"(acc2[dt][3])           \
                 : "a"(wf[i][dt]), AC##i(af[i][0]), AC##i(af[i][1]), AC##i(af[i][2]), AC##i(af[i][3]));
#define G2_SSTEP(k, i)                                                                                  \
    {                                                                                                   \
        if constexpr ((k) < 2) {                                                                        \
            const u32 sa = ((k) + 6) * 4096, sw = ((k) + 6) * 5120;                                     \
            G2_BODY(i)                                                                                  \
        } else {                                                                                        \
            G2_M4N(0, i) G2_M4N(1, i) G2_M4N(2, i) G2_M4N(3, i) G2_M4N(4, i)                            \
        }                                                                                               \
        __builtin_amdgcn_sched_barrier(0);                                                              \
    }
#define G2_ACC(a0) "+a"(acc2[a0][0]), "+a"(acc2[a0][1]), "+a"(acc2[a0][2]), "+a"(acc2[a0][3])
#define G2_ZERO()                                                                                       \
    _Pragma("unroll") for (int dt = 0; dt < 5; ++dt) _Pragma("unroll") for (int tt = 0; tt < 4; ++tt) acc2[dt][tt] = (f4v){0.f, 0.f, 0.f, 0.f}; \
    asm volatile("s_nop 1" : G2_ACC(0), G2_ACC(1), G2_ACC(2), G2_ACC(3), G2_ACC(4));
#undef STOFF
#define STOFF (4 << 20)
__device__ __forceinline__ NOPK void conv_wave(const Params& p, int d, u32 lb, char* img, u4v (&af)[6][4], u4v (&wf)[6][5],
                                                f4v (&acc2)[5][4], u4v ra, u4v rw, const u32 (&ao)[6], u32 svc, const u32* cnt,
                                                const char* yb, u32 ysl) {
    const int lane = threadIdx.x & 63, l15 = lane & 15, q4 = lane >> 4;
    const int q = l15 >> 2, pp = lane & 3;
    const u32 kra = lb + REC_K + (32 * q4 - 4 * q + 4 * pp) * 2;
    u32 xra[2][2][2];
#pragma unroll
    for (int ap = 0; ap < 2; ++ap)
#pragma unroll
        for (int h = 0; h < 2; ++h)
#pragma unroll
            for (int g = 0; g < 2; ++g) {
                int G = l15 + 8 * g - 2 * q4 - h + 8;
                xra[ap][h][g] = lb + 4160 + G * 32 + ((ap ^ ((G >> 3) & 1)) * 16);
            }
    const u32 csrc = (lane >= 16 ? lane - 16 : (lane == 0 ? 63 : lane + 47)) * 4;
    asm volatile("s_waitcnt vmcnt(5)" ::: "memory");
    u4v kf[16], xf[4];
    f4v acc[8];
    float Y[8][7];
    float scale;
    asm volatile("ds_read_b32 %0, %1" : "=v"(scale) : "v"(lb));
#define CV_XRD(s)                                                                                          \
    {                                                                                                      \
        u2v v0, v1;                                                                                        \
        asm volatile("ds_read_b64 %0, %2 offset:%4\n\tds_read_b64 %1, %3 offset:%4" : "=&v"(v0), "=&v"(v1) \
                     : "v"(xra[(s) >> 5][0][(s)&1]), "v"(xra[(s) >> 5][1][(s)&1]), "i"((((s)&15) >> 1) * 512 + (((s) >> 4) & 1) * 8)); \
        xf[(s)&3] = (u4v){v0[0], v0[1], v1[0], v1[1]};                                                     \
    }
    CV_XRD(0) CV_XRD(1) CV_XRD(2)
#pragma unroll
    for (int e = 0; e < 16; ++e) {
        u2v lo, hi;
        asm volatile("ds_read_b64_tr_b16 %0, %2 offset:%3\n\tds_read_b64_tr_b16 %1, %2 offset:%4" : "=&v"(lo), "=&v"(hi)
                     : "v"(kra), "i"(e * 256), "i"(e * 256 + 32));
        kf[e] = (u4v){lo[0], lo[1], hi[0], hi[1]};
    }
    asm volatile("s_waitcnt lgkmcnt(0)" : "+v"(kf[0]), "+v"(kf[1]), "+v"(kf[2]), "+v"(kf[3]), "+v"(kf[4]), "+v"(kf[5]), "+v"(kf[6]), "+v"(kf[7]),
                 "+v"(kf[8]), "+v"(kf[9]), "+v"(kf[10]), "+v"(kf[11]), "+v"(kf[12]), "+v"(kf[13]), "+v"(kf[14]), "+v"(kf[15]), "+v"(scale));
#define CV_STEP(s)                                                                                         \
    {                                                                                                      \
        constexpr int a = (s) >> 4, g = (s)&15;                                                            \
        if constexpr ((s) + 3 < 64) CV_XRD(((s) + 3) & 63)                                                 \
          \
        _Pragma("unroll") for (int e = (g + 1) & 1; e + g <= 15; e += 2) {                                 \
            if constexpr (g == 0) {                                                                        \
                if (e < 2) asm volatile("s_waitcnt lgkmcnt(6)\n\tv_mfma_f32_16x16x32_f16 %0, %1, %2, 0" : "=&v"(acc[(e + g) >> 1]) : "v"(kf[e]), "v"(xf[(s)&3])); \
                else asm volatile("v_mfma_f32_16x16x32_f16 %0, %1, %2, 0" : "=&v"(acc[(e + g) >> 1]) : "v"(kf[e]), "v"(xf[(s)&3])); \
            } else {                                                                                       \
                if (e < 2) asm volatile("s_waitcnt lgkmcnt(%3)\n\tv_mfma_f32_16x16x32_f16 %0, %1, %2, %0" : "+v"(acc[(e + g) >> 1]) : "v"(kf[e]), "v"(xf[(s)&3]), "i"((s) + 3 < 64 ? 6 : 2 * (63 - (s)))); \
                else asm volatile("v_mfma_f32_16x16x32_f16 %0, %1, %2, %0" : "+v"(acc[(e + g) >> 1]) : "v"(kf[e]), "v"(xf[(s)&3])); \
            }                                                                                              \
        }                                                                                                  \
        if constexpr (g == 15) {      \
            asm volatile("s_nop 7\n\ts_nop 3" : "+v"(acc[0]), "+v"(acc[1]), "+v"(acc[2]), "+v"(acc[3]), "+v"(acc[4]), "+v"(acc[5]), "+v"(acc[6]), "+v"(acc[7])); \
            _Pragma("unroll") for (int t = 0; t < 8; ++t) _Pragma("unroll") for (int r = 0; r < 4; ++r)    \
                Y[t][r + a] = (a == 0 || r == 3) ? acc[t][r] : Y[t][r + a] + acc[t][r];                    \
            __builtin_amdgcn_sched_barrier(0);                                                             \
        }                                                                                                  \
    }
#define CV_S4(s) CV_STEP(s) CV_STEP((s) + 1) CV_STEP((s) + 2) CV_STEP((s) + 3)
#define CV_S16(s) CV_S4(s) CV_S4((s) + 4) CV_S4((s) + 8) CV_S4((s) + 12)
#define CV_S8(s) CV_S4(s) CV_S4((s) + 4)
    CV_S16(0) G2_LOAD(0) CV_S8(16) G2_LOAD(1) CV_S8(24) G2_LOAD(2) CV_S8(32) G2_LOAD(3) CV_S8(40) G2_LOAD(4) CV_S8(48) G2_LOAD(5) CV_S8(56)
    float cr[8][3];
#pragma unroll
    for (int t = 0; t < 8; ++t)
#pragma unroll
        for (int r = 0; r < 3; ++r)
            cr[t][r] = __builtin_bit_cast(float, __builtin_amdgcn_ds_bpermute(csrc, __builtin_bit_cast(int, Y[t][4 + r])));
    __builtin_amdgcn_sched_barrier(0);
#pragma unroll
    for (int t = 0; t < 8; ++t) {
        float y[4];
#pragma unroll
        for (int r = 0; r < 3; ++r) y[r] = Y[t][r] + (lane == 0 ? (t ? cr[t - (t > 0)][r] : 0.f) : cr[t][r]);
        y[3] = Y[t][3];
        *(u2v*)(img + t * 512 + (16 * l15 + 4 * q4) * 2) = (u2v){pkbf(y[0] * scale, y[1] * scale), pkbf(y[2] * scale, y[3] * scale)};
    }
    char* dst = p.ws + WS_YT + (size_t)d * 4096 + lane * 16;
    {
        u4v o[4];
#pragma unroll
        for (int i = 0; i < 4; ++i) o[i] = *(const u4v*)(img + i * 1024 + lane * 16);
#pragma unroll
        for (int i = 0; i < 4; ++i)
            asm volatile("global_store_dwordx4 %0, %1, off offset:%2 sc1\n\ts_nop 1" ::"v"(dst), "v"(o[i]), "i"(i * 1024) : "memory");
    }
    G2_ZERO()
    asm volatile("s_waitcnt vmcnt(0)\n\ts_mov_b64 exec, 1\n\tglobal_atomic_add %0, %1, %2\n\ts_mov_b64 exec, -1"
                 ::"v"(0u), "v"(1u), "s"(p.ws + WS_CNT + (d / 80) * 256) : "memory");
    asm volatile("" : G2_BUF("+", 0), G2_BUF("+", 1), G2_BUF("+", 2)::"memory");
    asm volatile("" : G2_BUF("+", 3), G2_BUF("+", 4), G2_BUF("+", 5)::"memory");
    G2_SSTEP(0, 0) G2_SSTEP(1, 1) G2_SSTEP(2, 2) G2_SSTEP(3, 3)
    u32 cv = 0;
    asm volatile("s_cmp_lg_u32 %1, 0\n\ts_cbranch_scc1 .Lsv%=\n\tglobal_load_dword %0, %0, %2 sc1\n.Lsv%=:" : "+v"(cv) : "s"(svc), "s"(cnt) : "memory", "scc");
    G2_SSTEP(4, 4) G2_SSTEP(5, 5)
    asm volatile("s_waitcnt vmcnt(0)" : "+v"(cv), G2_BUF("+", 0), G2_BUF("+", 1)::"memory");
    if (svc == 0) {
        unsigned spins = 0;
        while (__builtin_amdgcn_readfirstlane(cv) < 80u && ++spins < (1u << 22)) {
            __builtin_amdgcn_s_sleep(4);
            cv = __hip_atomic_load(cnt, __ATOMIC_RELAXED, __HIP_MEMORY_SCOPE_AGENT);
        }
        const u32 yo = (lane >> 3) * 4096 + (lane & 7) * 16;
#pragma unroll
        for (int i = 0; i < 10; ++i)
            asm volatile("s_mov_b32 m0, %0\n\ts_nop 0\n\tglobal_load_lds_dwordx4 %1, %2 sc1" ::"s"(ysl + i * 1024), "v"(yo), "s"(yb + i * 32768) : "memory");
    }
    G2_SSTEP(6, 0) G2_SSTEP(7, 1)
    asm volatile("s_waitcnt vmcnt(0)\n\ts_nop 7\n\ts_nop 7" : G2_ACC(0), G2_ACC(1), G2_ACC(2), G2_ACC(3), G2_ACC(4)::"memory");
}

__global__ __launch_bounds__(256, 1) NOPK void k2(Params p) {
    extern __shared__ __attribute__((aligned(16))) char smem[];
    const int b = blockIdx.x;
    const int w = __builtin_amdgcn_readfirstlane(threadIdx.x >> 6);
    TLB()
    const int xcd = b & 7, j = b >> 3;
    const int mb = 8 * (xcd >> 1) + (j & 7), nb = 4 * (xcd & 1) + (j >> 3);
    const int nch = (j & 1) ? 2 : 3;
    const int ch0 = xcd * 80 + (j >> 1) * 5 + ((j & 1) ? 3 : 0);
    const int k0 = ((j & 1) && w == 3) ? 34 : 8 * w;
    const int nk = w < nch ? 8 : ((j & 1) ? 2 * w + 14 : 30);
    const char* ab = p.ws + WS_ACT + ((size_t)mb * KS2 + k0) * 4096;
    const char* wb = p.ws + WS_W2P + ((size_t)nb * KS2 + k0) * 5120;
    u4v ra = {(u32)(size_t)ab, (u32)((size_t)ab >> 32), (u32)nk * 4096u, 0x00020000u};
    u4v rw = {(u32)(size_t)wb, (u32)((size_t)wb >> 32), (u32)nk * 5120u, 0x00020000u};
    const u32 lo16 = (threadIdx.x & 63) * 16;
    const u32 lb = lds_addr(smem);
    const u32 ao[6] = {lo16 + (w & 3) * 1024, lo16 + ((w + 1) & 3) * 1024, lo16 + ((w + 2) & 3) * 1024, lo16 + ((w + 3) & 3) * 1024, lo16, lo16 + 4096};
    SGDECL SG(0)
    if (w < nch) {
        const char* src = p.ws + WS_REC + (size_t)(ch0 + w) * REC;
#pragma unroll
        for (int i = 0; i < 9; ++i) GLDS16(lb + w * CV_WAVE + i * 1024, lo16, src + i * 1024);
    }
    u4v af[6][4], wf[6][5];
    u2v xv[5];
    {
        const u16* xp = p.x + (size_t)(mb * 64 + w * 16 + (threadIdx.x & 15)) * D + nb * 80 + ((threadIdx.x >> 4) & 3) * 4;
#pragma unroll
        for (int dt = 0; dt < 5; ++dt) xv[dt] = *(const u2v*)(xp + dt * 16);
    }
    SG(1)
    f4v acc2[5][4];
    if (w < nch) {
        conv_wave(p, ch0 + w, lb + w * CV_WAVE, smem + w * CV_WAVE, af, wf, acc2, ra, rw, ao, (u32)w,
                  (const u32*)(p.ws + WS_CNT + nb * 256), p.ws + WS_YT + (size_t)(nb * 80) * 4096 + (size_t)mb * 128, lb + K2_YS);
    } else {
        G2_LOAD(0) G2_LOAD(1) G2_LOAD(2) G2_LOAD(3) G2_LOAD(4) G2_LOAD(5)
        G2_ZERO()
        u32 sa = 6 * 4096, sw = 6 * 5120;
#pragma nounroll
        for (int r = 6; r < nk; r += 6) { G2_STEP(0) G2_STEP(1) G2_STEP(2) G2_STEP(3) G2_STEP(4) G2_STEP(5) }
#define G2_LAST(i, NW) { asm volatile("s_waitcnt vmcnt(" #NW ")" : G2_BUF("+", i)::"memory"); G2_SSTEP(2, i) }
        G2_LAST(0, 45) G2_LAST(1, 36) G2_LAST(2, 27) G2_LAST(3, 18) G2_LAST(4, 9) G2_LAST(5, 0)
        asm volatile("s_nop 7\n\ts_nop 7" : G2_ACC(0), G2_ACC(1), G2_ACC(2), G2_ACC(3), G2_ACC(4)::"memory");
    }
    SG(2)
    SG(3)
    int lane = threadIdx.x & 63;
    asm volatile("" : "+v"(lane));
    const int l15 = lane & 15, q4 = lane >> 4;
    const int t = mb * 64 + w * 16 + l15;
    const int dbase = nb * 80 + 4 * q4;
    char* red = smem + K2_RED;
#pragma unroll
    for (int tt = 1; tt < 4; ++tt) {
        char* dst = red + (((tt + w) & 3) * 3 + tt - 1) * 5120 + lane * 16;
#pragma unroll
        for (int dt = 0; dt < 5; ++dt) *(f4v*)(dst + dt * 1024) = acc2[dt][tt];
    }
    RAWBAR()
    SG(4)
    const char* ys = smem + K2_YS + (4 * q4 + (l15 >> 2)) * 128 + w * 32 + (lane & 3) * 8;
    const char* rsrc = red + w * 15360 + lane * 16;
    u2v svv[5];
    f4v part[5][3];
#pragma unroll
    for (int dt = 0; dt < 5; ++dt) {
        svv[dt] = tr_read(ys + dt * 2048);
#pragma unroll
        for (int i = 0; i < 3; ++i) part[dt][i] = *(const f4v*)(rsrc + i * 5120 + dt * 1024);
    }
    __builtin_amdgcn_sched_barrier(0);
#pragma unroll
    for (int dt = 0; dt < 5; ++dt) {
        const u2v sv = svv[dt];
        const f4v g = (acc2[dt][0] + part[dt][0]) + (part[dt][1] + part[dt][2]);
        u2v o;
#pragma unroll
        for (int r2 = 0; r2 < 2; ++r2) {
            const float p0 = g[2 * r2], p1 = g[2 * r2 + 1];
            const float g0 = p0 * __builtin_amdgcn_rcpf(1.0f + __builtin_amdgcn_exp2f(-1.44269504f * p0));
            const float g1 = p1 * __builtin_amdgcn_rcpf(1.0f + __builtin_amdgcn_exp2f(-1.44269504f * p1));
            o[r2] = pkbf(fmaf(bflo(sv[r2]), g0, bflo(xv[dt][r2])), fmaf(bfhi(sv[r2]), g1, bfhi(xv[dt][r2])));
        }
        *(u2v*)(p.out + (size_t)t * D + dbase + dt * 16) = o;
    }
    SG(5) SGDUMP(6)
    TLE(1 << 20)
}

extern "C" void kernel_launch(void* const* d_in, const int* in_sizes, int n_in, void* d_out, int out_size, void* d_ws,
                              size_t ws_size, hipStream_t stream) {
    Params p;
    p.x = (const u16*)d_in[0];
    p.Are = (const u16*)d_in[1]; p.Aim = (const u16*)d_in[2]; p.Bre = (const u16*)d_in[3]; p.Bim = (const u16*)d_in[4];
    p.Cre = (const u16*)d_in[5]; p.Cim = (const u16*)d_in[6]; p.logdt = (const u16*)d_in[7]; p.Dskip = (const u16*)d_in[8];
    p.w1 = (const u16*)d_in[10]; p.b1 = (const u16*)d_in[11]; p.w2 = (const u16*)d_in[12];
    p.out = (u16*)d_out;
    p.ws = (char*)d_ws;
    (void)hipFuncSetAttribute((const void*)k1, hipFuncAttributeMaxDynamicSharedMemorySize, LDS_BYTES);
    (void)hipFuncSetAttribute((const void*)k2, hipFuncAttributeMaxDynamicSharedMemorySize, K2_LDS);
    k1<<<512, 512, LDS_BYTES, stream>>>(p);
    k2<<<256, 256, K2_LDS, stream>>>(p);
}
```

```cpp
#include <hip/hip_runtime.h>
#include <stdint.h>

#if defined(__HIP_DEVICE_COMPILE__) && !defined(ALLOWPK)
#define NOPK __attribute__((target("no-packed-fp32-ops")))
#else
#define NOPK
#endif
typedef unsigned short u16;
typedef unsigned int u32;
typedef short s4v __attribute__((ext_vector_type(4)));
typedef u32 u2v __attribute__((ext_vector_type(2)));
typedef u32 u4v __attribute__((ext_vector_type(4)));
typedef float f2v __attribute__((ext_vector_type(2)));
typedef float f4v __attribute__((ext_vector_type(4)));
typedef _Float16 h2v __attribute__((ext_vector_type(2)));
typedef _Float16 h8v __attribute__((ext_vector_type(8)));
typedef __bf16 b2v __attribute__((ext_vector_type(2)));
typedef __bf16 b8v __attribute__((ext_vector_type(8)));
#define LDSP(T, p) ((__attribute__((address_space(3))) T*)(p))
#define GLB(T, p) ((__attribute__((address_space(1))) T*)(p))

static constexpr int L = 2048, D = 640, NS = 128, F = 1728;
static constexpr int KS2 = F / 32;

static constexpr size_t WS_ACT = 0;
static constexpr size_t WS_W2P = WS_ACT + (size_t)L * F * 2;
static constexpr int REC_K = 32, REC_X = 4160 + 512, REC = 8768;
static constexpr size_t WS_REC = WS_W2P + (size_t)F * D * 2;
static constexpr size_t WS_YT = WS_REC + (size_t)641 * REC + 192;
static constexpr size_t WS_CNT = WS_YT + (size_t)L * D * 2;
static_assert(WS_REC % 256 == 0 && WS_YT % 256 == 0, "alignment");

static constexpr int LDS_BYTES = 80 * 1024;

__device__ __forceinline__ NOPK u32 f2u(float v) { return __builtin_bit_cast(u32, v); }
__device__ __forceinline__ NOPK float bf2f(u32 v) { return __builtin_bit_cast(float, (u32)(v << 16)); }
__device__ __forceinline__ NOPK float bflo(u32 v) { return __builtin_bit_cast(float, (u32)(v << 16)); }
__device__ __forceinline__ NOPK float bfhi(u32 v) { return __builtin_bit_cast(float, (u32)(v & 0xffff0000u)); }
__device__ __forceinline__ NOPK u32 pkbf(float a, float b) {
    f2v v = {a, b};
    return __builtin_bit_cast(u32, __builtin_convertvector(v, b2v));
}
__device__ __forceinline__ NOPK u32 pkh(float a, float b) {
    f2v v = {a, b};
    return __builtin_bit_cast(u32, __builtin_convertvector(v, h2v));
}
__device__ __forceinline__ NOPK float rbf(float a) { return bflo(pkbf(a, a)); }
__device__ __forceinline__ NOPK f4v mfma_bf(u4v a, u4v b, f4v c) {
    return __builtin_amdgcn_mfma_f32_16x16x32_bf16(__builtin_bit_cast(b8v, a), __builtin_bit_cast(b8v, b), c, 0, 0, 0);
}
__device__ __forceinline__ NOPK f4v mfma_h(u4v a, u4v b, f4v c) {
    return __builtin_amdgcn_mfma_f32_16x16x32_f16(__builtin_bit_cast(h8v, a), __builtin_bit_cast(h8v, b), c, 0, 0, 0);
}
__device__ __forceinline__ NOPK u2v tr_read(const char* p) {
    return __builtin_bit_cast(u2v, __builtin_amdgcn_ds_read_tr16_b64_v4i16(LDSP(s4v, p)));
}
__device__ __forceinline__ NOPK float dot2bf(u32 a, u32 b, float c) {
    return __builtin_amdgcn_fdot2_f32_bf16(__builtin_bit_cast(b2v, a), __builtin_bit_cast(b2v, b), c, false);
}
__device__ __forceinline__ NOPK void glds16(const void* g, void* l) {
    __builtin_amdgcn_global_load_lds((const __attribute__((address_space(1))) void*)g,
                                     (__attribute__((address_space(3))) void*)l, 16, 0, 0);
}
__device__ __forceinline__ NOPK u32 lds_addr(const void* l) { return (u32)(size_t)(__attribute__((address_space(3))) const char*)l; }
#define GLDS16(ldsa, voff, sbase) \
    asm volatile("s_mov_b32 m0, %0\n\ts_nop 0\n\tglobal_load_lds_dwordx4 %1, %2" ::"s"(ldsa), "v"(voff), "s"(sbase) : "memory")
#define GLDS4(ldsa, voff, sbase) \
    asm volatile("s_mov_b32 m0, %0\n\ts_nop 0\n\tglobal_load_lds_dword %1, %2" ::"s"(ldsa), "v"(voff), "s"(sbase) : "memory")
template <int CTRL> __device__ __forceinline__ NOPK float dpp(float v) {
    return __builtin_bit_cast(float, __builtin_amdgcn_update_dpp(0, __builtin_bit_cast(int, v), CTRL, 0xf, 0xf, true));
}
__device__ __forceinline__ NOPK float swap32(float v) {
    const u32 b = __builtin_bit_cast(u32, v);
    const auto r = __builtin_amdgcn_permlane32_swap(b, b, false, false);
    return __builtin_bit_cast(float, (threadIdx.x & 32) ? r[0] : r[1]);
}
__device__ __forceinline__ NOPK float red16(float v) {
    v += dpp<0xB1>(v); v += dpp<0x4E>(v); v += dpp<0x141>(v); v += dpp<0x140>(v);
    return v;
}
__device__ __forceinline__ NOPK float max64(float v) {
    v = fmaxf(v, dpp<0xB1>(v)); v = fmaxf(v, dpp<0x4E>(v)); v = fmaxf(v, dpp<0x141>(v)); v = fmaxf(v, dpp<0x140>(v));
    const int b = __builtin_bit_cast(int, v);
    float a0 = __builtin_bit_cast(float, __builtin_amdgcn_readlane(b, 0)), a1 = __builtin_bit_cast(float, __builtin_amdgcn_readlane(b, 16));
    float a2 = __builtin_bit_cast(float, __builtin_amdgcn_readlane(b, 32)), a3 = __builtin_bit_cast(float, __builtin_amdgcn_readlane(b, 48));
    return fmaxf(fmaxf(a0, a1), fmaxf(a2, a3));
}
__device__ __forceinline__ NOPK void st16(void* ptr, u4v v) {
    asm volatile("global_store_dwordx4 %0, %1, off sc1\n\ts_nop 1" ::"v"(ptr), "v"(v) : "memory");
}
__device__ __forceinline__ NOPK void ln_stats(float s1, float s2, float& mb, float& den) {
    float mean = s1 * (1.0f / D);
    float var = fmaxf(s2 * (1.0f / D) - mean * mean, 0.f);
    mb = rbf(mean);
    den = rbf(__builtin_amdgcn_sqrtf(rbf(rbf(var) + 1e-5f)));
}
__device__ __forceinline__ NOPK float ln_apply(float x, float mb, float rden) { return rbf(rbf(x - mb) * rden); }

#define SG(i) {}
#define SGDECL
#define SGDUMP(n)
#define TLB()
#define TLE(off)
struct Params {
    const u16 *x, *Are, *Aim, *Bre, *Bim, *Cre, *Cim, *logdt, *Dskip, *w1, *b1, *w2;
    u16* out;
    char* ws;
};

typedef float f16v __attribute__((ext_vector_type(16)));
__device__ __forceinline__ NOPK f16v mfma32_bf(u4v a, u4v b, f16v c) {
    return __builtin_amdgcn_mfma_f32_32x32x16_bf16(__builtin_bit_cast(b8v, a), __builtin_bit_cast(b8v, b), c, 0, 0, 0);
}
#define STATV 0
#define ACTST 0
#define VMWAIT(N) asm volatile("s_waitcnt vmcnt(" #N ") lgkmcnt(0)" ::: "memory")

#define RAWBAR() { asm volatile("s_waitcnt lgkmcnt(0)" ::: "memory"); __builtin_amdgcn_s_barrier(); asm volatile("" ::: "memory"); }

__device__ __forceinline__ NOPK void gemm1_role(const Params& p, int b, char* smem) {
    const int tid = threadIdx.x, lane = tid & 63;
    const int w = __builtin_amdgcn_readfirstlane(tid >> 6);
    const int wt = w >> 2, wf = w & 3;
    const int xcd = b & 7, j = b >> 3;
    const int mb = 4 * (xcd >> 1) + (j & 3), nt = 7 * (xcd & 1) + (j >> 2);
    const int l31 = lane & 31, hh = lane >> 5;
    const bool half = (nt == 13);
    SGDECL
#define STOFF 0
    SG(0)
    u32 xo[2], wo;
#pragma unroll
    for (int i = 0; i < 2; ++i) {
        int P = (2 * w + i) * 64 + lane, row = P >> 3, ch = (P & 7) ^ ((row >> 1) & 7);
        xo[i] = (u32)(row * (D * 2) + ch * 16);
    }
    {
        int P = w * 64 + lane, k = P >> 4, ch = (P & 15) ^ ((k & 3) << 2);
        if (half) ch &= 7;
        wo = (u32)(k * (F * 2) + ch * 16);
    }
    const char* xb = (const char*)p.x + (size_t)mb * 128 * (D * 2);
    const char* wb = (const char*)p.w1 + nt * 256;
    const u32 lx = lds_addr(smem) + w * 2048, lw = lds_addr(smem) + 49152 + w * 1024;
#define SROT(st) (st)
#define G1_X(st) { const char* s_ = xb + SROT(st) * 128; GLDS16(lx + ((st) % 3) * 16384, xo[0], s_); GLDS16(lx + ((st) % 3) * 16384 + 1024, xo[1], s_); }
#define G1_W(h) { const char* s_ = wb + (size_t)(SROT((h) >> 1) * 64 + ((h)&1) * 32) * (F * 2); GLDS16(lw + ((h)&3) * 8192, wo, s_); }
    G1_W(0) G1_X(0)
    u32 bia;
    asm volatile("global_load_ushort %0, %1, %2" : "=v"(bia) : "v"((u32)min(nt * 128 + 32 * wf + l31, F - 1) * 2u), "s"(p.b1) : "memory");
    G1_W(1) G1_X(1) G1_W(2) G1_W(3) G1_X(2)

    const int q = (lane & 15) >> 2, pp = lane & 3, blk = (lane >> 4) & 1;
    const int ro = (wf & 1) ^ wt, ri = wf >> 1;
    const char* xa[4][2];
    const char* wa[2];
#pragma unroll
    for (int u = 0; u < 4; ++u)
#pragma unroll
        for (int i = 0; i < 2; ++i)
            xa[u][i] = smem + (64 * wt + 32 * (i ^ ri) + l31) * 128 + (((2 * (u ^ ro) + hh) ^ ((l31 >> 1) & 7)) << 4);
#pragma unroll
    for (int o = 0; o < 2; ++o)
        wa[o] = smem + 49152 + (16 * (o ^ ro) + 8 * hh + q) * 256 + (((4 * wf + 2 * blk + (pp >> 1)) ^ (q << 2)) << 4) + 8 * (pp & 1);

    f16v acc[2];
#pragma unroll
    for (int i = 0; i < 16; ++i) acc[0][i] = acc[1][i] = 0.f;
    u4v xf[2][2][2], wfr[2][2];
    float s1a = 0.f, s1b = 0.f, s2a = 0.f, s2b = 0.f, csa = 0.f, csb = 0.f;
    SG(1)
#define G1_READ(h, o)                                                                                    \
    {                                                                                                    \
        u2v lo = tr_read(wa[o] + ((h)&3) * 8192), hi = tr_read(wa[o] + ((h)&3) * 8192 + 1024);           \
        wfr[(h)&1][o] = (u4v){lo[0], lo[1], hi[0], hi[1]};                                               \
        xf[(h)&1][o][0] = *(const u4v*)(xa[((h)&1) * 2 + o][0] + (((h) >> 1) % 3) * 16384);              \
        xf[(h)&1][o][1] = *(const u4v*)(xa[((h)&1) * 2 + o][1] + (((h) >> 1) % 3) * 16384);              \
    }
#define G1_M(h, o, i)                                                                                    \
    if ((h) >= 0) {                                                                                      \
        acc[i] = mfma32_bf(wfr[(h)&1][o], xf[(h)&1][o][i], acc[i]);                                      \
        asm volatile("" : "+v"(acc[i]));                                                                 \
    }                                                                                                    \
    __builtin_amdgcn_sched_barrier(0);
#define G1_STAT(h, e)                                                                                    \
    if ((h) >= 0) {                                                                                      \
        const u32 xv_ = xf[(h)&1][0][0][e];                                                              \
        s1a = dot2bf(xv_, 0x3f803f80u, s1a); s2a = dot2bf(xv_, xv_, s2a);                                \
        csa = dot2bf(wfr[(h)&1][0][e], 0x3f803f80u, csa);                                                \
    }
#define G1_PIN() asm volatile("" : "+v"(s1a), "+v"(s1b), "+v"(s2a), "+v"(s2b), "+v"(csa), "+v"(csb)); __builtin_amdgcn_sched_barrier(0);
#define G1_IT(h, NW)                                                                                     \
    {                                                                                                    \
        VMWAIT(NW);                                                                                      \
        __builtin_amdgcn_s_barrier();                                                                    \
        G1_M(h, 0, 0)                                                                                    \
        if ((h) >= 0 && (h) + 4 <= 19) G1_W((h) + 4)                                                     \
        if ((h) >= 1 && ((h)&1) && ((h) - 1) / 2 + 3 <= 9) G1_X(((h) - 1) / 2 + 3)                       \
        G1_M(h, 0, 1)                                                                                    \
        G1_READ((h) + 1, 0) G1_STAT(h, 0) G1_PIN()                                                       \
        G1_M(h, 1, 0)                                                                                    \
        G1_READ((h) + 1, 1) G1_STAT(h, 1) G1_PIN()                                                       \
        G1_M(h, 1, 1)                                                                                    \
        G1_STAT(h, 2) G1_STAT(h, 3) G1_PIN()                                                             \
    }
    G1_IT(-1, 8) G1_IT(0, 6) G1_IT(1, 4) G1_IT(2, 6) G1_IT(3, 4) SG(2) G1_IT(4, 6) G1_IT(5, 4) G1_IT(6, 6) G1_IT(7, 4)
    G1_IT(8, 6) SG(3) G1_IT(9, 4) G1_IT(10, 6) G1_IT(11, 4) G1_IT(12, 6) G1_IT(13, 4) SG(4) G1_IT(14, 6) G1_IT(15, 4)
    G1_IT(16, 4) G1_IT(17, 1) G1_IT(18, 0)
    G1_M(19, 0, 0) G1_M(19, 0, 1) G1_M(19, 1, 0) G1_M(19, 1, 1) G1_STAT(19, 0) G1_STAT(19, 1) G1_STAT(19, 2) G1_STAT(19, 3)
    s1a += s1b; s2a += s2b; csa += csb;
    SG(5)
    asm volatile("" : "+v"(bia));
    char* rsb = smem + 16384;
    char* csl = smem + 16384 + 2048;
    {
        const float a1 = s1a + swap32(s1a), a2 = s2a + swap32(s2a), ac = csa + swap32(csa);
        if (hh == 0) *(f2v*)(rsb + ((64 * wt + 32 * ri + l31) * 2 + (wf & 1)) * 8) = (f2v){a1, a2};
        else *(float*)(csl + ((32 * wf + l31) * 2 + wt) * 4) = ac;
    }
    RAWBAR()
    SG(6)
    if (!(half && wf >= 2)) {
        u4v ca = {0u, 0u, 0u, 0u};
        {
            const f2v c2 = *(const f2v*)(csl + (32 * wf + l31) * 8);
            const float cs = c2[0] + c2[1];
            const float c1 = rbf(cs), r1 = cs - c1, cm = rbf(r1);
            if (hh == 0) { ca[0] = pkbf(c1, cm); ca[1] = (pkbf(r1 - cm, 0.f) & 0xffffu) | (bia << 16); }
        }
        float rdl[2];
        {
            const f4v u0 = *(const f4v*)(rsb + (64 * wt + 32 * (hh ^ ri) + l31) * 16);
            float mean, den;
            ln_stats(u0[0] + u0[2], u0[1] + u0[3], mean, den);
            const float rd = __builtin_amdgcn_rcpf(den) * 1.44269504f;
            const u32 nm = f2u(-mean) >> 16, pk0 = nm | (nm << 16), pk1 = nm | (f2u(den) & 0xffff0000u);
            const float o0 = swap32(__builtin_bit_cast(float, pk0)), o1 = swap32(__builtin_bit_cast(float, pk1)), ord = swap32(rd);
            const u4v cb0 = {hh ? 0u : pk0, hh ? 0u : pk1, 0u, 0u}, cb1 = {hh ? 0u : f2u(o0), hh ? 0u : f2u(o1), 0u, 0u};
            acc[0] = mfma32_bf(ca, cb0, acc[0]);
            acc[1] = mfma32_bf(ca, cb1, acc[1]);
            rdl[0] = hh ? ord : rd;
            rdl[1] = hh ? rd : ord;
        }
        char* actb = p.ws + WS_ACT + ((size_t)(mb * 2 + wt) * KS2 + nt * 4 + wf) * 4096 + (size_t)((l31 >> 4) * 1024 + ((l31 & 15) + 16 * hh) * 16);
#pragma unroll
        for (int i = 0; i < 2; ++i) {
#pragma unroll
            for (int c = 0; c < 2; ++c) {
                u4v o;
#pragma unroll
                for (int e = 0; e < 4; ++e) {
                    float r[2];
#pragma unroll
                    for (int z = 0; z < 2; ++z) {
                        const float u = acc[i][8 * c + 2 * e + z] * rdl[i];
                        const float e1 = __builtin_amdgcn_exp2f(u) + 1.0f;
                        r[z] = u * fmaf(__builtin_amdgcn_rcpf(fmaf(e1, e1, 1.0f)), -1.38629436f, 0.69314718f);
                    }
                    o[e] = pkbf(r[0], r[1]);
                }
                st16(actb + (i ^ ri) * 2048 + c * 512, o);
            }
        }
    }
    SG(7)
    SGDUMP(8)
}

__device__ __forceinline__ NOPK void ln_role(const Params& p, int rb, char* smem) {
    const int tid = threadIdx.x, lane = tid & 63;
    const int w = __builtin_amdgcn_readfirstlane(tid >> 6);
    const int rl = 4 * w + (lane >> 4), t = rb * 32 + rl, c15 = lane & 15;
    u4v v[5];
#pragma unroll
    for (int i = 0; i < 5; ++i) v[i] = *(const u4v*)(p.x + (size_t)t * D + (c15 + 16 * i) * 8);
    float s = 0.f;
#pragma unroll
    for (int i = 0; i < 5; ++i)
#pragma unroll
        for (int e = 0; e < 4; ++e) s += bflo(v[i][e]) + bfhi(v[i][e]);
    const float mean = red16(s) * (1.0f / D);
    float qv = 0.f;
#pragma unroll
    for (int i = 0; i < 5; ++i)
#pragma unroll
        for (int e = 0; e < 4; ++e) {
            float a = bflo(v[i][e]) - mean, c = bfhi(v[i][e]) - mean;
            qv = fmaf(a, a, qv); qv = fmaf(c, c, qv);
        }
    const float var = red16(qv) * (1.0f / D);
    const float mbf = rbf(mean);
    const float rd = 1.0f / rbf(__builtin_sqrtf(rbf(rbf(var) + 1e-5f)));
#pragma unroll
    for (int i = 0; i < 5; ++i) {
        u2v o0, o1;
        o0[0] = pkh(ln_apply(bflo(v[i][0]), mbf, rd), ln_apply(bfhi(v[i][0]), mbf, rd));
        o0[1] = pkh(ln_apply(bflo(v[i][1]), mbf, rd), ln_apply(bfhi(v[i][1]), mbf, rd));
        o1[0] = pkh(ln_apply(bflo(v[i][2]), mbf, rd), ln_apply(bfhi(v[i][2]), mbf, rd));
        o1[1] = pkh(ln_apply(bflo(v[i][3]), mbf, rd), ln_apply(bfhi(v[i][3]), mbf, rd));
        *(u2v*)(smem + rl * 1288 + (c15 + 16 * i) * 16) = o0;
        *(u2v*)(smem + rl * 1288 + (c15 + 16 * i) * 16 + 8) = o1;
    }
    RAWBAR()
    const int G = lane >> 4, q = c15 >> 2, pp = lane & 3;
    const char* src = smem + (16 * (G >> 1) + 4 * q + 2 * (G & 1)) * 1288 + pp * 8;
    char* dst = p.ws + WS_REC + (size_t)c15 * REC + REC_X + (size_t)rb * 64 + (G ^ ((rb >> 2) & 1)) * 16;
#pragma unroll
    for (int it = 0; it < 5; ++it) {
        const int cg = w * 5 + it;
        u2v lo = tr_read(src + cg * 32), hi = tr_read(src + cg * 32 + 1288);
        st16(dst + (size_t)cg * 16 * REC, (u4v){lo[0], lo[1], hi[0], hi[1]});
    }
}

struct cplx2 { f2v r, i; };
__device__ __forceinline__ NOPK cplx2 cmul(cplx2 a, cplx2 b) {
    cplx2 o;
    o.r = a.r * b.r - a.i * b.i;
    o.i = a.r * b.i + a.i * b.r;
    return o;
}
struct cplxh { h2v r, i; };
__device__ __forceinline__ NOPK cplxh toh(cplx2 a) { return {__builtin_convertvector(a.r, h2v), __builtin_convertvector(a.i, h2v)}; }
__device__ __forceinline__ NOPK cplx2 cexp2(f2v zr, f2v zi, float c) {
    cplx2 o;
#pragma unroll
    for (int m = 0; m < 2; ++m) {
        float mag = __builtin_amdgcn_exp2f(zr[m] * c * 1.44269504089f);
        float ph = zi[m] * c;
        const float CH = 0.15915494f, CL = 6.4206382e-9f;
        float hi = ph * CH;
        float lo = fmaf(ph, CH, -hi) + ph * CL;
        float fr = __builtin_amdgcn_fractf(hi) + lo;
        o.r[m] = mag * __builtin_amdgcn_cosf(fr);
        o.i[m] = mag * __builtin_amdgcn_sinf(fr);
    }
    return o;
}
static constexpr int KG_ROW = 544;

__device__ __forceinline__ NOPK void kgen_role(const Params& p, int d, int part, char* sw  ) {
    const int lane = threadIdx.x & 63, l15 = lane & 15, q4 = lane >> 4;
    const int o = d * NS + 2 * lane;
    u32 are = *(const u32*)(p.Are + o), aim = *(const u32*)(p.Aim + o), bre = *(const u32*)(p.Bre + o);
    u32 bim = *(const u32*)(p.Bim + o), cre = *(const u32*)(p.Cre + o), cim = *(const u32*)(p.Cim + o);
    float dt = rbf(expf(bf2f(p.logdt[d])));
    f2v zr = {dt * bflo(are), dt * bfhi(are)}, zi = {dt * bflo(aim), dt * bfhi(aim)};
    cplx2 dB = {{dt * bflo(bre), dt * bfhi(bre)}, {dt * bflo(bim), dt * bfhi(bim)}};
    cplx2 C = {{bflo(cre), bfhi(cre)}, {bflo(cim), bfhi(cim)}};
    cplx2 W = cmul(C, dB);
    float wm = fmaxf(fmaxf(fabsf(W.r[0]), fabsf(W.r[1])), fmaxf(fabsf(W.i[0]), fabsf(W.i[1])));
    const float dsk = bf2f(p.Dskip[d]);
    wm = fmaxf(max64(wm), fabsf(dsk) * (1.0f / 128.0f));
    int eU = 14 - __builtin_amdgcn_frexp_expf(wm);
    eU = min(max(eU, -100), 100);
    const float sU = __builtin_amdgcn_ldexpf(1.0f, eU);
    W.r *= sU; W.i *= sU;
    char* rec = p.ws + WS_REC + (size_t)d * REC;
    if (part == 0 && lane < 34) {
        u4v z = {0u, 0u, 0u, 0u};
        if (lane == 0) z[0] = f2u(__builtin_amdgcn_ldexpf(1.0f, 8 - eU));
        *(u4v*)(rec + (lane < 2 ? lane * 16 : 4160 + (lane - 2) * 16)) = z;
    }
    const float KSC = 2.384185791015625e-07f;

    char* r0 = sw;
    char* kc = sw + 16 * KG_ROW;
#define KG_PUT(row, v) *(u2v*)(r0 + (row)*KG_ROW + lane * 8) = (u2v){pkh((v).r[0], (v).r[1]), pkh((v).i[0], (v).i[1])}
#define KG_PUTH(row, T_, E_)                                                                            \
    *(u2v*)(r0 + (row)*KG_ROW + lane * 8) = (u2v){__builtin_bit_cast(u32, __builtin_elementwise_fma(T_.r, E_.r, -(T_.i * E_.i))), \
                                                  __builtin_bit_cast(u32, __builtin_elementwise_fma(T_.r, E_.i, T_.i * E_.r))}
#define KG_MMA(A)                                         \
    A = (f4v){0.f, 0.f, 0.f, 0.f};                        \
    _Pragma("unroll") for (int ks = 0; ks < 8; ++ks) A = mfma_h(*(const u4v*)(r0 + l15 * KG_ROW + ks * 64 + q4 * 16), vf[ks], A);
    const cplx2 G = cexp2(zr, zi, 128.f);
    u16* kch = (u16*)kc;
    char* kx = rec + REC_K;
    u4v vf[8];
    if (part == 0) {
        {
            cplx2 v1 = cexp2(zr, zi, 8.f);
            cplx2 v = {{16384.f, 16384.f}, {0.f, 0.f}};
#pragma unroll
            for (int jj = 0; jj < 16; ++jj) {
                cplx2 t = {v.r, -v.i};
                KG_PUT(jj, t);
                if (jj < 15) v = cmul(v, v1);
            }
        }
        RAWBAR()
#pragma unroll
        for (int ks = 0; ks < 8; ++ks) vf[ks] = *(const u4v*)(r0 + l15 * KG_ROW + ks * 64 + q4 * 16);
        cplx2 E[8];
        E[1] = cexp2(zr, zi, 1.f);
        E[2] = cmul(E[1], E[1]); E[3] = cmul(E[2], E[1]); E[4] = cmul(E[2], E[2]);
        E[5] = cmul(E[4], E[1]); E[6] = cmul(E[4], E[2]); E[7] = cmul(E[4], E[3]);
        const cplxh T0 = toh(W), T1 = toh(cmul(W, G));
        RAWBAR()
        *(u2v*)(r0 + lane * 8) = (u2v){__builtin_bit_cast(u32, T0.r), __builtin_bit_cast(u32, T0.i)};
        *(u2v*)(r0 + 8 * KG_ROW + lane * 8) = (u2v){__builtin_bit_cast(u32, T1.r), __builtin_bit_cast(u32, T1.i)};
#pragma unroll
        for (int c = 1; c < 8; ++c) {
            const cplxh Eh = toh(E[c]);
            KG_PUTH(c, T0, Eh);
            KG_PUTH(8 + c, T1, Eh);
        }
        f4v acc;
        KG_MMA(acc)
        if (lane == 0) acc[0] = fmaf(dsk * sU, 16384.f, acc[0]);
        *(u2v*)(kx + (128 * (q4 >> 1) + 4 * (q4 & 1) + 8 * l15) * 2) = (u2v){pkh(acc[0] * KSC, acc[1] * KSC), pkh(acc[2] * KSC, acc[3] * KSC)};
    } else {
        const cplx2 E2 = cexp2(zr, zi, 2.f);
        const cplx2 E4 = cmul(E2, E2);
        const cplxh E2h = toh(E2), E4h = toh(E4), E6h = toh(cmul(E4, E2));
        cplx2 T = cmul(W, cexp2(zr, zi, 256.f));
#pragma unroll
        for (int a = 0; a < 6; ++a) {
            const cplxh Th = toh(T);
            const int row = a < 2 ? 4 * a : 4 + 2 * a;
            *(u2v*)(r0 + row * KG_ROW + lane * 8) = (u2v){__builtin_bit_cast(u32, Th.r), __builtin_bit_cast(u32, Th.i)};
            if (a < 2) { KG_PUTH(row + 1, Th, E2h); KG_PUTH(row + 2, Th, E4h); KG_PUTH(row + 3, Th, E6h); }
            else KG_PUTH(row + 1, Th, E4h);
            if (a < 5) T = cmul(T, G);
        }
        RAWBAR()
#pragma unroll
        for (int ks = 0; ks < 8; ++ks) vf[ks] = *(const u4v*)(r0 - 10240 + l15 * KG_ROW + ks * 64 + q4 * 16);
        RAWBAR()
        f4v acc1, acc2;
        KG_MMA(acc1)
        T = cmul(W, cexp2(zr, zi, 1024.f));
#pragma unroll
        for (int a = 0; a < 9; ++a) {
            KG_PUT(a, T);
            if (a < 8) T = cmul(T, G);
        }
        KG_MMA(acc2)
        {
            u32 h01 = pkh(acc1[0] * KSC, acc1[1] * KSC), h23 = pkh(acc1[2] * KSC, acc1[3] * KSC);
            if (q4 < 2) {
                *(u2v*)(kc + (64 * q4 + 4 * l15) * 2) = (u2v){h01, h23};
            } else {
                *(u32*)(kc + (128 + 64 * (q4 - 2) + 2 * l15) * 2) = h01;
                *(u32*)(kc + (128 + 64 * (q4 - 2) + 32 + 2 * l15) * 2) = h23;
            }
            u32 g01 = pkh(acc2[0] * KSC, acc2[1] * KSC), g23 = pkh(acc2[2] * KSC, acc2[3] * KSC);
            if (q4 < 2) {
                kch[256 + 64 * q4 + l15] = (u16)g01;
                kch[256 + 64 * q4 + 16 + l15] = (u16)(g01 >> 16);
                kch[256 + 64 * q4 + 32 + l15] = (u16)g23;
                kch[256 + 64 * q4 + 48 + l15] = (u16)(g23 >> 16);
            } else if (lane == 32) {
                kch[384] = (u16)g01;
            }
        }
        if (lane < 32) {
            u2v s = *(const u2v*)(kc + lane * 8);
            u32 s4 = kch[lane * 4 + 4];
            u4v b2;
            b2[0] = (s[0] & 0xffffu) | (s[0] << 16);
            b2[1] = (s[0] >> 16) | (s[1] << 16);
            b2[2] = (s[1] & 0xffffu) | (s[1] << 16);
            b2[3] = (s[1] >> 16) | (s4 << 16);
            st16(kx + 512 + lane * 16, b2);
        }
        {
            u32 s = *(const u32*)(kc + 256 + lane * 4);
            u32 c = kch[128 + lane * 2 + 2];
            u4v v;
            v[0] = (s & 0xffffu) | (s << 16);
            v[1] = s;
            v[2] = (s >> 16) | (s & 0xffff0000u);
            v[3] = c | (c << 16);
            st16(kx + 1024 + lane * 16, v);
        }
#pragma unroll
        for (int it = 0; it < 2; ++it) {
            int oc = lane + 64 * it;
            u32 a = kch[256 + oc], c = kch[257 + oc];
            u4v v;
            v[0] = a | (a << 16);
            v[1] = v[0];
            v[2] = ((oc & 1) ? c : a) | (c << 16);
            v[3] = c | (c << 16);
            st16(kx + 2048 + oc * 16, v);
        }
    }
}

__device__ __forceinline__ NOPK void w2pack_role(const Params& p, int unit, char* sw) {
    const int lane = threadIdx.x & 63, l15 = lane & 15, q4 = lane >> 4;
    const int nb2 = unit & 3, ks = unit >> 2;
    const char* src = (const char*)(p.w2 + (size_t)ks * 32 * D + nb2 * 160);
#pragma unroll
    for (int i = 0; i < 10; ++i) {
        const int c = lane + 64 * i, row = c / 20, ch = c - row * 20;
        GLDS16(lds_addr(sw) + i * 1024, (u32)(row * (D * 2) + ch * 16), src);
    }
    __builtin_amdgcn_s_barrier();
    __builtin_amdgcn_s_barrier();
    asm volatile("s_waitcnt vmcnt(0)" ::: "memory");
    const int q = l15 >> 2, pp = lane & 3;
    char* dst = p.ws + WS_W2P + ((size_t)(2 * nb2) * KS2 + ks) * 5120 + lane * 16;
    const char* rd = sw + (16 * (q4 >> 1) + 4 * (q4 & 1) + q) * 320 + pp * 8;
#pragma unroll
    for (int dt = 0; dt < 10; ++dt) {
        u2v lo = tr_read(rd + dt * 32);
        u2v hi = tr_read(rd + 8 * 320 + dt * 32);
        st16(dst + (dt >= 5 ? (size_t)KS2 * 5120 + (dt - 5) * 1024 : dt * 1024), (u4v){lo[0], lo[1], hi[0], hi[1]});
    }
}

__global__ __launch_bounds__(512, 4) NOPK void k1(Params p) {
    extern __shared__ __attribute__((aligned(16))) char smem[];
    const int b = blockIdx.x;
    const int w = __builtin_amdgcn_readfirstlane(threadIdx.x >> 6);
    TLB()
    if (b < 224) {
        gemm1_role(p, b, smem);
    } else if (b >= 256 && b < 480) {
        if (b >= 470) return;
        const int d = (b - 256) * 3 + (w >> 1);
        if (w < 6 && d < D) {
            kgen_role(p, d, w & 1, smem + w * 10240);
        } else {
            const int unit = w == 6 ? b - 256 : 214 + b - 256;
            if (w >= 6 && unit < 4 * KS2) {
                w2pack_role(p, unit, smem + 61440 + (w - 6) * 10240);
            } else {
                if (b == 469 && (threadIdx.x & 63) < 8 && w == 7) *(u32*)(p.ws + WS_CNT + (threadIdx.x & 63) * 256) = 0u;
                __builtin_amdgcn_s_barrier();
                __builtin_amdgcn_s_barrier();
            }
        }
    } else {
        const int jj = b < 256 ? (b - 224) >> 3 : 4 + ((b - 480) >> 3);
        ln_role(p, 16 * ((b & 7) >> 1) + 8 * (b & 1) + jj, smem);
    }
    TLE(0)
}

static constexpr int CV_WAVE = 9216;
static constexpr int K2_RED = 3 * CV_WAVE;
static constexpr int K2_YS = K2_RED + 61440;
static constexpr int K2_LDS = K2_YS + 10240;

#define AC0 "a"
#define AC1 "a"
#define AC2 "a"
#define AC3 "v"
#define AC4 "v"
#define AC5 "v"
#define G2_BUF(C, i) C AC##i(af[i][0]), C AC##i(af[i][1]), C AC##i(af[i][2]), C AC##i(af[i][3]), C "a"(wf[i][0]), C "a"(wf[i][1]), C "a"(wf[i][2]), C "a"(wf[i][3]), C "a"(wf[i][4])
#define G2_LOAD(i)                                                                                                   \
    asm volatile("buffer_load_dwordx4 %0, %9, %15, %17 offen\n\tbuffer_load_dwordx4 %4, %13, %16, %18 offen\n\t"     \
                 "buffer_load_dwordx4 %1, %10, %15, %17 offen\n\tbuffer_load_dwordx4 %2, %11, %15, %17 offen\n\t"    \
                 "buffer_load_dwordx4 %3, %12, %15, %17 offen\n\tbuffer_load_dwordx4 %5, %13, %16, %18 offen offset:1024\n\t" \
                 "buffer_load_dwordx4 %6, %13, %16, %18 offen offset:2048\n\tbuffer_load_dwordx4 %7, %13, %16, %18 offen offset:3072\n\t" \
                 "buffer_load_dwordx4 %8, %14, %16, %18 offen"                                                       \
                 : G2_BUF("=&", i) : "v"(ao[0]), "v"(ao[1]), "v"(ao[2]), "v"(ao[3]), "v"(ao[4]), "v"(ao[5]), "s"(ra), "s"(rw), \
                   "s"((i)*4096), "s"((i)*5120) : "memory");
#define G2_MF(d, w_, a_) "v_mfma_f32_16x16x32_bf16 %" #d ", %" #w_ ", %" #a_ ", %" #d "\n\t"
#define G2_M4(dt, i, VO, OFF)                                                                           \
    asm volatile(G2_MF(0, 4, 5) G2_MF(1, 4, 6) G2_MF(2, 4, 7) G2_MF(3, 4, 8)                            \
                 "buffer_load_dwordx4 %4, %9, %10, %11 offen offset:" #OFF                              \
                 : "+a"(acc2[dt][0]), "+a"(acc2[dt][1]), "+a"(acc2[dt][2]), "+a"(acc2[dt][3]), "+a"(wf[i][dt]) \
                 : AC##i(af[i][0]), AC##i(af[i][1]), AC##i(af[i][2]), AC##i(af[i][3]), "v"(ao[VO]), "s"(rw), "s"(sw) : "memory");
#define G2_BODY(i)                                                                                      \
    {                                                                                                   \
        G2_M4(0, i, 4, 0) G2_M4(1, i, 4, 1024) G2_M4(2, i, 4, 2048) G2_M4(3, i, 4, 3072) G2_M4(4, i, 5, 0) \
        asm volatile("buffer_load_dwordx4 %0, %4, %8, %9 offen\n\tbuffer_load_dwordx4 %1, %5, %8, %9 offen\n\t" \
                     "buffer_load_dwordx4 %2, %6, %8, %9 offen\n\tbuffer_load_dwordx4 %3, %7, %8, %9 offen" \
                     : "+" AC##i(af[i][0]), "+" AC##i(af[i][1]), "+" AC##i(af[i][2]), "+" AC##i(af[i][3])                   \
                     : "v"(ao[0]), "v"(ao[1]), "v"(ao[2]), "v"(ao[3]), "s"(ra), "s"(sa) : "memory");    \
    }
#define G2_STEP(i) { asm volatile("s_waitcnt vmcnt(45)" : G2_BUF("+", i)::"memory"); G2_BODY(i) sa += 4096; sw += 5120; }
#define G2_M4N(dt, i)                                                                                   \
    asm volatile(G2_MF(0, 4, 5) G2_MF(1, 4, 6) G2_MF(2, 4, 7) G2_MF(3, 4, 8) ""                         \
                 : "+a"(acc2[dt][0]), "+a"(acc2[dt][1]), "+a"(acc2[dt][2]), "+a"(acc2[dt][3])           \
                 : "a"(wf[i][dt]), AC##i(af[i][0]), AC##i(af[i][1]), AC##i(af[i][2]), AC##i(af[i][3]));
#define G2_SSTEP(k, i)                                                                                  \
    {                                                                                                   \
        if constexpr ((k) < 2) {                                                                        \
            const u32 sa = ((k) + 6) * 4096, sw = ((k) + 6) * 5120;                                     \
            G2_BODY(i)                                                                                  \
        } else {                                                                                        \
            G2_M4N(0, i) G2_M4N(1, i) G2_M4N(2, i) G2_M4N(3, i) G2_M4N(4, i)                            \
        }                                                                                               \
        __builtin_amdgcn_sched_barrier(0);                                                              \
    }
#define G2_ACC(a0) "+a"(acc2[a0][0]), "+a"(acc2[a0][1]), "+a"(acc2[a0][2]), "+a"(acc2[a0][3])
#define G2_ZERO()                                                                                       \
    _Pragma("unroll") for (int dt = 0; dt < 5; ++dt) _Pragma("unroll") for (int tt = 0; tt < 4; ++tt) acc2[dt][tt] = (f4v){0.f, 0.f, 0.f, 0.f}; \
    asm volatile("s_nop 1" : G2_ACC(0), G2_ACC(1), G2_ACC(2), G2_ACC(3), G2_ACC(4));
#undef STOFF
#define STOFF (4 << 20)
__device__ __forceinline__ NOPK void conv_wave(const Params& p, int d, u32 lb, char* img, u4v (&af)[6][4], u4v (&wf)[6][5],
                                                f4v (&acc2)[5][4], u4v ra, u4v rw, const u32 (&ao)[6], u32 svc, const u32* cnt,
                                                const char* yb, u32 ysl) {
    const int lane = threadIdx.x & 63, l15 = lane & 15, q4 = lane >> 4;
    const int q = l15 >> 2, pp = lane & 3;
    const u32 kra = lb + REC_K + (32 * q4 - 4 * q + 4 * pp) * 2;
    u32 xra[2][2][2];
#pragma unroll
    for (int ap = 0; ap < 2; ++ap)
#pragma unroll
        for (int h = 0; h < 2; ++h)
#pragma unroll
            for (int g = 0; g < 2; ++g) {
                int G = l15 + 8 * g - 2 * q4 - h + 8;
                xra[ap][h][g] = lb + 4160 + G * 32 + ((ap ^ ((G >> 3) & 1)) * 16);
            }
    const u32 csrc = (lane >= 16 ? lane - 16 : (lane == 0 ? 63 : lane + 47)) * 4;
    asm volatile("s_waitcnt vmcnt(5)" ::: "memory");
    u4v kf[16], xf[4];
    f4v acc[8];
    float Y[8][7];
    float scale;
    asm volatile("ds_read_b32 %0, %1" : "=v"(scale) : "v"(lb));
#define CV_XRD(s)                                                                                          \
    {                                                                                                      \
        u2v v0, v1;                                                                                        \
        asm volatile("ds_read_b64 %0, %2 offset:%4\n\tds_read_b64 %1, %3 offset:%4" : "=&v"(v0), "=&v"(v1) \
                     : "v"(xra[(s) >> 5][0][(s)&1]), "v"(xra[(s) >> 5][1][(s)&1]), "i"((((s)&15) >> 1) * 512 + (((s) >> 4) & 1) * 8)); \
        xf[(s)&3] = (u4v){v0[0], v0[1], v1[0], v1[1]};                                                     \
    }
    CV_XRD(0) CV_XRD(1) CV_XRD(2)
#pragma unroll
    for (int e = 0; e < 16; ++e) {
        u2v lo, hi;
        asm volatile("ds_read_b64_tr_b16 %0, %2 offset:%3\n\tds_read_b64_tr_b16 %1, %2 offset:%4" : "=&v"(lo), "=&v"(hi)
                     : "v"(kra), "i"(e * 256), "i"(e * 256 + 32));
        kf[e] = (u4v){lo[0], lo[1], hi[0], hi[1]};
    }
    asm volatile("s_waitcnt lgkmcnt(0)" : "+v"(kf[0]), "+v"(kf[1]), "+v"(kf[2]), "+v"(kf[3]), "+v"(kf[4]), "+v"(kf[5]), "+v"(kf[6]), "+v"(kf[7]),
                 "+v"(kf[8]), "+v"(kf[9]), "+v"(kf[10]), "+v"(kf[11]), "+v"(kf[12]), "+v"(kf[13]), "+v"(kf[14]), "+v"(kf[15]), "+v"(scale));
#define CV_STEP(s)                                                                                         \
    {                                                                                                      \
        constexpr int a = (s) >> 4, g = (s)&15;                                                            \
        if constexpr ((s) + 3 < 64) CV_XRD(((s) + 3) & 63)                                                 \
          \
        _Pragma("unroll") for (int e = (g + 1) & 1; e + g <= 15; e += 2) {                                 \
            if constexpr (g == 0) {                                                                        \
                if (e < 2) asm volatile("s_waitcnt lgkmcnt(6)\n\tv_mfma_f32_16x16x32_f16 %0, %1, %2, 0" : "=&v"(acc[(e + g) >> 1]) : "v"(kf[e]), "v"(xf[(s)&3])); \
                else asm volatile("v_mfma_f32_16x16x32_f16 %0, %1, %2, 0" : "=&v"(acc[(e + g) >> 1]) : "v"(kf[e]), "v"(xf[(s)&3])); \
            } else {                                                                                       \
                if (e < 2) asm volatile("s_waitcnt lgkmcnt(%3)\n\tv_mfma_f32_16x16x32_f16 %0, %1, %2, %0" : "+v"(acc[(e + g) >> 1]) : "v"(kf[e]), "v"(xf[(s)&3]), "i"((s) + 3 < 64 ? 6 : 2 * (63 - (s)))); \
                else asm volatile("v_mfma_f32_16x16x32_f16 %0, %1, %2, %0" : "+v"(acc[(e + g) >> 1]) : "v"(kf[e]), "v"(xf[(s)&3])); \
            }                                                                                              \
        }                                                                                                  \
        if constexpr (g == 15) {      \
            asm volatile("s_nop 7\n\ts_nop 3" : "+v"(acc[0]), "+v"(acc[1]), "+v"(acc[2]), "+v"(acc[3]), "+v"(acc[4]), "+v"(acc[5]), "+v"(acc[6]), "+v"(acc[7])); \
            _Pragma("unroll") for (int t = 0; t < 8; ++t) _Pragma("unroll") for (int r = 0; r < 4; ++r)    \
                Y[t][r + a] = (a == 0 || r == 3) ? acc[t][r] : Y[t][r + a] + acc[t][r];                    \
            __builtin_amdgcn_sched_barrier(0);                                                             \
        }                                                                                                  \
    }
#define CV_S4(s) CV_STEP(s) CV_STEP((s) + 1) CV_STEP((s) + 2) CV_STEP((s) + 3)
#define CV_S16(s) CV_S4(s) CV_S4((s) + 4) CV_S4((s) + 8) CV_S4((s) + 12)
#define CV_S8(s) CV_S4(s) CV_S4((s) + 4)
    CV_S16(0) G2_LOAD(0) CV_S8(16) G2_LOAD(1) CV_S8(24) G2_LOAD(2) CV_S8(32) G2_LOAD(3) CV_S8(40) G2_LOAD(4) CV_S8(48) G2_LOAD(5) CV_S8(56)
    float cr[8][3];
#pragma unroll
    for (int t = 0; t < 8; ++t)
#pragma unroll
        for (int r = 0; r < 3; ++r)
            cr[t][r] = __builtin_bit_cast(float, __builtin_amdgcn_ds_bpermute(csrc, __builtin_bit_cast(int, Y[t][4 + r])));
    __builtin_amdgcn_sched_barrier(0);
#pragma unroll
    for (int t = 0; t < 8; ++t) {
        float y[4];
#pragma unroll
        for (int r = 0; r < 3; ++r) y[r] = Y[t][r] + (lane == 0 ? (t ? cr[t - (t > 0)][r] : 0.f) : cr[t][r]);
        y[3] = Y[t][3];
        *(u2v*)(img + t * 512 + (16 * l15 + 4 * q4) * 2) = (u2v){pkbf(y[0] * scale, y[1] * scale), pkbf(y[2] * scale, y[3] * scale)};
    }
    char* dst = p.ws + WS_YT + (size_t)d * 4096 + lane * 16;
    {
        u4v o[4];
#pragma unroll
        for (int i = 0; i < 4; ++i) o[i] = *(const u4v*)(img + i * 1024 + lane * 16);
#pragma unroll
        for (int i = 0; i < 4; ++i)
            asm volatile("global_store_dwordx4 %0, %1, off offset:%2 sc1\n\ts_nop 1" ::"v"(dst), "v"(o[i]), "i"(i * 1024) : "memory");
    }
    G2_ZERO()
    asm volatile("s_waitcnt vmcnt(0)\n\ts_mov_b64 exec, 1\n\tglobal_atomic_add %0, %1, %2\n\ts_mov_b64 exec, -1"
                 ::"v"(0u), "v"(1u), "s"(p.ws + WS_CNT + (d / 80) * 256) : "memory");
    asm volatile("" : G2_BUF("+", 0), G2_BUF("+", 1), G2_BUF("+", 2)::"memory");
    asm volatile("" : G2_BUF("+", 3), G2_BUF("+", 4), G2_BUF("+", 5)::"memory");
    G2_SSTEP(0, 0) G2_SSTEP(1, 1) G2_SSTEP(2, 2) G2_SSTEP(3, 3)
    u32 cv = 0;
    asm volatile("s_cmp_lg_u32 %1, 0\n\ts_cbranch_scc1 .Lsv%=\n\tglobal_load_dword %0, %0, %2 sc1\n.Lsv%=:" : "+v"(cv) : "s"(svc), "s"(cnt) : "memory", "scc");
    G2_SSTEP(4, 4) G2_SSTEP(5, 5)
    asm volatile("s_waitcnt vmcnt(0)" : "+v"(cv), G2_BUF("+", 0), G2_BUF("+", 1)::"memory");
    if (svc == 0) {
        unsigned spins = 0;
        while (__builtin_amdgcn_readfirstlane(cv) < 80u && ++spins < (1u << 22)) {
            __builtin_amdgcn_s_sleep(4);
            cv = __hip_atomic_load(cnt, __ATOMIC_RELAXED, __HIP_MEMORY_SCOPE_AGENT);
        }
        const u32 yo = (lane >> 3) * 4096 + (lane & 7) * 16;
#pragma unroll
        for (int i = 0; i < 10; ++i)
            asm volatile("s_mov_b32 m0, %0\n\ts_nop 0\n\tglobal_load_lds_dwordx4 %1, %2 sc1" ::"s"(ysl + i * 1024), "v"(yo), "s"(yb + i * 32768) : "memory");
    }
    G2_SSTEP(6, 0) G2_SSTEP(7, 1)
    asm volatile("s_waitcnt vmcnt(0)\n\ts_nop 7\n\ts_nop 7" : G2_ACC(0), G2_ACC(1), G2_ACC(2), G2_ACC(3), G2_ACC(4)::"memory");
}

__global__ __launch_bounds__(256, 1) NOPK void k2(Params p) {
    extern __shared__ __attribute__((aligned(16))) char smem[];
    const int b = blockIdx.x;
    const int w = __builtin_amdgcn_readfirstlane(threadIdx.x >> 6);
    TLB()
    const int xcd = b & 7, j = b >> 3;
    const int mb = 8 * (xcd >> 1) + (j & 7), nb = 4 * (xcd & 1) + (j >> 3);
    const int nch = (j & 1) ? 2 : 3;
    const int ch0 = xcd * 80 + (j >> 1) * 5 + ((j & 1) ? 3 : 0);
    const int k0 = ((j & 1) && w == 3) ? 34 : 8 * w;
    const int nk = w < nch ? 8 : ((j & 1) ? 2 * w + 14 : 30);
    const char* ab = p.ws + WS_ACT + ((size_t)mb * KS2 + k0) * 4096;
    const char* wb = p.ws + WS_W2P + ((size_t)nb * KS2 + k0) * 5120;
    u4v ra = {(u32)(size_t)ab, (u32)((size_t)ab >> 32), (u32)nk * 4096u, 0x00020000u};
    u4v rw = {(u32)(size_t)wb, (u32)((size_t)wb >> 32), (u32)nk * 5120u, 0x00020000u};
    const u32 lo16 = (threadIdx.x & 63) * 16;
    const u32 lb = lds_addr(smem);
    const u32 ao[6] = {lo16 + (w & 3) * 1024, lo16 + ((w + 1) & 3) * 1024, lo16 + ((w + 2) & 3) * 1024, lo16 + ((w + 3) & 3) * 1024, lo16, lo16 + 4096};
    SGDECL SG(0)
    if (w < nch) {
        const char* src = p.ws + WS_REC + (size_t)(ch0 + w) * REC;
#pragma unroll
        for (int i = 0; i < 9; ++i) GLDS16(lb + w * CV_WAVE + i * 1024, lo16, src + i * 1024);
    }
    u4v af[6][4], wf[6][5];
    u2v xv[5];
    {
        const u16* xp = p.x + (size_t)(mb * 64 + w * 16 + (threadIdx.x & 15)) * D + nb * 80 + ((threadIdx.x >> 4) & 3) * 4;
#pragma unroll
        for (int dt = 0; dt < 5; ++dt) xv[dt] = *(const u2v*)(xp + dt * 16);
    }
    SG(1)
    f4v acc2[5][4];
    if (w < nch) {
        conv_wave(p, ch0 + w, lb + w * CV_WAVE, smem + w * CV_WAVE, af, wf, acc2, ra, rw, ao, (u32)w,
                  (const u32*)(p.ws + WS_CNT + nb * 256), p.ws + WS_YT + (size_t)(nb * 80) * 4096 + (size_t)mb * 128, lb + K2_YS);
    } else {
        G2_LOAD(0) G2_LOAD(1) G2_LOAD(2) G2_LOAD(3) G2_LOAD(4) G2_LOAD(5)
        G2_ZERO()
        u32 sa = 6 * 4096, sw = 6 * 5120;
#pragma nounroll
        for (int r = 6; r < nk; r += 6) { G2_STEP(0) G2_STEP(1) G2_STEP(2) G2_STEP(3) G2_STEP(4) G2_STEP(5) }
#define G2_LAST(i, NW) { asm volatile("s_waitcnt vmcnt(" #NW ")" : G2_BUF("+", i)::"memory"); G2_SSTEP(2, i) }
        G2_LAST(0, 45) G2_LAST(1, 36) G2_LAST(2, 27) G2_LAST(3, 18) G2_LAST(4, 9) G2_LAST(5, 0)
        asm volatile("s_nop 7\n\ts_nop 7" : G2_ACC(0), G2_ACC(1), G2_ACC(2), G2_ACC(3), G2_ACC(4)::"memory");
    }
    SG(2)
    SG(3)
    int lane = threadIdx.x & 63;
    asm volatile("" : "+v"(lane));
    const int l15 = lane & 15, q4 = lane >> 4;
    const int t = mb * 64 + w * 16 + l15;
    const int dbase = nb * 80 + 4 * q4;
    char* red = smem + K2_RED;
#pragma unroll
    for (int tt = 1; tt < 4; ++tt) {
        char* dst = red + (((tt + w) & 3) * 3 + tt - 1) * 5120 + lane * 16;
#pragma unroll
        for (int dt = 0; dt < 5; ++dt) *(f4v*)(dst + dt * 1024) = acc2[dt][tt];
    }
    RAWBAR()
    SG(4)
    const char* ys = smem + K2_YS + (4 * q4 + (l15 >> 2)) * 128 + w * 32 + (lane & 3) * 8;
    const char* rsrc = red + w * 15360 + lane * 16;
    u2v svv[5];
    f4v part[5][3];
#pragma unroll
    for (int dt = 0; dt < 5; ++dt) {
        svv[dt] = tr_read(ys + dt * 2048);
#pragma unroll
        for (int i = 0; i < 3; ++i) part[dt][i] = *(const f4v*)(rsrc + i * 5120 + dt * 1024);
    }
    __builtin_amdgcn_sched_barrier(0);
#pragma unroll
    for (int dt = 0; dt < 5; ++dt) {
        const u2v sv = svv[dt];
        const f4v g = (acc2[dt][0] + part[dt][0]) + (part[dt][1] + part[dt][2]);
        u2v o;
#pragma unroll
        for (int r2 = 0; r2 < 2; ++r2) {
            const float p0 = g[2 * r2], p1 = g[2 * r2 + 1];
            const float g0 = p0 * __builtin_amdgcn_rcpf(1.0f + __builtin_amdgcn_exp2f(-1.44269504f * p0));
            const float g1 = p1 * __builtin_amdgcn_rcpf(1.0f + __builtin_amdgcn_exp2f(-1.44269504f * p1));
            o[r2] = pkbf(fmaf(bflo(sv[r2]), g0, bflo(xv[dt][r2])), fmaf(bfhi(sv[r2]), g1, bfhi(xv[dt][r2])));
        }
        *(u2v*)(p.out + (size_t)t * D + dbase + dt * 16) = o;
    }
    SG(5) SGDUMP(6)
    TLE(1 << 20)
}

extern "C" void kernel_launch(void* const* d_in, const int* in_sizes, int n_in, void* d_out, int out_size, void* d_ws,
                              size_t ws_size, hipStream_t stream) {
    Params p;
    p.x = (const u16*)d_in[0];
    p.Are = (const u16*)d_in[1]; p.Aim = (const u16*)d_in[2]; p.Bre = (const u16*)d_in[3]; p.Bim = (const u16*)d_in[4];
    p.Cre = (const u16*)d_in[5]; p.Cim = (const u16*)d_in[6]; p.logdt = (const u16*)d_in[7]; p.Dskip = (const u16*)d_in[8];
    p.w1 = (const u16*)d_in[10]; p.b1 = (const u16*)d_in[11]; p.w2 = (const u16*)d_in[12];
    p.out = (u16*)d_out;
    p.ws = (char*)d_ws;
    (void)hipFuncSetAttribute((const void*)k1, hipFuncAttributeMaxDynamicSharedMemorySize, LDS_BYTES);
    (void)hipFuncSetAttribute((const void*)k2, hipFuncAttributeMaxDynamicSharedMemorySize, K2_LDS);
    k1<<<512, 512, LDS_BYTES, stream>>>(p);
    k2<<<256, 256, K2_LDS, stream>>>(p);
}
```
